```python
import torch

def _softmax(x, axis=-1):
    return torch.softmax(x, dim=axis)
def _gelu(x):
    return torch.nn.functional.gelu(x, approximate="tanh")
def _silu(x):
    return torch.nn.functional.silu(x)
def _sigmoid(x):
    return torch.sigmoid(x)
def _layernorm(x, axis=-1, eps=1e-5):
    m = x.mean(dim=axis, keepdim=True)
    v = x.var(dim=axis, keepdim=True, unbiased=False)
    return (x - m) / torch.sqrt(v + eps)
def _scatter_add(tgt, idx, src, axis=0):
    return tgt.clone().index_add_(axis, idx, src)
def _segment_sum(src, seg, n):
    out = torch.zeros((n,) + src.shape[1:], dtype=src.dtype)
    return out.index_add_(0, seg, src)
def _pad(x, pw):
    flat = tuple(v for pair in reversed(pw) for v in pair)
    return torch.nn.functional.pad(x, flat)

kernel_name = 'interleave_layers__sliding_block_cached__dense_activation_gelu__decode'

def setup_inputs():

    _w = (lambda a: (lambda t: t.to(torch.bfloat16) if t.is_floating_point() else t)(torch.as_tensor(a)))
    def _A_setup():
        B, Sq, D = 1, 1, 640
        Hq, Hkv, Sk, Dh = 32, 32, 32768, 192
        assert Hq % Hkv == 0, (Hq, Hkv)
        return {
            "x": _w((lambda *s: torch.randn(*s, dtype=torch.bfloat16) * 0.1)(B, Sq, D)),
            "k_cache": _w((lambda *s: torch.randn(*s, dtype=torch.bfloat16) * 0.8)(B, Hkv, Sk, Dh)),
            "v_cache": _w((lambda *s: torch.randn(*s, dtype=torch.bfloat16) * 1.6)(B, Hkv, Sk, Dh)),
            "wq": _w((lambda *s: torch.randn(*s, dtype=torch.bfloat16) * 0.8)(D, Hq * Dh) * (D ** -0.5)),
            "wo": _w((lambda *s: torch.randn(*s, dtype=torch.bfloat16) * 1.6)(Hq * Dh, D) * ((Hq * Dh) ** -0.5)),
        }
    _A_in = _A_setup()

    def _B_setup():
        B, S, D, F = 1, 1, 640, 1536
        return {
            "x": _w((lambda *s: torch.randn(*s, dtype=torch.bfloat16) * 0.1)(B, S, D)),
            "w1": _w((lambda *s: torch.randn(*s, dtype=torch.bfloat16) * 0.4)(D, F) * (D ** -0.5)),
            "b1": _w((lambda *s: torch.randn(*s, dtype=torch.bfloat16) * 0.4)(F) * 0.01),
            "w2": _w((lambda *s: torch.randn(*s, dtype=torch.bfloat16) * 0.4)(F, D) * (F ** -0.5)),
            "b2": _w((lambda *s: torch.randn(*s, dtype=torch.bfloat16) * 0.4)(D) * 0.01),
        }
    _B_in = _B_setup()

    return {
        "A_x": _A_in["x"],
        "A_k_cache": _A_in["k_cache"],
        "A_v_cache": _A_in["v_cache"],
        "A_wq": _A_in["wq"],
        "A_wo": _A_in["wo"],
        "B_x": _B_in["x"],
        "B_w1": _B_in["w1"],
        "B_b1": _B_in["b1"],
        "B_w2": _B_in["w2"],
        "B_b2": _B_in["b2"],
    }

def reference(A_x, A_k_cache, A_v_cache, A_wq, A_wo, B_x, B_w1, B_b1, B_w2, B_b2):
    def _A_ref(x, k_cache, v_cache, wq, wo):
        B, Sq, D = x.shape
        Hq, Dh = 32, 192
        Hkv, Sk = k_cache.shape[1], k_cache.shape[2]
        W = min(4096, max(256, Sk // 4))
        rep = Hq // Hkv
        q = (lambda x, *d: x.permute(*d))((x @ wq).reshape(B, Sq, Hq, Dh), 0, 2, 1, 3)
        k = (lambda x, n, axis: torch.repeat_interleave(x, n, dim=axis))(k_cache[:, :, Sk - W:, :], rep, axis=1)
        v = (lambda x, n, axis: torch.repeat_interleave(x, n, dim=axis))(v_cache[:, :, Sk - W:, :], rep, axis=1)
        scores = torch.einsum("bhqd,bhkd->bhqk", q, k) * (Dh ** -0.5)
        p = _softmax(scores, axis=-1)
        o = torch.einsum("bhqk,bhkd->bhqd", p, v)
        return (lambda x, *d: x.permute(*d))(o, 0, 2, 1, 3).reshape(B, Sq, Hq * Dh) @ wo

    def _B_ref(x, w1, b1, w2, b2):
        h = torch.einsum("bsd,df->bsf", x, w1) + b1
        h = _gelu(h)
        return torch.einsum("bsf,fd->bsd", h, w2) + b2

    x = A_x
    x = x + _A_ref(_layernorm(x, axis=-1), A_k_cache, A_v_cache, A_wq, A_wo)
    x = x + _B_ref(_layernorm(x, axis=-1), B_w1, B_b1, B_w2, B_b2)
    x = x + _A_ref(_layernorm(x, axis=-1), A_k_cache, A_v_cache, A_wq, A_wo)
    x = x + _B_ref(_layernorm(x, axis=-1), B_w1, B_b1, B_w2, B_b2)
    return x
```

```cpp
#include <hip/hip_runtime.h>
#include <hip/hip_bf16.h>
#include <stdint.h>

#define GAS __attribute__((address_space(1)))
#define LAS __attribute__((address_space(3)))
typedef GAS unsigned long long gu64;
typedef GAS unsigned gu32;
typedef uint32_t u32x2 __attribute__((ext_vector_type(2)));
typedef uint32_t u32x4 __attribute__((ext_vector_type(4)));
typedef uint32_t u32x8 __attribute__((ext_vector_type(8)));
typedef float f32x2 __attribute__((ext_vector_type(2)));
typedef float f32x4 __attribute__((ext_vector_type(4)));
typedef short s16x4 __attribute__((ext_vector_type(4)));
typedef __attribute__((ext_vector_type(2))) __bf16 bf16x2;
typedef __attribute__((ext_vector_type(8))) __bf16 bf16x8;
typedef __attribute__((ext_vector_type(16))) __bf16 bf16x16;
#define RLX_AGENT __ATOMIC_RELAXED, __HIP_MEMORY_SCOPE_AGENT

constexpr int DM = 640, NH = 32, DH = 192, SK = 32768, WIN = 4096, FF = 1536, QD = NH * DH;
constexpr int NWG = 256, NT = 512, NW = NT / 64;
#define SPINB 20
constexpr unsigned SPIN_LIMIT = 1u << SPINB;
constexpr int TW = 7, SW = 6;
constexpr int CODEPF = 15;
#define UP 3
#define KWIN (3 * UP)
#define VWIN (4 * UP)
constexpr int nreq(int i0, int i1) { int n = 0; for (int i = i0; i <= i1 && i < 16; ++i) n += i == 15 ? 9 : (i == 0 || i > 8) ? 4 : 3; return n; }
#define XOFF 0
constexpr int kord(int n) { return 2 * (n & 3) + (n >> 2); }
constexpr int vord(int n) { return (n & 4) + 2 * (n & 1) + ((n >> 1) & 1); }
constexpr int RT = UP + 1;
constexpr int BKD = 3, BKO = 1;
constexpr float LOG2E = 1.4426950408889634f;
constexpr float SMALL = 40.f;
constexpr uint32_t SENT = 0xffffffffu;
constexpr int OOR = 1 << 22;

constexpr int RP = 27 * 16;
constexpr int G_P = 0, G_R = G_P + NWG * RP, G_F = G_R + NH * RP;
constexpr int BLK = 16384, NB = 21, O_SG = 6144, O_SH = 6400, O_H = 6656, O_C = 12 * BLK, O_DB = 20 * BLK;

constexpr int QS = 400;
constexpr int RS = 800;
constexpr int S_XP = 0, S_OV = 256, S_HV = 768, S_QB = 896, S_ZQ = 1280, S_RED = 1472, S_ML = S_RED + NW * RS, S_END = S_ML + 128;
constexpr int L_SM = 0, L_CS = L_SM + S_END, L_ONE = L_CS + 960, L_FLAG = L_ONE + 192, L_SCH = L_FLAG + 64, L_TAB = L_SCH + 64,
              L_WQ = L_TAB + 6 * NT * 16, L_WO = L_WQ + 32768, L_W2 = L_WO + 30720, L_W1 = L_W2 + 8192, L_TOTAL = L_W1 + 8192;
static_assert(L_TOTAL <= 160 * 1024 && L_TOTAL > 80 * 1024 && S_END % 16 == 0, "LDS");

enum { P3E, P2, P3, P4, P5, P1, RMIN, RCONS, CPAR, PO, PT, PA, PB, NSCH };
#define SCHED_INIT {122, 132, 62, 63, 59, 65, 5, 50, 2, 420, 88, 35, 92}
constexpr int sched[NSCH] = SCHED_INIT;
#define SCHED_ARG
#define SC0(i) sched[i]
#define SC(i) sched[i]

__device__ __forceinline__ float lo2f(uint32_t p) { return __uint_as_float(p << 16); }
__device__ __forceinline__ float hi2f(uint32_t p) { return __uint_as_float(p & 0xffff0000u); }
__device__ __forceinline__ uint32_t pk2(float a, float b) {
  f32x2 v = {a, b};
  return __builtin_bit_cast(uint32_t, __builtin_convertvector(v, bf16x2));
}
__device__ __forceinline__ float rbf(float f) { return __uint_as_float(pk2(0.f, f)); }
__device__ __forceinline__ uint32_t pair_lo(uint32_t a, uint32_t b) { return __builtin_amdgcn_perm(b, a, 0x05040100u); }
__device__ __forceinline__ uint32_t pair_hi(uint32_t a, uint32_t b) { return __builtin_amdgcn_perm(b, a, 0x07060302u); }
template <int CTRL> __device__ __forceinline__ float dppv(float v) {
  return __int_as_float(__builtin_amdgcn_update_dpp(0, __float_as_int(v), CTRL, 0xf, 0xf, false));
}
template <int CTRL, int RM> __device__ __forceinline__ float dpp_add(float v) {
  return v + __int_as_float(__builtin_amdgcn_update_dpp(0, __float_as_int(v), CTRL, RM, 0xf, CTRL < 0x140));
}
__device__ __forceinline__ float wsum63(float v) {
  v = dpp_add<0x111, 0xf>(v); v = dpp_add<0x112, 0xf>(v); v = dpp_add<0x114, 0xf>(v); v = dpp_add<0x118, 0xf>(v);
  v = dpp_add<0x142, 0xa>(v); v = dpp_add<0x143, 0xc>(v);
  return v;
}
__device__ __forceinline__ float wsum_bcast(float v) {
  return __int_as_float(__builtin_amdgcn_readlane(__float_as_int(wsum63(v)), 63));
}
__device__ __forceinline__ float swapadd32(float a, float b) {
  auto r = __builtin_amdgcn_permlane32_swap(__float_as_uint(a), __float_as_uint(b), false, false);
  return __uint_as_float(r[0]) + __uint_as_float(r[1]);
}
__device__ __forceinline__ float swapadd16(float a, float b) {
  auto r = __builtin_amdgcn_permlane16_swap(__float_as_uint(a), __float_as_uint(b), false, false);
  return __uint_as_float(r[0]) + __uint_as_float(r[1]);
}
__device__ __forceinline__ float ror8add(float v) { return v + dppv<0x128>(v); }
__device__ __forceinline__ float ror8add2(float a, float b) {
  float r;
  asm("s_nop 1\n\tv_add_f32_dpp %0, %1, %1 row_ror:8 row_mask:0xf bank_mask:0x3\n\tv_add_f32_dpp %0, %2, %2 row_ror:8 row_mask:0xf bank_mask:0xc" : "=&v"(r) : "v"(a), "v"(b));
  return r;
}
__device__ __forceinline__ float red16(float a, float b, float c, float d) { return swapadd16(swapadd32(a, b), swapadd32(c, d)); }
__device__ __forceinline__ float red_cols(const u32x4& a, const u32x4& b) {
  float x, y, z, u;
  asm("v_add_f32 %0, %4, %8\n\tv_add_f32 %1, %5, %9\n\tv_add_f32 %2, %6, %10\n\tv_add_f32 %3, %7, %11\n\t"
      "v_permlane32_swap_b32 %0, %1\n\ts_nop 0\n\tv_permlane32_swap_b32 %2, %3\n\t"
      "v_add_f32 %0, %0, %1\n\tv_add_f32 %2, %2, %3\n\ts_nop 1\n\t"
      "v_permlane16_swap_b32 %0, %2\n\tv_add_f32 %0, %0, %2\n\ts_nop 1\n\t"
      "v_add_f32_dpp %0, %0, %0 row_ror:8 row_mask:0xf bank_mask:0xf\n\ts_nop 1\n\t"
      "v_add_f32_dpp %0, %0, %0 row_ror:4 row_mask:0xf bank_mask:0xf"
      : "=&v"(x), "=&v"(y), "=&v"(z), "=&v"(u) : "v"(a.x), "v"(a.y), "v"(a.z), "v"(a.w), "v"(b.x), "v"(b.y), "v"(b.z), "v"(b.w));
  return x;
}
__device__ __forceinline__ float sum_lo3(float v) { v += dppv<0xb1>(v); v += dppv<0x4e>(v); return v + dppv<0x141>(v); }
__device__ __forceinline__ float max_lo3a(float v) {
  float r;
  asm("s_nop 1\n\tv_max_f32_dpp %0, %1, %1 quad_perm:[1,0,3,2] row_mask:0xf bank_mask:0xf\n\ts_nop 1\n\t"
      "v_max_f32_dpp %0, %0, %0 quad_perm:[2,3,0,1] row_mask:0xf bank_mask:0xf\n\ts_nop 1\n\t"
      "v_max_f32_dpp %0, %0, %0 row_half_mirror row_mask:0xf bank_mask:0xf" : "=&v"(r) : "v"(v));
  return r;
}
__device__ __forceinline__ f32x4 scale_sum_lo3(float f, const u32x4& v) {
  float a, b, c, d;
#define DPP4(ctl) "v_add_f32_dpp %0, %0, %0 " ctl " row_mask:0xf bank_mask:0xf\n\tv_add_f32_dpp %1, %1, %1 " ctl " row_mask:0xf bank_mask:0xf\n\t" \
                  "v_add_f32_dpp %2, %2, %2 " ctl " row_mask:0xf bank_mask:0xf\n\tv_add_f32_dpp %3, %3, %3 " ctl " row_mask:0xf bank_mask:0xf\n\t"
  asm("s_nop 0\n\tv_mul_f32 %0, %4, %5\n\tv_mul_f32 %1, %4, %6\n\tv_mul_f32 %2, %4, %7\n\tv_mul_f32 %3, %4, %8\n\t"
      DPP4("quad_perm:[1,0,3,2]") DPP4("quad_perm:[2,3,0,1]") DPP4("row_half_mirror")
      : "=&v"(a), "=&v"(b), "=&v"(c), "=&v"(d) : "v"(f), "v"(v.x), "v"(v.y), "v"(v.z), "v"(v.w));
  return (f32x4){a, b, c, d};
}
__device__ __forceinline__ float sum_b320(const u32x4& v) {
  float a, b, c;
  asm("v_add_f32_dpp %0, %3, %3 row_ror:8 row_mask:0xf bank_mask:0x3\n\tv_add_f32_dpp %1, %4, %4 row_ror:8 row_mask:0xf bank_mask:0x3\n\t"
      "v_add_f32_dpp %0, %5, %5 row_ror:8 row_mask:0xf bank_mask:0xc\n\tv_add_f32_dpp %1, %6, %6 row_ror:8 row_mask:0xf bank_mask:0xc\n\ts_nop 0\n\t"
      "v_add_f32_dpp %2, %0, %0 row_ror:12 row_mask:0xf bank_mask:0x5\n\tv_add_f32_dpp %2, %1, %1 row_ror:4 row_mask:0xf bank_mask:0xa\n\ts_nop 1\n\t"
      "v_add_f32_dpp %2, %2, %2 quad_perm:[1,0,3,2] row_mask:0xf bank_mask:0xf"
      : "=&v"(a), "=&v"(b), "=&v"(c) : "v"(v.x), "v"(v.y), "v"(v.z), "v"(v.w));
  return c;
}
template <int N> __device__ __forceinline__ uint32_t rbc(uint32_t v) { return __builtin_amdgcn_mov_dpp(v, 0x150 + N, 0xf, 0xf, false); }
template <int I> __device__ __forceinline__ u32x4 pset(uint32_t p) { return (u32x4){rbc<4 * I>(p), rbc<4 * I + 1>(p), rbc<4 * I + 2>(p), rbc<4 * I + 3>(p)}; }
#define RQ "+{v[194:197]}"(r), "+{v[198:201]}"(q)
__device__ __forceinline__ float red_o(u32x4 r, u32x4 q, uint32_t l) {
  float v, t;
  asm("v_permlane32_swap_b32 v194, v195\n\tv_permlane32_swap_b32 v196, v197\n\tv_add_f32_dpp %1, %4, %4 quad_perm:[1,0,3,2] row_mask:0xf bank_mask:0xf\n\t"
      "v_permlane32_swap_b32 v198, v199\n\tv_permlane32_swap_b32 v200, v201\n\t"
      "v_add_f32 v194, v194, v195\n\tv_add_f32 v196, v196, v197\n\tv_add_f32_dpp %1, %1, %1 quad_perm:[2,3,0,1] row_mask:0xf bank_mask:0xf\n\t"
      "v_add_f32 v198, v198, v199\n\tv_add_f32 v200, v200, v201\n\tv_permlane16_swap_b32 v194, v196\n\t"
      "v_add_f32_dpp %1, %1, %1 row_half_mirror row_mask:0xf bank_mask:0xf\n\tv_permlane16_swap_b32 v198, v200\n\t"
      "v_add_f32 v194, v194, v196\n\tv_add_f32 v198, v198, v200\n\tv_rcp_f32 %1, %1\n\t"
      "v_add_f32_dpp %0, v194, v194 row_ror:8 row_mask:0xf bank_mask:0x3\n\tv_add_f32_dpp %0, v198, v198 row_ror:8 row_mask:0xf bank_mask:0xc\n\t"
      "v_mul_f32 %0, %0, %1"
      : "=&v"(v), "=&v"(t), RQ : "v"(l));
  return v;
}
__device__ __forceinline__ float red_q(u32x4 r, u32x4 q, uint32_t sx, uint32_t sy, float cs, float& s2, float& mean) {
  float v;
  asm("v_permlane32_swap_b32 v194, v195\n\tv_permlane32_swap_b32 v196, v197\n\t"
      "v_add_f32_dpp %2, %6, %6 quad_perm:[1,0,3,2] row_mask:0xf bank_mask:0xf\n\tv_add_f32_dpp %1, %5, %5 quad_perm:[1,0,3,2] row_mask:0xf bank_mask:0xf\n\t"
      "v_permlane32_swap_b32 v198, v199\n\tv_permlane32_swap_b32 v200, v201\n\tv_add_f32 v194, v194, v195\n\tv_add_f32 v196, v196, v197\n\t"
      "v_add_f32_dpp %2, %2, %2 quad_perm:[2,3,0,1] row_mask:0xf bank_mask:0xf\n\tv_add_f32_dpp %1, %1, %1 quad_perm:[2,3,0,1] row_mask:0xf bank_mask:0xf\n\t"
      "v_add_f32 v198, v198, v199\n\tv_add_f32 v200, v200, v201\n\tv_permlane16_swap_b32 v194, v196\n\t"
      "v_add_f32_dpp %2, %2, %2 row_half_mirror row_mask:0xf bank_mask:0xf\n\tv_permlane16_swap_b32 v198, v200\n\t"
      "v_add_f32 v194, v194, v196\n\tv_add_f32 v198, v198, v200\n\tv_mul_f32 %2, 0x3acccccd, %2\n\t"
      "v_add_f32_dpp %0, v194, v194 row_ror:8 row_mask:0xf bank_mask:0x3\n\tv_add_f32_dpp %0, v198, v198 row_ror:8 row_mask:0xf bank_mask:0xc\n\t"
      "v_cvt_pk_bf16_f32 v195, 0, %2\n\tv_fma_f32 %0, -v195, %7, %0\n\t"
      "v_add_f32_dpp %1, %1, %1 row_half_mirror row_mask:0xf bank_mask:0xf"
      : "=&v"(v), "=&v"(s2), "=&v"(mean), RQ : "v"(sx), "v"(sy), "v"(cs));
  return v;
}
#define ZERO4(d, a, b, c, e) asm volatile("v_mov_b64 v[" #a ":" #b "], 0\n\tv_mov_b64 v[" #c ":" #e "], 0" : "={v[" #a ":" #e "]}"(d))
__device__ __forceinline__ float ln_rinv(float s2, float mean) {
  const float var = rbf(fmaf(-mean, mean, s2 * (1.0f / DM)));
  return __builtin_amdgcn_rcpf(rbf(__builtin_amdgcn_sqrtf(rbf(fabsf(var) + 1e-5f))));
}
__device__ __forceinline__ int opos(int k) { return ((k >> 1) & 1) * 192 + (k >> 6) * 64 + (k & 32) + (k & 8) * 2 + (k & 4) * 2 + ((k >> 4) & 1) * 4 + (k & 1) * 2; }
__device__ __forceinline__ void sum2_lo3(float x, float y, float& a, float& b) {
#define DPP2(ctl) "s_nop 0\n\tv_add_f32_dpp %0, %0, %0 " ctl " row_mask:0xf bank_mask:0xf\n\tv_add_f32_dpp %1, %1, %1 " ctl " row_mask:0xf bank_mask:0xf\n\t"
  asm("s_nop 1\n\tv_add_f32_dpp %0, %2, %2 quad_perm:[1,0,3,2] row_mask:0xf bank_mask:0xf\n\tv_add_f32_dpp %1, %3, %3 quad_perm:[1,0,3,2] row_mask:0xf bank_mask:0xf\n\t"
      DPP2("quad_perm:[2,3,0,1]") DPP2("row_half_mirror") : "=&v"(a), "=&v"(b) : "v"(x), "v"(y));
}
__device__ __forceinline__ void lds_barrier() { asm volatile("s_waitcnt lgkmcnt(0)\n\ts_barrier" ::: "memory"); }
__device__ __forceinline__ uint32_t tagbad(const u32x4& r, uint32_t tag) { return (r.y ^ tag) | (r.w ^ tag); }
__device__ __forceinline__ float uf(uint32_t u) { return __uint_as_float(u); }
__device__ __forceinline__ uint32_t rt32() {
  unsigned long long t;
  asm volatile("s_memrealtime %0\n\ts_waitcnt lgkmcnt(0)" : "=s"(t)::"memory");
  return (uint32_t)t;
}
__device__ __forceinline__ f32x4 mfma32(const u32x4& a, const u32x4& b, const f32x4& c) {
  return __builtin_amdgcn_mfma_f32_16x16x32_bf16(__builtin_bit_cast(bf16x8, a), __builtin_bit_cast(bf16x8, b), c, 0, 0, 0);
}
template <int H> __device__ __forceinline__ f32x4 smf(const u32x4& a, const u32x8& b, const f32x4& c, uint32_t ix) {
  return __builtin_amdgcn_smfmac_f32_16x16x64_bf16(__builtin_bit_cast(bf16x8, a), __builtin_bit_cast(bf16x16, b), c, ix, 0, H);
}
__device__ __forceinline__ u32x8 cat8(const u32x4& a, const u32x4& b) { return __builtin_shufflevector(a, b, 0, 1, 2, 3, 4, 5, 6, 7); }
__device__ __forceinline__ int qpos(int v) { return ((v >> 1) & 1) * 192 + (v >> 2) * 4 + (v & 1) * 2; }
__device__ __forceinline__ u32x4 cat(const u32x2& a, const u32x2& b) { return (u32x4){a.x, a.y, b.x, b.y}; }
__device__ __forceinline__ uint32_t umax4(const u32x4& r) { return max(max(r.x, r.y), max(r.z, r.w)); }
__device__ __forceinline__ int kpos(int k) { return (k & ~31) | ((k & 12) << 1) | ((k & 16) >> 2) | (k & 3); }

#define DUMP(i) do {} while (0)
#define STAMPW(i) do { const uint32_t t_ = rt32(); if ((int)__lane_id() == (i)) stv = t_; } while (0)
#define STAMPV(i) do {} while (0)
#define CSTAMP(on, i) do {} while (0)
#define ESTAMP(i) do {} while (0)
#define PIN1(a) asm volatile("" : "+v"(a))
#define PINS(a) asm volatile("" : "+s"(a))
#define PINS2(a, c) asm volatile("" : "+s"(a), "+s"(c))
#define SB() do { asm volatile("" ::: "memory"); __builtin_amdgcn_sched_barrier(0); } while (0)
#define VM0() __builtin_amdgcn_s_waitcnt(0x0f70)
#define BAR0() asm volatile("s_barrier" ::: "memory")
#define BARX(t) asm volatile("s_barrier ; " t ::: "memory")
#define LBAR(t) asm volatile("s_waitcnt lgkmcnt(0)\n\ts_barrier ; " t ::: "memory")
template <int V> using IC = std::integral_constant<int, V>;

#define TAIL_ASM \
  "s_mov_b32 s80, 0\n\ts_mov_b32 s84, -1\n\ts_getpc_b64 s[98:99]\n\t.LPC%=:\ns_add_u32 s98, s98, .LCLK%=-.LPC%=\n\ts_addc_u32 s99, s99, 0\n\t.LLOOP%=:\n" \
  "s_lshl_b32 s85, s80, 17\n\ts_lshl_b32 s86, s80, 16\n\ts_cmp_gt_u32 %[w], 4\n\ts_cbranch_scc1 .LH2X%=\n\t" \
  "ds_read_b64 v[240:241], %[t16] offset:%[tab]+8192\n\ts_add_u32 s81, %[oG], s85\n\ts_add_u32 s82, s81, %[osg]\n\ts_add_u32 s83, %[eC], s86\n\t" \
  "v_and_b32 v242, 16, %[t16]\n\tv_mov_b64 v[232:233], 0\n\tv_mov_b64 v[234:235], 0\n\tv_mad_u32_u24 v242, v242, 12, %[bo]\n\ts_cmp_eq_u32 s80, 0\n\t" \
  "s_cbranch_scc1 .LH2A%=\n\ts_waitcnt lgkmcnt(0)\n\tds_read_b64_tr_b16 v[204:205], v241\n\tds_read_b64_tr_b16 v[206:207], v241 offset:2560\n\t" \
  "ds_read_b64_tr_b16 v[208:209], v241 offset:5120\n\tds_read_b64_tr_b16 v[210:211], v241 offset:7680\n\t" \
  "ds_read_b64_tr_b16 v[212:213], v241 offset:10240\n\tds_read_b64_tr_b16 v[214:215], v241 offset:12800\n\t" \
  "ds_read_b64_tr_b16 v[216:217], v241 offset:15360\n\tds_read_b64_tr_b16 v[218:219], v241 offset:17920\n\t" \
  "ds_read_b64_tr_b16 v[220:221], v241 offset:20480\n\tds_read_b64_tr_b16 v[222:223], v241 offset:23040\n\t" \
  "ds_read_b64_tr_b16 v[224:225], v241 offset:25600\n\tds_read_b64_tr_b16 v[226:227], v241 offset:28160\n\t.LH2A%=:\ns_cmp_gt_u32 %[w], 2\n\t" \
  "s_cbranch_scc1 .LH2M%=\n\ts_barrier\n\tbuffer_load_dwordx2 v[202:203], %[ss], %[rs], s82 offen sc1\n\t" \
  "buffer_load_dwordx4 v[194:197], %[s0], %[rs], s81 offen sc1\n\tbuffer_load_dwordx4 v[198:201], %[s0], %[rs], s81 offen offset:128 sc1\n\t" \
  "s_waitcnt vmcnt(2) lgkmcnt(0)\n\t.LRED2%=:\nv_cmp_ne_u32 s[90:91], 0, v202\n\ts_waitcnt vmcnt(0)\n\tv_permlane32_swap_b32 v194, v195\n\t" \
  "v_permlane32_swap_b32 v196, v197\n\tv_add_f32_dpp v247, v203, v203 quad_perm:[1,0,3,2] row_mask:0xf bank_mask:0xf\n\t" \
  "v_permlane32_swap_b32 v198, v199\n\tv_permlane32_swap_b32 v200, v201\n\tv_add_f32 v194, v194, v195\n\tv_add_f32 v196, v196, v197\n\t" \
  "v_add_f32_dpp v247, v247, v247 quad_perm:[2,3,0,1] row_mask:0xf bank_mask:0xf\n\tv_add_f32 v198, v198, v199\n\tv_add_f32 v200, v200, v201\n\t" \
  "v_permlane16_swap_b32 v194, v196\n\tv_add_f32_dpp v247, v247, v247 row_half_mirror row_mask:0xf bank_mask:0xf\n\tv_permlane16_swap_b32 v198, v200\n\t" \
  "v_add_f32 v194, v194, v196\n\tv_add_f32 v198, v198, v200\n\tv_rcp_f32 v247, v247\n\t" \
  "v_add_f32_dpp v246, v194, v194 row_ror:8 row_mask:0xf bank_mask:0x3\n\tv_add_f32_dpp v246, v198, v198 row_ror:8 row_mask:0xf bank_mask:0xc\n\t" \
  "v_mul_f32 v246, v246, v247\n\tv_cvt_pk_bf16_f32 v247, v246, v246\n\tds_write_b16 v240, v247\n\tv_cmp_u_f32 s[92:93], v246, v246\n\t" \
  "s_or_b64 s[90:91], s[90:91], s[92:93]\n\ts_cbranch_scc1 .LSL2%=\n\t.LH2B%=:\ns_cmp_eq_u32 s80, 0\n\ts_cbranch_scc1 .LWO0%=\n\ts_waitcnt lgkmcnt(0)\n\t" \
  "s_barrier\n\tds_read_b128 v[194:197], v242 offset:%[ov]\n\tds_read_b128 v[198:201], v242 offset:%[ov]+64\n\t" \
  "ds_read_b128 v[228:231], v242 offset:%[ov]+128\n\ts_waitcnt lgkmcnt(2)\n\tv_smfmac_f32_16x16x64_bf16 v[232:235], v[194:197], v[204:211], %[ix]\n\t" \
  "s_waitcnt lgkmcnt(1)\n\tv_smfmac_f32_16x16x64_bf16 v[232:235], v[198:201], v[212:219], %[ix]\n\ts_waitcnt lgkmcnt(0)\n\t" \
  "v_smfmac_f32_16x16x64_bf16 v[232:235], v[228:231], v[220:227], %[ix]\n\t.LWOE%=:\ns_nop 7\n\tv_add_f32 v232, v232, v233\n\t" \
  "buffer_store_dword v232, %[dt], %[rs], s83 offen sc1\n\tds_read_b64 v[240:241], %[t16] offset:%[tab]+8192+8\n\t" \
  "ds_read_b32 %[s0], %[t16] offset:%[tab]+16384+12\n\tds_read_b32 v242, %[t16] offset:%[tab]+32768\n\ts_add_u32 s81, %[rC], s86\n\t" \
  "s_add_u32 s83, %[eH], s85\n\tv_mov_b64 v[214:215], 0\n\ts_waitcnt lgkmcnt(2)\n\tds_read_b64_tr_b16 v[204:205], v241\n\t" \
  "ds_read_b64_tr_b16 v[206:207], v241 offset:1536\n\tds_read_b64_tr_b16 v[208:209], v241 offset:3072\n\t" \
  "ds_read_b64_tr_b16 v[210:211], v241 offset:4608\n\tds_read_b64_tr_b16 v[212:213], v241 offset:6144\n\ts_waitcnt lgkmcnt(0)\n\ts_barrier\n\t" \
  "buffer_load_dwordx4 v[194:197], %[t16], %[rs], s81 offen sc1\n\tbuffer_load_dwordx4 v[198:201], %[t16], %[rs], s81 offen offset:1024 sc1\n\t" \
  "s_waitcnt vmcnt(0)\n\t.LRED3%=:\nv_add_f32 v194, v194, v198\n\tv_add_f32 v195, v195, v199\n\tv_add_f32 v196, v196, v200\n\t" \
  "v_add_f32 v197, v197, v201\n\tv_permlane32_swap_b32 v194, v195\n\ts_nop 0\n\tv_permlane32_swap_b32 v196, v197\n\tv_add_f32 v194, v194, v195\n\t" \
  "v_add_f32 v196, v196, v197\n\ts_nop 1\n\tv_permlane16_swap_b32 v194, v196\n\tv_add_f32 v194, v194, v196\n\ts_nop 1\n\t" \
  "v_add_f32_dpp v194, v194, v194 row_ror:8 row_mask:0xf bank_mask:0xf\n\ts_nop 1\n\t" \
  "v_add_f32_dpp v246, v194, v194 row_ror:4 row_mask:0xf bank_mask:0xf\n\tv_cvt_pk_bf16_f32 v247, 0, v246\n\tv_add_f32 v247, v242, v247\n\t" \
  "v_cvt_pk_bf16_f32 v243, v247, 0\n\tds_write_b16 v240, v243\n\tv_cmp_u_f32 vcc, v246, v246\n\ts_cbranch_vccnz .LSL3%=\n\t.LH3B%=:\n" \
  "s_cmp_gt_u32 %[w], 2\n\ts_cbranch_scc1 .LH3M%=\n\ts_waitcnt lgkmcnt(0)\n\ts_barrier\n\tds_read_b128 v[194:197], %[bo] offset:%[xp]\n\t" \
  "ds_read_b128 v[198:201], %[bo] offset:%[xp]+64\n\tds_read_b128 v[228:231], %[bo] offset:%[xp]+128\n\ts_waitcnt lgkmcnt(2)\n\t" \
  "v_mfma_f32_16x16x32_bf16 v[232:235], v[194:197], v[204:207], 0\n\ts_waitcnt lgkmcnt(1)\n\t" \
  "v_mfma_f32_16x16x32_bf16 v[232:235], v[198:201], v[208:211], v[232:235]\n\ts_waitcnt lgkmcnt(0)\n\t" \
  "v_mfma_f32_16x16x32_bf16 v[232:235], v[228:231], v[212:215], v[232:235]\n\ts_nop 7\n\tbuffer_store_dword v232, %[dt], %[rs], s83 offen sc1\n\t" \
  ".LH3E%=:\nv_lshlrev_b32 v243, 16, v243\n\tds_write_b32 %[t16], v243 offset:%[tab]+32768\n\tds_read_b128 v[240:243], %[t16] offset:%[tab]+16384\n\t" \
  "ds_read_b32 v244, %[t16] offset:%[tab]+32768+8\n\ts_add_u32 s81, %[rH], s85\n\ts_add_u32 s82, s81, %[osh]\n\ts_add_u32 s81, s81, %[oh]\n\t" \
  "s_add_u32 s83, %[eC], s86\n\ts_add_u32 s83, s83, 0x8000\n\tv_mov_b64 v[210:211], 0\n\tv_mov_b32 v245, 0xc0135761\n\ts_waitcnt lgkmcnt(1)\n\t" \
  "ds_read_b64_tr_b16 v[204:205], v241\n\tds_read_b64_tr_b16 v[206:207], v241 offset:2560\n\tds_read_b64_tr_b16 v[208:209], v241 offset:5120\n\t" \
  "ds_read_b32 v242, v242\n\ts_cmp_gt_u32 %[w], 1\n\ts_cbranch_scc1 .LH4M%=\n\ts_waitcnt lgkmcnt(0)\n\ts_barrier\n\t" \
  "buffer_load_dwordx2 v[202:203], %[ss], %[rs], s82 offen sc1\n\tbuffer_load_dwordx4 v[194:197], %[s0], %[rs], s81 offen sc1\n\ts_waitcnt vmcnt(1)\n\t" \
  ".LRED4%=:\nv_add_f32_dpp v198, v202, v202 quad_perm:[1,0,3,2] row_mask:0xf bank_mask:0xf\n\t" \
  "v_add_f32_dpp v199, v203, v203 quad_perm:[1,0,3,2] row_mask:0xf bank_mask:0xf\n\ts_waitcnt vmcnt(0)\n\tv_permlane32_swap_b32 v194, v195\n\t" \
  "v_add_f32_dpp v198, v198, v198 quad_perm:[2,3,0,1] row_mask:0xf bank_mask:0xf\n\t" \
  "v_add_f32_dpp v199, v199, v199 quad_perm:[2,3,0,1] row_mask:0xf bank_mask:0xf\n\tv_permlane32_swap_b32 v196, v197\n\tv_add_f32 v194, v194, v195\n\t" \
  "v_add_f32_dpp v198, v198, v198 row_half_mirror row_mask:0xf bank_mask:0xf\n\t" \
  "v_add_f32_dpp v199, v199, v199 row_half_mirror row_mask:0xf bank_mask:0xf\n\tv_add_f32 v196, v196, v197\n\tv_mul_f32 v199, 0x3acccccd, v199\n\t" \
  "v_mul_f32 v198, 0x3acccccd, v198\n\tv_permlane16_swap_b32 v194, v196\n\tv_fma_f32 v198, -v199, v199, v198\n\tv_add_f32 v194, v194, v196\n\t" \
  "v_cvt_pk_bf16_f32 v198, 0, v198\n\tv_cvt_pk_bf16_f32 v199, 0, v199\n\tv_add_f32 v198, |v198|, %[eps]\n\t" \
  "v_add_f32_dpp v194, v194, v194 row_ror:8 row_mask:0xf bank_mask:0xf\n\tv_cvt_pk_bf16_f32 v198, 0, v198\n\tv_sqrt_f32 v198, v198\n\t" \
  "v_fma_f32 v194, -v199, v242, v194\n\tv_cvt_pk_bf16_f32 v198, 0, v198\n\tv_rcp_f32 v198, v198\n\ts_nop 0\n\tv_mul_f32 v246, v194, v198\n\t" \
  "v_cvt_pk_bf16_f32 v247, 0, v246\n\tv_add_f32 v247, v244, v247\n\tv_cvt_pk_bf16_f32 v247, 0, v247\n\tv_mul_f32 v198, v247, v247\n\t" \
  "v_fmamk_f32 v198, v198, 0xbdd2d3e7, v245\n\tv_mul_f32 v198, v198, v247\n\tv_exp_f32 v198, v198\n\tv_cmp_u_f32 vcc, v246, v246\n\t" \
  "v_add_f32 v198, 1.0, v198\n\tv_rcp_f32 v198, v198\n\ts_nop 0\n\tv_mul_f32 v198, v198, v247\n\tv_cvt_pk_bf16_f32 v198, v198, v198\n\t" \
  "ds_write_b16 v240, v198\n\ts_cbranch_vccnz .LSL4%=\n\t.LH4B%=:\ns_waitcnt lgkmcnt(0)\n\ts_barrier\n\tds_read_b128 v[194:197], %[bo] offset:%[hv]\n\t" \
  "ds_read_b128 v[198:201], %[bo] offset:%[hv]+64\n\ts_waitcnt lgkmcnt(1)\n\tv_mfma_f32_16x16x32_bf16 v[232:235], v[194:197], v[204:207], 0\n\t" \
  "s_waitcnt lgkmcnt(0)\n\tv_mfma_f32_16x16x32_bf16 v[232:235], v[198:201], v[208:211], v[232:235]\n\ts_nop 7\n\t" \
  "buffer_store_dword v232, %[dt], %[rs], s83 offen sc1\n\t.LH4E%=:\ns_and_b32 s88, s80, %[far]\n\ts_cbranch_scc1 .LEND%=\n\t" \
  "ds_read_b64 v[240:241], %[t16] offset:%[tab]+24576+8\n\tds_read_b32 v242, %[t16] offset:%[tab]+8192+8\n\t" \
  "ds_read_b64 v[244:245], %[t16] offset:%[tab]+32768\n\ts_add_u32 s81, %[rC], s86\n\ts_add_u32 s81, s81, 0x8000\n\ts_mov_b32 s83, %[eQ]\n\t" \
  "v_mov_b64 v[214:215], 0\n\tv_mov_b64 v[226:227], 0\n\ts_waitcnt lgkmcnt(2)\n\tds_read_b64_tr_b16 v[204:205], v240\n\t" \
  "ds_read_b64_tr_b16 v[216:217], v240 offset:32\n\tds_read_b64_tr_b16 v[206:207], v240 offset:6400\n\t" \
  "ds_read_b64_tr_b16 v[218:219], v240 offset:6432\n\tds_read_b64_tr_b16 v[208:209], v240 offset:12800\n\t" \
  "ds_read_b64_tr_b16 v[220:221], v240 offset:12832\n\tds_read_b64_tr_b16 v[210:211], v240 offset:19200\n\t" \
  "ds_read_b64_tr_b16 v[222:223], v240 offset:19232\n\tds_read_b64_tr_b16 v[212:213], v240 offset:25600\n\t" \
  "ds_read_b64_tr_b16 v[224:225], v240 offset:25632\n\ts_cmp_gt_u32 %[w], 4\n\ts_cbranch_scc1 .LH5M%=\n\ts_waitcnt lgkmcnt(0)\n\ts_barrier\n\t" \
  "buffer_load_dwordx4 v[194:197], %[t16], %[rs], s81 offen sc1\n\tbuffer_load_dwordx4 v[198:201], %[t16], %[rs], s81 offen offset:1024 sc1\n\t" \
  "s_waitcnt vmcnt(0)\n\t.LRED5%=:\nv_add_f32 v194, v194, v198\n\tv_add_f32 v195, v195, v199\n\tv_add_f32 v196, v196, v200\n\t" \
  "v_add_f32 v197, v197, v201\n\tv_permlane32_swap_b32 v194, v195\n\ts_nop 0\n\tv_permlane32_swap_b32 v196, v197\n\tv_add_f32 v194, v194, v195\n\t" \
  "v_add_f32 v196, v196, v197\n\ts_nop 1\n\tv_permlane16_swap_b32 v194, v196\n\tv_add_f32 v194, v194, v196\n\ts_nop 1\n\t" \
  "v_add_f32_dpp v194, v194, v194 row_ror:8 row_mask:0xf bank_mask:0xf\n\ts_nop 1\n\t" \
  "v_add_f32_dpp v246, v194, v194 row_ror:4 row_mask:0xf bank_mask:0xf\n\tv_cvt_pk_bf16_f32 v247, 0, v246\n\tv_cmp_u_f32 vcc, v246, v246\n\t" \
  "v_add_f32 v247, v245, v247\n\tv_cvt_pk_bf16_f32 v247, 0, v247\n\tv_add_f32 v247, v244, v247\n\tv_cvt_pk_bf16_f32 v243, v247, 0\n\t" \
  "ds_write_b16 v242, v243\n\ts_cbranch_vccnz .LSL5%=\n\t.LH5B%=:\ns_cmp_lg_u32 s80, 0\n\ts_cbranch_scc1 .LFIN%=\n\t.LH5C%=:\ns_waitcnt lgkmcnt(0)\n\t" \
  "s_barrier\n\tds_read_b128 v[194:197], %[bo] offset:%[xp]\n\tds_read_b128 v[198:201], %[bo] offset:%[xp]+64\n\t" \
  "ds_read_b128 v[228:231], %[bo] offset:%[xp]+128\n\ts_waitcnt lgkmcnt(2)\n\tv_mfma_f32_16x16x32_bf16 v[232:235], v[194:197], v[204:207], 0\n\t" \
  "v_mfma_f32_16x16x32_bf16 v[236:239], v[194:197], v[216:219], 0\n\ts_waitcnt lgkmcnt(1)\n\t" \
  "v_mfma_f32_16x16x32_bf16 v[232:235], v[198:201], v[208:211], v[232:235]\n\tv_mfma_f32_16x16x32_bf16 v[236:239], v[198:201], v[220:223], v[236:239]\n\t" \
  "s_waitcnt lgkmcnt(0)\n\tv_mfma_f32_16x16x32_bf16 v[232:235], v[228:231], v[212:215], v[232:235]\n\t" \
  "v_mfma_f32_16x16x32_bf16 v[236:239], v[228:231], v[224:227], v[236:239]\n\ts_nop 6\n\tbuffer_store_dword v232, %[dt], %[rs], s83 offen sc1\n\t" \
  "buffer_store_dword v236, %[dt], %[rs], s83 offen offset:64 sc1\n\tv_lshlrev_b32 v243, 16, v243\n\tds_write_b32 %[t16], v243 offset:%[tab]+32768\n\t" \
  ".LH1%=:\nds_read_b64 v[204:205], %[t16] offset:%[tab]+24576\n\tds_read_b128 v[240:243], %[t16] offset:%[tab]\n\t" \
  "ds_read_b128 v[236:239], %[t16] offset:%[tab]+40960\n\ts_add_u32 s81, %[oG], 0x10000\n\ts_and_b32 s82, s81, %[oor]-1\n\ts_add_u32 s82, s82, %[osg]\n\t" \
  "s_mov_b32 s92, 0x00ff00ff\n\ts_mov_b32 s93, 0xff00ff00\n\tv_mov_b64 v[208:209], 0\n\tv_mov_b64 v[210:211], 0\n\tv_mov_b64 v[212:213], 0\n\t" \
  "v_mov_b64 v[214:215], 0\n\tv_mov_b64 v[216:217], 0\n\tv_mov_b64 v[218:219], 0\n\tv_mov_b64 v[220:221], 0\n\tv_mov_b64 v[222:223], 0\n\t" \
  "v_mov_b64 v[224:225], 0\n\tv_mov_b64 v[226:227], 0\n\tv_mov_b64 v[228:229], 0\n\tv_mov_b64 v[230:231], 0\n\tv_mov_b64 v[232:233], 0\n\t" \
  "v_mov_b64 v[234:235], 0\n\ts_waitcnt lgkmcnt(2)\n\tds_read_b32 v205, v205\n\ts_cmp_gt_u32 %[w], 2\n\ts_cbranch_scc1 .LH1X%=\n\t" \
  "s_waitcnt lgkmcnt(0)\n\ts_barrier\n\tbuffer_load_dwordx2 v[202:203], %[ss], %[rs], s82 offen sc1\n\t" \
  "buffer_load_dwordx4 v[194:197], v243, %[rs], s81 offen sc1\n\tbuffer_load_dwordx4 v[198:201], v243, %[rs], s81 offen offset:128 sc1\n\t" \
  "s_waitcnt vmcnt(0)\n\t.LRED1%=:\nv_permlane32_swap_b32 v194, v195\n\tv_permlane32_swap_b32 v196, v197\n\t" \
  "v_add_f32_dpp v206, v203, v203 quad_perm:[1,0,3,2] row_mask:0xf bank_mask:0xf\n\tv_permlane32_swap_b32 v198, v199\n\t" \
  "v_permlane32_swap_b32 v200, v201\n\tv_add_f32 v194, v194, v195\n\tv_add_f32 v196, v196, v197\n\t" \
  "v_add_f32_dpp v206, v206, v206 quad_perm:[2,3,0,1] row_mask:0xf bank_mask:0xf\n\tv_add_f32 v198, v198, v199\n\tv_add_f32 v200, v200, v201\n\t" \
  "v_permlane16_swap_b32 v194, v196\n\tv_add_f32_dpp v206, v206, v206 row_half_mirror row_mask:0xf bank_mask:0xf\n\tv_permlane16_swap_b32 v198, v200\n\t" \
  "v_add_f32 v194, v194, v196\n\tv_add_f32 v198, v198, v200\n\tv_mul_f32 v206, 0x3acccccd, v206\n\t" \
  "v_add_f32_dpp v246, v194, v194 row_ror:8 row_mask:0xf bank_mask:0x3\n\tv_add_f32_dpp v246, v198, v198 row_ror:8 row_mask:0xf bank_mask:0xc\n\t" \
  "v_cvt_pk_bf16_f32 v247, 0, v206\n\tv_fma_f32 v246, -v247, v205, v246\n\tv_cvt_pk_bf16_f32 v247, v246, v246\n\tds_write_b16 v204, v247\n\t" \
  "v_cmp_u_f32 vcc, v246, v246\n\ts_cbranch_vccnz .LSL1%=\n\t.LH1B%=:\ns_waitcnt lgkmcnt(0)\n\ts_barrier\n\tds_read_b128 v[194:197], v236\n\t" \
  "ds_read_b128 v[198:201], v236 offset:64\n\tds_read_b128 v[204:207], v236 offset:128\n\tds_read_b32 v202, v239 offset:%[qsw]\n\t" \
  "s_waitcnt lgkmcnt(3)\n\tv_smfmac_f32_16x16x64_bf16 v[208:211], v[194:197], v[2:9], %[ix]\n\t" \
  "v_smfmac_f32_16x16x64_bf16 v[212:215], v[194:197], v[26:33], %[ix]\n\tv_smfmac_f32_16x16x64_bf16 v[216:219], v[194:197], v[50:57], %[ix]\n\t" \
  "v_smfmac_f32_16x16x64_bf16 v[220:223], v[194:197], v[74:81], %[ix]\n\ts_waitcnt lgkmcnt(2)\n\t" \
  "v_smfmac_f32_16x16x64_bf16 v[208:211], v[198:201], v[10:17], %[ix]\n\tv_smfmac_f32_16x16x64_bf16 v[212:215], v[198:201], v[34:41], %[ix]\n\t" \
  "v_smfmac_f32_16x16x64_bf16 v[216:219], v[198:201], v[58:65], %[ix]\n\tv_smfmac_f32_16x16x64_bf16 v[220:223], v[198:201], v[82:89], %[ix]\n\t" \
  "s_waitcnt lgkmcnt(1)\n\tv_smfmac_f32_16x16x64_bf16 v[208:211], v[204:207], v[18:25], %[ix]\n\t" \
  "v_smfmac_f32_16x16x64_bf16 v[212:215], v[204:207], v[42:49], %[ix]\n\tv_smfmac_f32_16x16x64_bf16 v[216:219], v[204:207], v[66:73], %[ix]\n\t" \
  "v_smfmac_f32_16x16x64_bf16 v[220:223], v[204:207], v[90:97], %[ix]\n\ts_setprio 1\n\ts_nop 3\n\tv_add_f32 v246, v208, v209\n\t" \
  "v_add_f32 v203, v212, v213\n\tv_cndmask_b32 v246, v203, v246, s[92:93]\n\tv_add_f32 v247, v216, v217\n\tv_add_f32 v203, v220, v221\n\t" \
  "v_cndmask_b32 v247, v203, v247, s[92:93]\n\ts_waitcnt lgkmcnt(0)\n\ts_nop 0\n\tv_permlane32_swap_b32 v246, v247\n\ts_nop 1\n\t" \
  "v_add_f32_dpp v246, v247, v246 row_ror:8 row_mask:0xf bank_mask:0xf\n\tv_mul_f32 v246, v246, v202\n\tv_cvt_pk_bf16_f32 v246, 0, v246\n\t" \
  "v_mul_f32 v246, 0x3d93cd3a, v246\n\tv_cvt_pk_bf16_f32 v246, 0, v246\n\tv_mul_f32 v246, 0x3fb8aa3b, v246\n\tv_exp_f32 v203, v246\n\t" \
  "v_cmp_nle_f32 vcc, |v246|, %[small]\n\tv_cvt_pk_bf16_f32 v244, v203, 0\n\tv_add_f32_dpp v203, v203, v203 row_ror:1 row_mask:0xf bank_mask:0xf\n\t" \
  "s_nop 0\n\tv_mov_b32_dpp v245, v244 row_shl:1 row_mask:0xf bank_mask:0xf\n\ts_cbranch_vccnz .LSLM%=\n\t.LPV%=:\ns_setprio 0\n\t" \
  "v_mov_b64_dpp v[208:209], v[244:245] row_newbcast:0 row_mask:0xf bank_mask:0xf\n\t" \
  "v_mov_b64_dpp v[210:211], v[244:245] row_newbcast:2 row_mask:0xf bank_mask:0xf\n\t" \
  "v_add_f32_dpp v203, v203, v203 row_ror:2 row_mask:0xf bank_mask:0xf\n\t" \
  "v_mov_b64_dpp v[212:213], v[244:245] row_newbcast:4 row_mask:0xf bank_mask:0xf\n\t" \
  "v_smfmac_f32_16x16x64_bf16 v[224:227], v[208:211], v[98:105], %[ix] abid:1\n\t" \
  "v_mov_b64_dpp v[214:215], v[244:245] row_newbcast:6 row_mask:0xf bank_mask:0xf\n\t" \
  "v_smfmac_f32_16x16x64_bf16 v[228:231], v[208:211], v[106:113], %[ix] abid:1\n\tv_add_f32_dpp v203, v203, v203 row_ror:4 row_mask:0xf bank_mask:0xf\n\t" \
  "v_smfmac_f32_16x16x64_bf16 v[232:235], v[208:211], v[114:121], %[ix] abid:1\n\t" \
  "v_smfmac_f32_16x16x64_bf16 v[224:227], v[212:215], v[122:129], %[ix] abid:1\n\t" \
  "v_mov_b64_dpp v[208:209], v[244:245] row_newbcast:8 row_mask:0xf bank_mask:0xf\n\t" \
  "v_mov_b64_dpp v[210:211], v[244:245] row_newbcast:10 row_mask:0xf bank_mask:0xf\n\t" \
  "v_smfmac_f32_16x16x64_bf16 v[228:231], v[212:215], v[130:137], %[ix] abid:1\n\tv_add_f32_dpp v203, v203, v203 row_ror:8 row_mask:0xf bank_mask:0xf\n\t" \
  "v_smfmac_f32_16x16x64_bf16 v[232:235], v[212:215], v[138:145], %[ix] abid:1\n\t" \
  "v_smfmac_f32_16x16x64_bf16 v[224:227], v[208:211], v[146:153], %[ix] abid:1\n\t" \
  "v_mov_b64_dpp v[212:213], v[244:245] row_newbcast:12 row_mask:0xf bank_mask:0xf\n\t" \
  "v_mov_b64_dpp v[214:215], v[244:245] row_newbcast:14 row_mask:0xf bank_mask:0xf\n\t" \
  "v_smfmac_f32_16x16x64_bf16 v[228:231], v[208:211], v[154:161], %[ix] abid:1\n\t" \
  "v_add_f32_dpp v203, v203, v203 row_bcast:15 row_mask:0xa bank_mask:0xf\n\t" \
  "v_smfmac_f32_16x16x64_bf16 v[232:235], v[208:211], v[162:169], %[ix] abid:1\n\t" \
  "v_smfmac_f32_16x16x64_bf16 v[224:227], v[212:215], v[170:177], %[ix] abid:1\n\t" \
  "v_smfmac_f32_16x16x64_bf16 v[228:231], v[212:215], v[178:185], %[ix] abid:1\n\t" \
  "v_smfmac_f32_16x16x64_bf16 v[232:235], v[212:215], v[186:193], %[ix] abid:1\n\t" \
  "v_add_f32_dpp v203, v203, v203 row_bcast:31 row_mask:0xc bank_mask:0xf\n\ts_lshl_b64 exec, 1, 63\n\tds_write2_b32 v238, v239, v203 offset1:1\n\t" \
  "s_mov_b64 exec, -1\n\ts_mov_b32 s80, 1\n\ts_add_u32 s83, %[oD], 0x20000\n\ts_nop 0\n\tds_write2st64_b32 v237, v224, v228 offset1:1\n\t" \
  "ds_write_b32 v237, v232 offset:512\n\ts_waitcnt lgkmcnt(0)\n\ts_barrier\n\tds_read_b128 v[194:197], v241\n\tds_read_b32 v204, v240\n\t" \
  "v_mov_b32 %[s0], v243\n\ts_waitcnt lgkmcnt(1)\n\t.LCMB%=:\nv_add_f32_dpp v205, v194, v194 row_ror:8 row_mask:0xf bank_mask:0x3\n\t" \
  "v_add_f32_dpp v206, v195, v195 row_ror:8 row_mask:0xf bank_mask:0x3\n\tv_add_f32_dpp v205, v196, v196 row_ror:8 row_mask:0xf bank_mask:0xc\n\t" \
  "v_add_f32_dpp v206, v197, v197 row_ror:8 row_mask:0xf bank_mask:0xc\n\ts_waitcnt lgkmcnt(0)\n\tv_cmp_ne_u32 vcc, 0, v204\n\t" \
  "v_add_f32_dpp v246, v205, v205 row_ror:12 row_mask:0xf bank_mask:0x5\n\tv_add_f32_dpp v246, v206, v206 row_ror:4 row_mask:0xf bank_mask:0xa\n\t" \
  "s_nop 1\n\tv_add_f32_dpp v246, v246, v246 quad_perm:[1,0,3,2] row_mask:0xf bank_mask:0xf\n\ts_cbranch_vccnz .LSLC%=\n\t.LCMS%=:\n" \
  "buffer_store_dword v246, v242, %[rs], s83 offen sc1\n\ts_branch .LLOOP%=\n\t.LWO0%=:\ns_waitcnt lgkmcnt(0)\n\ts_barrier\n\t" \
  "ds_read_b128 v[194:197], v242 offset:%[ov]+0\n\tds_read_b64_tr_b16 v[204:205], v241\n\tds_read_b64_tr_b16 v[206:207], v241 offset:2560\n\t" \
  "ds_read_b64_tr_b16 v[208:209], v241 offset:5120\n\tds_read_b64_tr_b16 v[210:211], v241 offset:7680\n\t" \
  "ds_read_b128 v[198:201], v242 offset:%[ov]+64\n\tds_read_b64_tr_b16 v[212:213], v241 offset:10240\n\t" \
  "ds_read_b64_tr_b16 v[214:215], v241 offset:12800\n\tds_read_b64_tr_b16 v[216:217], v241 offset:15360\n\t" \
  "ds_read_b64_tr_b16 v[218:219], v241 offset:17920\n\tds_read_b128 v[228:231], v242 offset:%[ov]+128\n\t" \
  "ds_read_b64_tr_b16 v[220:221], v241 offset:20480\n\tds_read_b64_tr_b16 v[222:223], v241 offset:23040\n\t" \
  "ds_read_b64_tr_b16 v[224:225], v241 offset:25600\n\tds_read_b64_tr_b16 v[226:227], v241 offset:28160\n\ts_waitcnt lgkmcnt(10)\n\t" \
  "v_smfmac_f32_16x16x64_bf16 v[232:235], v[194:197], v[204:211], %[ix]\n\ts_waitcnt lgkmcnt(5)\n\t" \
  "v_smfmac_f32_16x16x64_bf16 v[232:235], v[198:201], v[212:219], %[ix]\n\ts_waitcnt lgkmcnt(0)\n\t" \
  "v_smfmac_f32_16x16x64_bf16 v[232:235], v[228:231], v[220:227], %[ix]\n\ts_branch .LWOE%=\n\t.LH1X%=:\ns_cmp_eq_u32 %[w], 3\n\t" \
  "s_cbranch_scc1 .LH1S%=\n\ts_cmp_lg_u32 %[w], 7\n\ts_cbranch_scc1 .LH1Y%=\n\ts_mov_b32 s88, %[p1]\n\ts_swappc_b64 s[96:97], s[98:99]\n\t.LH1Y%=:\n" \
  "s_barrier\n\ts_branch .LH1B%=\n\t.LH1S%=:\ns_waitcnt lgkmcnt(0)\n\ts_barrier\n\tbuffer_load_dwordx2 v[202:203], %[ss], %[rs], s82 offen sc1\n\t" \
  "s_waitcnt vmcnt(0)\n\t.LREDS%=:\nv_add_f32_dpp v194, v202, v202 quad_perm:[1,0,3,2] row_mask:0xf bank_mask:0xf\n\t" \
  "v_add_f32_dpp v195, v203, v203 quad_perm:[1,0,3,2] row_mask:0xf bank_mask:0xf\n\ts_nop 0\n\t" \
  "v_add_f32_dpp v194, v194, v194 quad_perm:[2,3,0,1] row_mask:0xf bank_mask:0xf\n\t" \
  "v_add_f32_dpp v195, v195, v195 quad_perm:[2,3,0,1] row_mask:0xf bank_mask:0xf\n\ts_nop 0\n\t" \
  "v_add_f32_dpp v194, v194, v194 row_half_mirror row_mask:0xf bank_mask:0xf\n\t" \
  "v_add_f32_dpp v195, v195, v195 row_half_mirror row_mask:0xf bank_mask:0xf\n\tv_mul_f32 v194, 0x3acccccd, v194\n\tv_mul_f32 v195, 0x3acccccd, v195\n\t" \
  "v_fma_f32 v194, -v195, v195, v194\n\tv_cvt_pk_bf16_f32 v194, 0, v194\n\tv_add_f32 v194, |v194|, %[eps]\n\tv_cvt_pk_bf16_f32 v194, 0, v194\n\t" \
  "v_sqrt_f32 v194, v194\n\ts_nop 0\n\tv_cvt_pk_bf16_f32 v194, 0, v194\n\tv_rcp_f32 v194, v194\n\ts_nop 0\n\tds_write_b32 v239, v194 offset:%[qsw]\n\t" \
  "v_cmp_u_f32 vcc, v194, v194\n\ts_cbranch_vccz .LH1B%=\n\ts_branch .LSLS%=\n\t.LH2M%=:\ns_barrier\n\ts_waitcnt vmcnt(0)\n\ts_branch .LH2B%=\n\t" \
  ".LH3M%=:\ns_waitcnt lgkmcnt(0)\n\ts_barrier\n\ts_branch .LH3E%=\n\t.LH4M%=:\ns_barrier\n\ts_branch .LH4B%=\n\t.LH5M%=:\ns_barrier\n\t" \
  "s_cmp_eq_u32 s80, 0\n\ts_cbranch_scc1 .LH5C%=\n\ts_branch .LFE%=\n\t.LH2X%=:\ns_cmp_eq_u32 %[w], 7\n\ts_cbranch_scc1 .LTW%=\n\ts_barrier\n\t" \
  "s_waitcnt vmcnt(0)\n\ts_barrier\n\ts_add_u32 s83, %[eH], s85\n\ts_cmp_eq_u32 %[w], 6\n\ts_cbranch_scc1 .LSW3%=\n\ts_barrier\n\ts_barrier\n\t" \
  "s_barrier\n\ts_waitcnt vmcnt(0)\n\ts_barrier\n\ts_branch .LH4E%=\n\t.LSW3%=:\ns_barrier\n\ts_barrier\n\t" \
  "ds_read_b128 v[194:197], %[bo] offset:%[xp]\n\tds_read_b128 v[204:207], %[ss]\n\tds_read_b128 v[198:201], %[bo] offset:%[xp]+64\n\t" \
  "ds_read_b128 v[208:211], %[ss] offset:64\n\tds_read_b128 v[228:231], %[bo] offset:%[xp]+128\n\tds_read_b128 v[212:215], %[ss] offset:128\n\t" \
  "s_waitcnt lgkmcnt(4)\n\tv_mfma_f32_16x16x32_bf16 v[232:235], v[194:197], v[204:207], 0\n\ts_waitcnt lgkmcnt(2)\n\t" \
  "v_mfma_f32_16x16x32_bf16 v[232:235], v[198:201], v[208:211], v[232:235]\n\ts_waitcnt lgkmcnt(0)\n\t" \
  "v_mfma_f32_16x16x32_bf16 v[232:235], v[228:231], v[212:215], v[232:235]\n\ts_nop 7\n\tbuffer_store_dword v232, %[dt], %[rs], s83 offen sc1\n\t" \
  "s_barrier\n\ts_barrier\n\ts_and_b32 s88, s80, %[far]\n\ts_cbranch_scc1 .LEND%=\n\ts_mov_b32 s83, %[eQ]\n\ts_barrier\n\ts_cmp_lg_u32 s80, 0\n\t" \
  "s_cbranch_scc1 .LFE%=\n\ts_barrier\n\tds_read_b128 v[194:197], %[bo] offset:%[xp]\n\tds_read_b128 v[204:207], %[ss]\n\t" \
  "ds_read_b128 v[198:201], %[bo] offset:%[xp]+64\n\tds_read_b128 v[208:211], %[ss] offset:64\n\tds_read_b128 v[228:231], %[bo] offset:%[xp]+128\n\t" \
  "ds_read_b128 v[212:215], %[ss] offset:128\n\ts_waitcnt lgkmcnt(4)\n\tv_mfma_f32_16x16x32_bf16 v[232:235], v[194:197], v[204:207], 0\n\t" \
  "s_waitcnt lgkmcnt(2)\n\tv_mfma_f32_16x16x32_bf16 v[232:235], v[198:201], v[208:211], v[232:235]\n\ts_waitcnt lgkmcnt(0)\n\t" \
  "v_mfma_f32_16x16x32_bf16 v[232:235], v[228:231], v[212:215], v[232:235]\n\ts_nop 7\n\tbuffer_store_dword v232, %[dt], %[rs], s83 offen sc1\n\t" \
  "s_branch .LH1%=\n\t.LTW%=:\ns_cmp_lg_u32 s80, 0\n\ts_cbranch_scc1 .LTW1%=\n\ts_memrealtime s[94:95]\n\tv_and_b32 v240, 0x3e0, %[t16]\n\t" \
  "v_lshlrev_b32 v241, 9, %[t16]\n\tv_and_b32 v241, 0x2000, v241\n\tv_cmp_eq_u32 vcc, 0, v240\n\tv_mov_b32 v240, %[oor]\n\t" \
  "v_cndmask_b32 v240, v240, v241, vcc\n\ts_waitcnt lgkmcnt(0)\n\ts_mov_b32 %[tc], s94\n\ts_min_u32 s88, s94, -2\n\tv_mov_b32 v241, s88\n\t" \
  "buffer_store_dword v241, v240, %[rs], %[cw] offen sc1\n\ts_barrier\n\ts_waitcnt vmcnt(1)\n\ts_barrier\n\ts_mov_b32 s87, %[spin]\n\t.LTWP%=:\n" \
  "buffer_load_dwordx4 v[194:197], %[t16], %[rs], %[rK] offen sc1\n\tbuffer_load_dwordx4 v[198:201], %[t16], %[rs], %[rK] offen offset:1024 sc1\n\t" \
  "s_waitcnt vmcnt(0)\n\tv_max_u32 v247, v194, v196\n\tv_max3_u32 v247, v198, v200, v247\n\tv_cmp_eq_u32 vcc, -1, v247\n\ts_cbranch_vccz .LTWQ%=\n\t" \
  "s_sleep 3\n\ts_sub_u32 s87, s87, 1\n\ts_cbranch_scc0 .LTWP%=\n\t.LTWQ%=:\nv_subrev_u32 v194, %[tc], v194\n\tv_subrev_u32 v196, %[tc], v196\n\t" \
  "v_subrev_u32 v198, %[tc], v198\n\tv_subrev_u32 v200, %[tc], v200\n\tv_max_i32 v247, v194, v196\n\tv_max3_i32 v247, v198, v200, v247\n\ts_nop 1\n\t" \
  "v_max_i32_dpp v247, v247, v247 row_ror:1 row_mask:0xf bank_mask:0xf\n\ts_nop 1\n\t" \
  "v_max_i32_dpp v247, v247, v247 row_ror:2 row_mask:0xf bank_mask:0xf\n\ts_nop 1\n\t" \
  "v_max_i32_dpp v247, v247, v247 row_ror:4 row_mask:0xf bank_mask:0xf\n\ts_nop 1\n\t" \
  "v_max_i32_dpp v247, v247, v247 row_ror:8 row_mask:0xf bank_mask:0xf\n\ts_nop 1\n\t" \
  "v_max_i32_dpp v247, v247, v247 row_bcast:15 row_mask:0xa bank_mask:0xf\n\ts_nop 1\n\t" \
  "v_max_i32_dpp v247, v247, v247 row_bcast:31 row_mask:0xc bank_mask:0xf\n\ts_nop 1\n\tv_readlane_b32 s88, v247, 63\n\t" \
  "s_add_u32 %[tc], %[tc], %[p3e]\n\ts_add_u32 %[tc], %[tc], s88\n\ts_mov_b32 %[te], %[tc]\n\ts_add_u32 s94, s98, .LUNT%=-.LCLK%=\n\t" \
  "s_addc_u32 s95, s99, 0\n\ts_swappc_b64 s[96:97], s[94:95]\n\ts_branch .LTW3%=\n\t.LTW1%=:\ns_mov_b32 s88, %[p2]\n\ts_swappc_b64 s[96:97], s[98:99]\n\t" \
  "s_barrier\n\ts_barrier\n\ts_mov_b32 s88, %[p3]\n\ts_swappc_b64 s[96:97], s[98:99]\n\t.LTW3%=:\ns_barrier\n\ts_barrier\n\ts_mov_b32 s88, %[p4]\n\t" \
  "s_swappc_b64 s[96:97], s[98:99]\n\ts_barrier\n\ts_barrier\n\ts_and_b32 s88, s80, %[far]\n\ts_cbranch_scc1 .LEND%=\n\ts_mov_b32 s88, %[p5]\n\t" \
  "s_swappc_b64 s[96:97], s[98:99]\n\ts_barrier\n\ts_cmp_lg_u32 s80, 0\n\ts_cbranch_scc1 .LFE%=\n\ts_barrier\n\ts_branch .LH1%=\n\t.LCLK%=:\n" \
  "s_memrealtime s[94:95]\n\ts_add_u32 %[tc], %[tc], s88\n\ts_movk_i32 s87, 64\n\ts_waitcnt lgkmcnt(0)\n\t.LCL1%=:\ns_sub_i32 s88, %[tc], s94\n\t" \
  "s_cmp_ge_i32 s88, %[rmin]\n\ts_cbranch_scc1 .LUNT%=\n\ts_add_u32 %[tc], %[tc], %[rcons]\n\ts_sub_u32 s87, s87, 1\n\ts_cbranch_scc0 .LCL1%=\n\t" \
  ".LUNT%=:\ns_movk_i32 s87, 0x1000\n\t.LUN1%=:\ns_memrealtime s[94:95]\n\ts_sub_u32 s87, s87, 1\n\ts_cbranch_scc1 .LUN2%=\n\ts_waitcnt lgkmcnt(0)\n\t" \
  "s_sub_i32 s88, %[tc], s94\n\ts_cmp_gt_i32 s88, 3\n\ts_cbranch_scc1 .LUN1%=\n\ts_cmp_lt_i32 s88, 1\n\ts_cbranch_scc1 .LUN2%=\n\ts_nop 3\n\t" \
  "s_cmp_lt_i32 s88, 2\n\ts_cbranch_scc1 .LUN2%=\n\ts_nop 3\n\ts_cmp_lt_i32 s88, 3\n\ts_cbranch_scc1 .LUN2%=\n\ts_nop 9\n\t.LUN2%=:\n" \
  "s_waitcnt lgkmcnt(0)\n\ts_setpc_b64 s[96:97]\n\t.LSL2%=:\ns_add_u32 s88, s80, 2\n\ts_cmp_eq_u32 s84, s88\n\ts_cbranch_scc1 .LH2B%=\n\t" \
  "s_mov_b32 s84, s88\n\ts_mov_b32 s87, %[spin]\n\t.LSL2a%=:\nbuffer_load_dwordx2 v[202:203], %[ss], %[rs], s82 offen sc1\n\t" \
  "buffer_load_dwordx4 v[194:197], %[s0], %[rs], s81 offen sc1\n\tbuffer_load_dwordx4 v[198:201], %[s0], %[rs], s81 offen offset:128 sc1\n\t" \
  "s_waitcnt vmcnt(0)\n\tv_max3_u32 v247, v194, v195, v196\n\tv_max3_u32 v247, v247, v197, v198\n\tv_max3_u32 v247, v247, v199, v200\n\t" \
  "v_max3_u32 v247, v247, v201, v202\n\tv_max_u32 v247, v247, v203\n\tv_cmp_eq_u32 vcc, -1, v247\n\ts_cbranch_vccz .LSL2b%=\n\ts_sub_u32 s87, s87, 1\n\t" \
  "s_cbranch_scc0 .LSL2a%=\n\t.LSL2b%=:\nv_cmp_ne_u32 vcc, 0, v202\n\ts_cbranch_vccz .LRED2%=\n\tv_lshrrev_b32 v247, 5, %[t16]\n\t" \
  "v_and_b32 v247, 28, v247\n\tds_bpermute_b32 v247, v247, v202\n\tv_max_f32_dpp v246, v202, v202 quad_perm:[1,0,3,2] row_mask:0xf bank_mask:0xf\n\t" \
  "s_nop 1\n\tv_max_f32_dpp v246, v246, v246 quad_perm:[2,3,0,1] row_mask:0xf bank_mask:0xf\n\ts_nop 1\n\t" \
  "v_max_f32_dpp v246, v246, v246 row_half_mirror row_mask:0xf bank_mask:0xf\n\ts_waitcnt lgkmcnt(0)\n\tv_sub_f32 v247, v247, v246\n\t" \
  "v_sub_f32 v202, v202, v246\n\tv_exp_f32 v247, v247\n\tv_exp_f32 v202, v202\n\ts_nop 0\n\tv_mul_f32 v194, v247, v194\n\tv_mul_f32 v195, v247, v195\n\t" \
  "v_mul_f32 v196, v247, v196\n\tv_mul_f32 v197, v247, v197\n\tv_mul_f32 v198, v247, v198\n\tv_mul_f32 v199, v247, v199\n\tv_mul_f32 v200, v247, v200\n\t" \
  "v_mul_f32 v201, v247, v201\n\tv_mul_f32 v203, v202, v203\n\tv_mov_b32 v202, 0\n\ts_nop 1\n\ts_branch .LRED2%=\n\t.LSL3%=:\ns_add_u32 s88, s80, 4\n\t" \
  "s_cmp_eq_u32 s84, s88\n\ts_cbranch_scc1 .LH3B%=\n\ts_mov_b32 s84, s88\n\ts_mov_b32 s87, %[spin]\n\t.LSL3a%=:\n" \
  "buffer_load_dwordx4 v[194:197], %[t16], %[rs], s81 offen sc1\n\tbuffer_load_dwordx4 v[198:201], %[t16], %[rs], s81 offen offset:1024 sc1\n\t" \
  "s_waitcnt vmcnt(0)\n\tv_max3_u32 v247, v194, v195, v196\n\tv_max3_u32 v247, v247, v197, v198\n\tv_max3_u32 v247, v247, v199, v200\n\t" \
  "v_max_u32 v247, v247, v201\n\tv_cmp_eq_u32 vcc, -1, v247\n\ts_cbranch_vccz .LSL3b%=\n\ts_sleep 1\n\ts_sub_u32 s87, s87, 1\n\t" \
  "s_cbranch_scc0 .LSL3a%=\n\t.LSL3b%=:\ns_branch .LRED3%=\n\t.LSL4%=:\ns_add_u32 s88, s80, 6\n\ts_cmp_eq_u32 s84, s88\n\ts_cbranch_scc1 .LH4B%=\n\t" \
  "s_mov_b32 s84, s88\n\ts_mov_b32 s87, %[spin]\n\t.LSL4a%=:\nbuffer_load_dwordx2 v[202:203], %[ss], %[rs], s82 offen sc1\n\t" \
  "buffer_load_dwordx4 v[194:197], %[s0], %[rs], s81 offen sc1\n\ts_waitcnt vmcnt(0)\n\tv_max3_u32 v247, v194, v195, v196\n\t" \
  "v_max3_u32 v247, v247, v197, v202\n\tv_max_u32 v247, v247, v203\n\tv_cmp_eq_u32 vcc, -1, v247\n\ts_cbranch_vccz .LSL4b%=\n\ts_sleep 1\n\t" \
  "s_sub_u32 s87, s87, 1\n\ts_cbranch_scc0 .LSL4a%=\n\t.LSL4b%=:\ns_branch .LRED4%=\n\t.LSL5%=:\ns_add_u32 s88, s80, 8\n\ts_cmp_eq_u32 s84, s88\n\t" \
  "s_cbranch_scc1 .LH5B%=\n\ts_mov_b32 s84, s88\n\ts_mov_b32 s87, %[spin]\n\t.LSL5a%=:\nbuffer_load_dwordx4 v[194:197], %[t16], %[rs], s81 offen sc1\n\t" \
  "buffer_load_dwordx4 v[198:201], %[t16], %[rs], s81 offen offset:1024 sc1\n\ts_waitcnt vmcnt(0)\n\tv_max3_u32 v247, v194, v195, v196\n\t" \
  "v_max3_u32 v247, v247, v197, v198\n\tv_max3_u32 v247, v247, v199, v200\n\tv_max_u32 v247, v247, v201\n\tv_cmp_eq_u32 vcc, -1, v247\n\t" \
  "s_cbranch_vccz .LSL5b%=\n\ts_sleep 1\n\ts_sub_u32 s87, s87, 1\n\ts_cbranch_scc0 .LSL5a%=\n\t.LSL5b%=:\ns_branch .LRED5%=\n\t.LSL1%=:\n" \
  "s_add_u32 s88, s80, 10\n\ts_cmp_eq_u32 s84, s88\n\ts_cbranch_scc1 .LH1B%=\n\ts_mov_b32 s84, s88\n\ts_mov_b32 s87, %[spin]\n\t.LSL1a%=:\n" \
  "buffer_load_dwordx2 v[202:203], %[ss], %[rs], s82 offen sc1\n\tbuffer_load_dwordx4 v[194:197], v243, %[rs], s81 offen sc1\n\t" \
  "buffer_load_dwordx4 v[198:201], v243, %[rs], s81 offen offset:128 sc1\n\ts_waitcnt vmcnt(0)\n\tv_max3_u32 v247, v194, v195, v196\n\t" \
  "v_max3_u32 v247, v247, v197, v198\n\tv_max3_u32 v247, v247, v199, v200\n\tv_max3_u32 v247, v247, v201, v202\n\tv_max_u32 v247, v247, v203\n\t" \
  "v_cmp_eq_u32 vcc, -1, v247\n\ts_cbranch_vccz .LSL1b%=\n\ts_sleep 1\n\ts_sub_u32 s87, s87, 1\n\ts_cbranch_scc0 .LSL1a%=\n\t.LSL1b%=:\n" \
  "s_branch .LRED1%=\n\t.LSLS%=:\ns_add_u32 s88, s80, 10\n\ts_cmp_eq_u32 s84, s88\n\ts_cbranch_scc1 .LH1B%=\n\ts_mov_b32 s84, s88\n\t" \
  "s_mov_b32 s87, %[spin]\n\t.LSLSa%=:\nbuffer_load_dwordx2 v[202:203], %[ss], %[rs], s82 offen sc1\n\ts_waitcnt vmcnt(0)\n\t" \
  "v_max3_u32 v247, v202, v203, v203\n\tv_cmp_eq_u32 vcc, -1, v247\n\ts_cbranch_vccz .LSLSb%=\n\ts_sleep 1\n\ts_sub_u32 s87, s87, 1\n\t" \
  "s_cbranch_scc0 .LSLSa%=\n\t.LSLSb%=:\ns_branch .LREDS%=\n\t.LSLM%=:\nv_max_f32_dpp v247, v246, v246 row_ror:1 row_mask:0xf bank_mask:0xf\n\t" \
  "s_nop 1\n\tv_max_f32_dpp v247, v247, v247 row_ror:2 row_mask:0xf bank_mask:0xf\n\ts_nop 1\n\t" \
  "v_max_f32_dpp v247, v247, v247 row_ror:4 row_mask:0xf bank_mask:0xf\n\ts_nop 1\n\t" \
  "v_max_f32_dpp v247, v247, v247 row_ror:8 row_mask:0xf bank_mask:0xf\n\ts_nop 1\n\t" \
  "v_max_f32_dpp v247, v247, v247 row_bcast:15 row_mask:0xa bank_mask:0xf\n\ts_nop 1\n\t" \
  "v_max_f32_dpp v247, v247, v247 row_bcast:31 row_mask:0xc bank_mask:0xf\n\ts_nop 1\n\tv_readlane_b32 s88, v247, 63\n\ts_nop 3\n\t" \
  "v_mov_b32 v239, s88\n\tv_sub_f32 v246, v246, v239\n\tv_exp_f32 v203, v246\n\ts_nop 0\n\tv_cvt_pk_bf16_f32 v244, v203, 0\n\t" \
  "v_add_f32_dpp v203, v203, v203 row_ror:1 row_mask:0xf bank_mask:0xf\n\ts_nop 0\n\tv_mov_b32_dpp v245, v244 row_shl:1 row_mask:0xf bank_mask:0xf\n\t" \
  "s_branch .LPV%=\n\t.LSLC%=:\nv_max_f32_dpp v247, v204, v204 row_ror:8 row_mask:0xf bank_mask:0xf\n\ts_nop 1\n\t" \
  "v_max_f32_dpp v247, v247, v247 row_ror:4 row_mask:0xf bank_mask:0xf\n\ts_nop 1\n\t" \
  "v_max_f32_dpp v247, v247, v247 quad_perm:[1,0,3,2] row_mask:0xf bank_mask:0xf\n\tv_sub_f32 v204, v204, v247\n\tv_exp_f32 v204, v204\n\ts_nop 0\n\t" \
  "v_mul_f32 v194, v204, v194\n\tv_mul_f32 v195, v204, v195\n\tv_mul_f32 v196, v204, v196\n\tv_mul_f32 v197, v204, v197\n\ts_nop 0\n\t" \
  "v_add_f32_dpp v205, v194, v194 row_ror:8 row_mask:0xf bank_mask:0x3\n\tv_add_f32_dpp v206, v195, v195 row_ror:8 row_mask:0xf bank_mask:0x3\n\t" \
  "v_add_f32_dpp v205, v196, v196 row_ror:8 row_mask:0xf bank_mask:0xc\n\tv_add_f32_dpp v206, v197, v197 row_ror:8 row_mask:0xf bank_mask:0xc\n\t" \
  "s_nop 0\n\tv_add_f32_dpp v246, v205, v205 row_ror:12 row_mask:0xf bank_mask:0x5\n\t" \
  "v_add_f32_dpp v246, v206, v206 row_ror:4 row_mask:0xf bank_mask:0xa\n\ts_nop 1\n\t" \
  "v_add_f32_dpp v246, v246, v246 quad_perm:[1,0,3,2] row_mask:0xf bank_mask:0xf\n\ts_cmp_lg_u32 %[w], 6\n\ts_cbranch_scc1 .LCMS%=\n\t" \
  "v_cmp_eq_u32 vcc, 0, v242\n\tv_cndmask_b32 v246, v246, v247, vcc\n\ts_branch .LCMS%=\n\t.LFIN%=:\n" \
  "buffer_store_short v243, v241, %[ro], %[oo] offen\n\t.LFE%=:\n.LEND%=:\n"
static_assert(BLK == 16384 && NT == 512 && QS == 400 && L_SM == 0 && TW == 7 && SW == 6, "constants of the hand-written part");

__global__ void __launch_bounds__(NT, 2)
mega_kernel(const uint16_t* __restrict__ x_in, const uint16_t* __restrict__ kc, const uint16_t* __restrict__ vc,
            const uint16_t* __restrict__ wq, const uint16_t* __restrict__ wo, const uint16_t* __restrict__ w1,
            const uint16_t* __restrict__ b1, const uint16_t* __restrict__ w2, const uint16_t* __restrict__ b2,
            uint16_t* __restrict__ out, unsigned* epoch SCHED_ARG) {
  extern __shared__ __attribute__((aligned(16))) char lds[];
  uint32_t cp;
  const uint64_t code0 = __builtin_amdgcn_s_getpc() & ~(uint64_t)127;
  asm volatile("global_load_dword %0, %1, %2" : "=v"(cp) : "v"(min((int)((blockIdx.x >> 3) * 1024 + (threadIdx.x >> 6) * 128), CODEPF * 1024 - 128)),
               "s"(code0) : "memory");
  __builtin_amdgcn_sched_barrier(0);
  const int tid0 = threadIdx.x, w0 = __builtin_amdgcn_readfirstlane(tid0 >> 6);
  const int b0 = blockIdx.x;
  unsigned long long* gran_base = (unsigned long long*)epoch + 512;
  char* tail = (char*)(((uintptr_t)epoch + (4u << 20)) & ~(uintptr_t)((2u << 20) - 1));
  const auto rs = __builtin_amdgcn_make_buffer_rsrc(tail, 0, 128 * BLK, 0x00020000);
  auto ld16 = [&](int voff, int so) -> u32x4 { return __builtin_amdgcn_raw_buffer_load_b128(rs, voff, so, 16); };
  auto ld8 = [&](int voff, int so) -> u32x2 { return __builtin_amdgcn_raw_buffer_load_b64(rs, voff, so, 16); };
  auto ld4 = [&](int voff, int so) -> uint32_t { return __builtin_amdgcn_raw_buffer_load_b32(rs, voff, so, 16); };
  auto st16 = [&](int voff, int so, const f32x4& v) { __builtin_amdgcn_raw_buffer_store_b128(__builtin_bit_cast(u32x4, v), rs, voff, so, 16); };
  auto st4 = [&](int voff, int so, float a) { __builtin_amdgcn_raw_buffer_store_b32(__float_as_uint(a), rs, voff, so, 16); };
  auto trd = [&](int off) -> u32x2 {
    return __builtin_bit_cast(u32x2, __builtin_amdgcn_ds_read_tr16_b64_v4i16((LAS s16x4*)(uintptr_t)(uint32_t)off));
  };
  auto lrd16 = [&](int off) -> u32x4 { return *(const LAS u32x4*)(uintptr_t)(uint32_t)off; };
  auto lrd8 = [&](int off) -> u32x2 { return *(const LAS u32x2*)(uintptr_t)(uint32_t)off; };
  auto lrd4 = [&](int off) -> uint32_t { return *(const LAS uint32_t*)(uintptr_t)(uint32_t)off; };
  auto lwr2 = [&](int off, uint32_t v) { *(LAS uint16_t*)(uintptr_t)(uint32_t)off = (uint16_t)v; };
  auto lwr4 = [&](int off, uint32_t v) { *(LAS uint32_t*)(uintptr_t)(uint32_t)off = v; };
  constexpr int sm = L_SM;

  uint32_t tag;
  uint32_t tE = 0;
  uint32_t tcur = 0;
  int pm;
  u32x8 kt[12], vt[12];
  uint32_t ix;
  int t16, bo, sS, dT;

  auto qk_softmax = [&](float& mx, float& ev, f32x4 (&dv)[3], int qb, float qs, bool cs = false) -> uint32_t {
    u32x4 qa[3];
    f32x4 d[4];
#pragma unroll
    for (int u = 0; u < 3; ++u) qa[u] = lrd16(qb + 64 * u);
#pragma unroll
    for (int a = 0; a < 1; ++a) { ZERO4(d[0], 204, 205, 206, 207); ZERO4(d[1], 208, 209, 210, 211); ZERO4(d[2], 212, 213, 214, 215); ZERO4(d[3], 216, 217, 218, 219); }
    SB();
#pragma unroll
    for (int u = 0; u < 3; ++u)
#pragma unroll
      for (int a = 0; a < 4; ++a) {
        d[a] = smf<0>(qa[u], kt[3 * a + u], d[a], ix);
        SB();
        if (u == 1 && a == 0) ZERO4(dv[0], 220, 221, 222, 223);
        if (u == 1 && a == 2) ZERO4(dv[1], 224, 225, 226, 227);
        if (u == 2 && a == 0) ZERO4(dv[2], 228, 229, 230, 231);
        SB();
      }
    __builtin_amdgcn_s_setprio(1);
    const bool same = __builtin_amdgcn_inverse_ballot_w64(0xff00ff0000ff00ffull);
    const float f0 = same ? d[0][0] + d[0][1] : d[1][0] + d[1][1], f1 = same ? d[2][0] + d[2][1] : d[3][0] + d[3][1];
    CSTAMP(cs, 41);
    auto r = __builtin_amdgcn_permlane32_swap(__float_as_uint(f0), __float_as_uint(f1), false, false);
    const float sraw = uf(r[0]) + dppv<0x128>(uf(r[1]));
    const float sv = rbf(rbf(sraw) * 0.07216878364870322f) * LOG2E;
    uint64_t big = __builtin_amdgcn_ballot_w64(!(fabsf(sv) <= SMALL));
    PINS(big);
    SB();
    mx = 0.f;
    ev = __builtin_amdgcn_exp2f(sv);
    uint32_t pb = pk2(ev, 0.f);
    PIN1(pb);
    SB();
    if (__builtin_expect(big != 0, 0)) {
      float mxv;
      asm volatile("s_nop 1\n\t"
                   "v_max_f32_dpp %0, %1, %1 row_ror:1 row_mask:0xf bank_mask:0xf\n\ts_nop 1\n\t"
                   "v_max_f32_dpp %0, %0, %0 row_ror:2 row_mask:0xf bank_mask:0xf\n\ts_nop 1\n\t"
                   "v_max_f32_dpp %0, %0, %0 row_ror:4 row_mask:0xf bank_mask:0xf\n\ts_nop 1\n\t"
                   "v_max_f32_dpp %0, %0, %0 row_ror:8 row_mask:0xf bank_mask:0xf\n\ts_nop 1\n\t"
                   "v_max_f32_dpp %0, %0, %0 row_bcast:15 row_mask:0xa bank_mask:0xf\n\ts_nop 1\n\t"
                   "v_max_f32_dpp %0, %0, %0 row_bcast:31 row_mask:0xc bank_mask:0xf\n\ts_nop 0"
                   : "=&v"(mxv) : "v"(sv));
      mx = __int_as_float(__builtin_amdgcn_readlane(__float_as_int(mxv), 63));
      ev = __builtin_amdgcn_exp2f(sv - mx);
      pb = pk2(ev, 0.f);
    }
    __builtin_amdgcn_s_setprio(0);
    CSTAMP(cs, 42);
    return pb;
  };
  auto pv_combine = [&](const u32x4& cT, int od, bool sw, bool cs = false) {
    CSTAMP(cs, 45);
    const uint32_t m = lrd4(cT.x);
    u32x4 rv = lrd16(cT.y);
    SB();
    uint64_t shifted = __builtin_amdgcn_ballot_w64(m != 0);
    PINS(shifted);
    SB();
    float o = sum_b320(rv);
    PIN1(o);
    SB();
    if (__builtin_expect(shifted != 0, 0)) {
      float M = fmaxf(uf(m), dppv<0x128>(uf(m)));
      M = fmaxf(M, dppv<0x124>(M));
      M = fmaxf(M, dppv<0xb1>(M));
      const float f = __builtin_amdgcn_exp2f(uf(m) - M);
      rv = __builtin_bit_cast(u32x4, __builtin_bit_cast(f32x4, rv) * f);
      asm volatile("s_nop 1" : "+v"(rv));
      o = sum_b320(rv);
      if (sw && (t16 & 0xc0) == 0) o = M;
    }
    st4(cT.z, od, o);
    CSTAMP(cs, 46);
  };
  int s0;
  int oG, oD, rC, eC, rH, eH, eQ, rK;
  auto rbase = [&](int x) { return (x & 3) * (NB * BLK) + ((pm >> x) & 1) * 8192; };

  {
    const int w = w0, b = b0, lane = tid0 & 63;
    const int g = (b & 7) * 4 + (b >> 6), j = (b >> 3) & 7, jg = b >> 3, jj = b & 7;
    const int ck = lane & 7, jp = lane >> 3, n16 = lane & 15, kq = lane >> 4;
    const int sub = (lane >> 5) + 2 * ((lane >> 4) & 1), trl = 4 * kq + (n16 >> 2), trc = 8 * (lane & 3);
    const int xc = min(16 * w + 4 * (lane & 3) + sub, 79);
    const int gc = 64 * (w < 3 ? w : 0) + 4 * (lane & 8) + 4 * ck + sub, hc = (w & 1) ? 32 + 4 * (lane & 3) + sub : 4 * ck + sub;
    gu64* G = (gu64*)gran_base;
    STAMPV(0);
    const size_t row0 = (size_t)g * SK + (SK - WIN) + j * 64 + w * 8;
    const uint16_t *kp = kc + row0 * DH, *vp = vc + row0 * DH;
    const int rot = g, hx = (g >> 2) & 1;
    const int vo = ((kq >> 1) ^ hx) * (2048 * 384) + (kq & 1) * 384 + n16 * 16, vo2 = vo + 256 - n16 * 8;
    const int ko = ((lane & 4) + ((lane & 1) << 1) + ((lane >> 1) & 1)) * 384 + (lane & 8) * 8 + kq * 16, ko2 = ko ^ 64;
#define GRP(lo, hi) "+{v[" #lo ":" #hi "]}"
#define LOADK(t, r0, r1, r2, g0, g1, g2) if constexpr (T == t) \
      asm volatile("global_load_dwordx4 " r0 ", %3, %4 nt\n\ts_waitcnt vmcnt(%5)\n\tglobal_load_dwordx4 " r1 ", %3, %4 offset:128 nt\n\ts_waitcnt vmcnt(%5)\n\t" \
                   "global_load_dwordx4 " r2 ", %3, %4 offset:256 nt\n\ts_waitcnt vmcnt(%5)" \
                   : g0(kt[a]), g1(kt[a + 1]), g2(kt[a + 2]) : "v"(o), "s"(p), "n"(decltype(WN)::value) : "memory")
    auto load_k = [&](auto Tc, auto WN) {
      constexpr int T = decltype(Tc)::value, a = 3 * (T >> 1);
      const int o = (T & 2) ? ko2 : ko;
      const uint16_t* p = kp + ((((T >> 1) + rot) & 3) * 512 + ((T & 1) ^ hx) * 2048) * DH;
      if (!(T & 1)) asm volatile("" : "=v"(kt[a]), "=v"(kt[a + 1]), "=v"(kt[a + 2]));
      LOADK(0, "v[2:5]", "v[10:13]", "v[18:21]", GRP(2, 9), GRP(10, 17), GRP(18, 25));
      LOADK(1, "v[6:9]", "v[14:17]", "v[22:25]", GRP(2, 9), GRP(10, 17), GRP(18, 25));
      LOADK(2, "v[26:29]", "v[34:37]", "v[42:45]", GRP(26, 33), GRP(34, 41), GRP(42, 49));
      LOADK(3, "v[30:33]", "v[38:41]", "v[46:49]", GRP(26, 33), GRP(34, 41), GRP(42, 49));
      LOADK(4, "v[50:53]", "v[58:61]", "v[66:69]", GRP(50, 57), GRP(58, 65), GRP(66, 73));
      LOADK(5, "v[54:57]", "v[62:65]", "v[70:73]", GRP(50, 57), GRP(58, 65), GRP(66, 73));
      LOADK(6, "v[74:77]", "v[82:85]", "v[90:93]", GRP(74, 81), GRP(82, 89), GRP(90, 97));
      LOADK(7, "v[78:81]", "v[86:89]", "v[94:97]", GRP(74, 81), GRP(82, 89), GRP(90, 97));
      SB();
    };
#define LOADV(v, x0, y0, x1, y1, g0, g1, g2) if constexpr (V == v) \
      asm volatile("global_load_dwordx4 " x0 ", %3, %5 nt\n\tglobal_load_dwordx2 " y0 ", %4, %5 nt\n\ts_waitcnt vmcnt(%6)\n\t" \
                   "global_load_dwordx4 " x1 ", %3, %5 offset:768 nt\n\tglobal_load_dwordx2 " y1 ", %4, %5 offset:768 nt\n\ts_waitcnt vmcnt(%6)" \
                   : g0(vt[a]), g1(vt[a + 1]), g2(vt[a + 2]) : "v"(vo), "v"(vo2), "s"(p), "n"(decltype(WN)::value) : "memory")
    auto load_v = [&](auto Vc, auto WN) {
      constexpr int V = decltype(Vc)::value, a = 3 * (V >> 1);
      const uint16_t* p = vp + (((2 * (V & 1) + (V >> 2) + rot) & 3) * 512 + ((V >> 1) & 1) * 4) * DH;
      if (!(V & 1)) asm volatile("" : "=v"(vt[a]), "=v"(vt[a + 1]), "=v"(vt[a + 2]));
      LOADV(0, "v[98:101]", "v[114:115]", "v[106:109]", "v[116:117]", GRP(98, 105), GRP(106, 113), GRP(114, 121));
      LOADV(1, "v[102:105]", "v[118:119]", "v[110:113]", "v[120:121]", GRP(98, 105), GRP(106, 113), GRP(114, 121));
      LOADV(2, "v[122:125]", "v[138:139]", "v[130:133]", "v[140:141]", GRP(122, 129), GRP(130, 137), GRP(138, 145));
      LOADV(3, "v[126:129]", "v[142:143]", "v[134:137]", "v[144:145]", GRP(122, 129), GRP(130, 137), GRP(138, 145));
      LOADV(4, "v[146:149]", "v[162:163]", "v[154:157]", "v[164:165]", GRP(146, 153), GRP(154, 161), GRP(162, 169));
      LOADV(5, "v[150:153]", "v[166:167]", "v[158:161]", "v[168:169]", GRP(146, 153), GRP(154, 161), GRP(162, 169));
      LOADV(6, "v[170:173]", "v[186:187]", "v[178:181]", "v[188:189]", GRP(170, 177), GRP(178, 185), GRP(186, 193));
      LOADV(7, "v[174:177]", "v[190:191]", "v[182:185]", "v[192:193]", GRP(170, 177), GRP(178, 185), GRP(186, 193));
      SB();
    };
    int go[5];
    auto wo_prep = [&]() {
      int l = lane;
      PIN1(l);
#pragma unroll
      for (int i = 0; i < 5; ++i) { const int c = 64 * i + l, row = (c * 205) >> 11; go[i] = row * (2 * DM) + (c - 10 * row) * 16; PIN1(go[i]); }
    };
    auto load_wo = [&]() {
      const uint16_t* src = (w < 6 ? wo + (size_t)(192 * g + 32 * w) * DM : w2 + (size_t)(48 * g + 16 * (w - 6)) * DM) + 80 * j;
      const int dst = w < 7 ? L_WO + w * 5120 : L_W2 + 2560;
      unsigned keep;
      asm volatile("s_mov_b32 %0, m0\n\ts_mov_b32 m0, %7\n\ts_nop 0\n\tglobal_load_lds_dwordx4 %1, %6 nt\n\t"
                   "s_add_u32 m0, m0, 0x400\n\ts_nop 0\n\tglobal_load_lds_dwordx4 %2, %6 nt\n\t"
                   "s_add_u32 m0, m0, 0x400\n\ts_nop 0\n\tglobal_load_lds_dwordx4 %3, %6 nt\n\t"
                   "s_add_u32 m0, m0, 0x400\n\ts_nop 0\n\tglobal_load_lds_dwordx4 %4, %6 nt\n\t"
                   "s_add_u32 m0, m0, 0x400\n\ts_nop 0\n\tglobal_load_lds_dwordx4 %5, %6 nt\n\t"
                   "s_mov_b32 m0, %0"
                   : "=&s"(keep) : "v"(go[0]), "v"(go[1]), "v"(go[2]), "v"(go[3]), "v"(go[4]), "s"(src), "s"(dst) : "memory", "scc");
      SB();
    };
    load_v(IC<0>{}, IC<63>{});
    ESTAMP(56);
    uint32_t tp = rt32() + SC0(PO);
    int pstep = SC0(PT);
    if (w != TW) tp -= 1 << 20;
    int rix = 40;
    auto pace = [&]() {
      int rem;
      asm volatile("1:\n\ts_memrealtime vcc\n\ts_waitcnt lgkmcnt(0)\n\ts_sub_i32 %0, %1, vcc_lo\n\ts_cmp_lt_u32 %0, 0x800\n\ts_cbranch_scc1 1b\n\ts_add_u32 %1, %1, %2"
                   : "=&s"(rem), "+s"(tp) : "s"(pstep) : "memory", "scc", "vcc");
    };
    auto tile = [&](auto Ic, auto Xc) {
      constexpr int i = decltype(Ic)::value, x = decltype(Xc)::value;
      if constexpr (i <= 8) load_k(IC<kord(i - 1)>{}, IC<KWIN + x>{});
      else if constexpr (i < 15) load_v(IC<vord(i - 8)>{}, IC<VWIN + x>{});
      else if constexpr (i == 15) { wo_prep(); SB(); load_v(IC<7>{}, IC<63>{}); BAR0(); load_wo(); }
    };
    auto paddr = [&](int o) { return (o >> 13) * BLK + (w >> 2) * 8192 + (o & 8191); };
    uint32_t tv = 0;
    PINS(tv);
    if (w == SW) asm volatile("s_load_dword %0, %1, 0x80" : "+s"(tv) : "s"(tail + paddr(4 * NB * 8192 + b * 1024)) : "memory");
    SB();
    uint32_t ep = 0, xf[5] = {0, 0, 0, 0, 0}, xs = 0;
    asm volatile("" : "+v"(xf[0]), "+v"(xf[1]), "+v"(xf[2]), "+v"(xf[3]), "+v"(xf[4]), "+v"(xs), "+v"(ep));
    if (w == 0)
      asm volatile("global_load_dword %0, %7, %8\n\tglobal_load_dword %1, %7, %8 offset:256\n\tglobal_load_dword %2, %7, %8 offset:512\n\t"
                   "global_load_dword %3, %7, %8 offset:768\n\tglobal_load_dword %4, %7, %8 offset:1024\n\tglobal_load_dword %5, %9, %8\n\tglobal_load_dword %6, %10, %11 sc1"
                   : "+v"(xf[0]), "+v"(xf[1]), "+v"(xf[2]), "+v"(xf[3]), "+v"(xf[4]), "+v"(xs), "+v"(ep)
                   : "v"(4 * lane), "s"(x_in), "v"(160 * jj + 4 * min(lane, 39)), "v"(0), "s"(epoch) : "memory");
    SB();
    {
      int go[4];
#pragma unroll
      for (int r = 0; r < 4; ++r) { const int c = min(64 * r + lane, 249), row = (c * 41) >> 10; go[r] = row * (2 * QD) + min(c - 25 * row, 23) * 16; }
      unsigned keep;
      asm volatile("s_mov_b32 %0, m0\n\ts_mov_b32 m0, %6\n\ts_nop 0\n\tglobal_load_lds_dwordx4 %1, %5 nt\n\t"
                   "s_add_u32 m0, m0, 0x400\n\ts_nop 0\n\tglobal_load_lds_dwordx4 %2, %5 nt\n\t"
                   "s_add_u32 m0, m0, 0x400\n\ts_nop 0\n\tglobal_load_lds_dwordx4 %3, %5 nt\n\t"
                   "s_add_u32 m0, m0, 0x400\n\ts_mov_b64 exec, %7\n\tglobal_load_lds_dwordx4 %4, %5 nt\n\t"
                   "s_mov_b64 exec, -1\n\ts_mov_b32 m0, %0"
                   : "=&s"(keep) : "v"(go[0]), "v"(go[1]), "v"(go[2]), "v"(go[3]),
                     "s"(wq + (size_t)(80 * jj + 10 * w) * QD + 192 * jg), "s"(L_WQ + w * 10 * QS), "s"((1ull << 58) - 1) : "memory", "scc");
    }
    SB();
    ESTAMP(57);
    BAR0();
    ESTAMP(58);
    load_k(IC<0>{}, IC<63>{});
    ESTAMP(59);
    auto probe = [&](auto Oc) -> uint32_t {
      unsigned long long c0, c1; uint32_t v;
      asm volatile("s_memtime %0\n\ts_waitcnt lgkmcnt(0)\n\ts_load_dword %2, %3, %4\n\ts_waitcnt lgkmcnt(0)\n\ts_memtime %1\n\ts_waitcnt lgkmcnt(0)"
                   : "=&s"(c0), "=&s"(c1), "=&s"(v) : "s"(tail + paddr(4 * NB * 8192 + b * 1024 + (w & 3) * 256)), "n"(decltype(Oc)::value) : "memory");
      return (uint32_t)(c1 - c0);
    };
    {
      t16 = w * 1024 + lane * 16; bo = sm + kq * 16;
      sS = w == SW ? (n16 == 0 ? sm + S_XP + kq * 16 : L_ONE) : ck * 32;
      dT = lane < (w == SW ? 2 : 16) ? lane * 4 : OOR;
      LAS u32x4* T = (LAS u32x4*)(uintptr_t)(uint32_t)(L_TAB + t16);
      const int pk = ((lane >> 1) & 6) | (lane & 1), c4 = 8 * w + ((lane >> 3) & 6) + ((lane >> 1) & 1);
      T[0] = (u32x4){(uint32_t)(sm + S_ML + pk * 16), (uint32_t)(w < 6 ? sm + S_RED + pk * RS + c4 * 16 : sm + S_ML + pk * 16),
                     (uint32_t)(w == SW ? ((lane & ~4) == 0 ? lane : OOR) : (!(lane & 1) && w < 6) ? c4 * 16 + (lane & 12) : OOR), (uint32_t)(jp * 768 + w * 256 + ck * 16)};
      T[5 * NT] = (u32x4){(uint32_t)((kq >> 1) == ((n16 >> 2) & 1) ? sm + S_QB + (n16 & 1) * 192 + (n16 & 8) * 4 + (kq & 1) * 16 : sm + S_ZQ),
                          (uint32_t)(sm + S_RED + w * RS + lane * 4), (uint32_t)(sm + S_ML + w * 16), 0u};
      ix = ((n16 & 1) ? 0xeeeeu : 0x4444u) | (n16 >> 2) * 0x55550000u;
      const int gd = (gc >> 4) & 3, dimo = gd + (gc < 128 ? 8 * (gc & 15) + 4 * (gc >> 6) : 128 + 4 * (gc & 15));
      if (w == 1 && lane < 12) *(LAS u32x4*)(uintptr_t)(uint32_t)(sm + S_ZQ + lane * 16) = (u32x4){0, 0, 0, 0};
      T[NT] = (u32x4){(uint32_t)(sm + S_OV + opos(dimo)), (uint32_t)(L_WO + (w < 5 ? w : 0) * 32 + trl * 160 + trc),
                      (uint32_t)(sm + S_XP + 2 * kpos(xc)), (uint32_t)(L_W1 + (w < 3 ? w : 0) * 32 + trl * 96 + trc)};
      T[2 * NT] = (u32x4){(uint32_t)(sm + S_HV + 2 * kpos(hc)), (uint32_t)(L_W2 + (w < 5 ? w : 0) * 32 + trl * 160 + trc),
                          (uint32_t)(L_CS + 768 + 4 * hc), (uint32_t)((w & 1) ? 1024 + jp * 64 + (ck & 3) * 16 : lane * 16)};
      T[3 * NT] = (u32x4){(uint32_t)(sm + S_QB + qpos(gc)), (uint32_t)(L_CS + 4 * gc), (uint32_t)(L_WQ + (w < 6 ? w : 0) * 64 + trl * QS + trc), (uint32_t)(2 * xc)};
    }
    ESTAMP(60);
    SB();
    {
      const int c = min(lane, 59), row = (c * 43) >> 8;
      unsigned keep;
      asm volatile("s_mov_b32 %0, m0\n\ts_mov_b32 m0, %3\n\ts_mov_b64 exec, %4\n\tglobal_load_lds_dwordx4 %1, %2 nt\n\ts_mov_b64 exec, -1\n\ts_mov_b32 m0, %0"
                   : "=&s"(keep) : "v"(row * (2 * FF) + (c - 6 * row) * 16), "s"(w1 + (size_t)(80 * jj + 10 * w) * FF + 48 * jg), "s"(L_W1 + w * 960), "s"((1ull << 60) - 1) : "memory");
    }
    SB();
#pragma unroll
    for (int t = 1; t < UP - 1; ++t) { BAR0(); if (t == 1) load_k(IC<kord(1)>{}, IC<63>{}); else load_k(IC<kord(2)>{}, IC<63>{}); }
    ESTAMP(61);
    asm volatile("s_waitcnt vmcnt(%8)" : "+v"(ep), "+v"(xf[0]), "+v"(xf[1]), "+v"(xf[2]), "+v"(xf[3]), "+v"(xf[4]), "+v"(xs), "+v"(cp) : "n"(3 * UP + 2) : "memory");
    if (w == 0) {
      float s1 = 0.f;
#pragma unroll
      for (int i = 0; i < 5; ++i) s1 += lo2f(xf[i]) + hi2f(xf[i]);
      const float mean = wsum_bcast(s1) * (1.0f / DM);
      float q0 = 0.f;
#pragma unroll
      for (int i = 0; i < 5; ++i) { const float d0 = lo2f(xf[i]) - mean, d1 = hi2f(xf[i]) - mean; q0 = fmaf(d0, d0, q0); q0 = fmaf(d1, d1, q0); }
      const float var = wsum_bcast(q0) * (1.0f / DM);
      const uint32_t mv = pk2(mean, var);
      const float rinv = __builtin_amdgcn_rcpf(rbf(__builtin_amdgcn_sqrtf(rbf(hi2f(mv) + 1e-5f))));
      if (lane < 48) lwr4(sm + S_XP + 2 * kpos(2 * lane), lane < 40 ? pk2(rbf(lo2f(xs) - lo2f(mv)) * rinv, rbf(hi2f(xs) - lo2f(mv)) * rinv) : 0u);
      if (lane < 48) lwr4(L_ONE + lane * 4, 0x3f803f80u);
      if (lane < 16) lwr2(sm + S_HV + 2 * kpos(48 + lane), 0);
      lwr4(L_FLAG, ep * 1664525u + 1013904223u);
    }
    if (!(XOFF & 8)) __builtin_amdgcn_raw_buffer_store_b128((u32x4){SENT, SENT, SENT, SENT}, rs,
                                           lane >= 56 ? paddr(4 * NB * 8192 + b * 1024 + (w & 3) * 256 + (lane - 56) * 16) : (w & 3) != 3 ? paddr((3 * b + (w & 3)) * 896 + lane * 16) : OOR, 0, 16);
    SB();
    asm volatile("s_waitcnt vmcnt(%0)" :: "n"(3 * UP - 1) : "memory");
    STAMPV(1);
    lds_barrier();
    PINS(tv);
    tile(IC<UP>{}, IC<1>{});
    tag = __builtin_amdgcn_readfirstlane(lrd4(L_FLAG));
    auto partial = [&](int tl, auto Q) {
      constexpr int rstr = decltype(Q)::value ? QS : 96;
      const int xb = n16 >= 4 ? L_ONE : sm + S_XP + kq * 16;
      const u32x4 x0 = lrd16(xb), x1 = lrd16(xb + 64), x2 = lrd16(xb + 128);
      gu64* dst = G + G_P + b * RP + (kq == 0 ? 0 : 192) + n16;
      const int a0 = (decltype(Q)::value ? L_WQ : L_W1 - 12 * 32) + tl * 32 + trl * rstr + trc;
      f32x4 d = {0.f, 0.f, 0.f, 0.f};
      d = mfma32(x0, cat(trd(a0), trd(a0 + 16 * rstr)), d);
      d = mfma32(x1, cat(trd(a0 + 32 * rstr), trd(a0 + 48 * rstr)), d);
      d = mfma32(x2, cat(trd(a0 + 64 * rstr), (u32x2){0, 0}), d);
      if (!(XOFF & 16)) if (kq == 1 || (kq == 0 && decltype(Q)::value)) __hip_atomic_store(dst + 16 * tl, ((unsigned long long)tag << 32) | __float_as_uint(d[0]), RLX_AGENT);
    };
    partial(w, IC<1>{});
    if (w < 4) partial(8 + w, IC<1>{});
    SB();
    asm volatile("s_waitcnt vmcnt(%0)" :: "n"(3 * UP - 2) : "memory");
    STAMPV(2);
    pace();
    BAR0();
    tile(IC<RT>{}, IC<3>{});
    if (!(XOFF & 1)) if ((w & 3) == 0) lwr4(L_FLAG + 32 + 4 * w, probe(IC<0>{}));
    if (w == 3 || w >= 6) partial(w == 3 ? 12 : w + 7, IC<0>{});
    pace();
    BAR0();
    tile(IC<RT + 1>{}, IC<3>{});
    if (!(XOFF & 1)) if ((w & 3) == 1) lwr4(L_FLAG + 32 + 4 * w, probe(IC<0>{}));
    pace();
    BAR0();
    tile(IC<RT + 2>{}, IC<0>{});
    if (!(XOFF & 1)) if ((w & 3) == 2) lwr4(L_FLAG + 32 + 4 * w, probe(IC<0>{}));
    asm volatile("s_waitcnt vmcnt(%0)" :: "n"(nreq(UP, RT + 2)) : "memory");
    pace();
    lds_barrier();
    u32x4 er = {0, 0, 0, 0};
    asm volatile("" : "+v"(er));
    auto ereq = [&](const char* src) { if (XOFF & 32) return; asm volatile("global_load_dwordx4 %0, %1, off sc1" : "+v"(er) : "v"(src) : "memory"); };
    auto efin = [&](auto Nc, const char* src) {
      if (XOFF & 2) return;
      asm volatile("s_waitcnt vmcnt(%1)" : "+v"(er) : "n"(decltype(Nc)::value) : "memory");
      if (XOFF & 4) return;
#pragma unroll 1
      for (unsigned sp = 0; sp < SPIN_LIMIT; ++sp) {
        if (__all(tagbad(er, tag) == 0)) break;
        __builtin_amdgcn_s_sleep(4);
        asm volatile("global_load_dwordx4 %0, %1, off sc1\n\ts_waitcnt vmcnt(0)" : "+v"(er) : "v"(src) : "memory");
      }
    };
    {
      const int un = 4 * j + w;
      const bool mine = w < 4 && un < 27;
      const char* src = (const char*)(G + G_P + (8 * g + jp) * RP) + un * 128 + ck * 16;
      if (mine) ereq(src);
      SB();
      tile(IC<RT + 3>{}, IC<0>{});
      if (w == 7) {
        unsigned xcc;
        asm volatile("s_getreg_b32 %0, hwreg(HW_REG_XCC_ID)" : "=s"(xcc));
        const u32x4 l0 = lrd16(L_FLAG + 32), l1 = lrd16(L_FLAG + 48);
        const uint32_t vote = ((min(min(l1.x, l1.y), l1.z) < min(min(l0.x, l0.y), l0.z) ? 1u : 0u) ^ (xcc >> 2)) & 1u;
        if (lane == 0) __hip_atomic_store(G + G_F + b, ((unsigned long long)tag << 32) | vote | ((xcc >> 1) & 2) | (rt32() << 2), RLX_AGENT);
      }
      SB();
      pace();
      BAR0();
      tile(IC<RT + 4>{}, IC<0>{});
      if (mine) {
        efin(IC<nreq(RT + 3, RT + 4)>{}, src);
        const float t = swapadd32(uf(er.x), uf(er.z));
        const float v = ror8add(swapadd16(t, t));
        if (!(lane & 24)) __hip_atomic_store(G + G_R + g * RP + un * 16 + 2 * ck + (lane >> 5), ((unsigned long long)tag << 32) | __float_as_uint(v), RLX_AGENT);
      }
      SB();
      pace();
      BAR0();
    }
    uint32_t xv, bv2, bv1;
    asm volatile("global_load_ushort %0, %3, %4\n\tglobal_load_ushort %1, %3, %5\n\tglobal_load_ushort %2, %6, %7"
                 : "=&v"(xv), "=&v"(bv2), "=&v"(bv1) : "v"(2 * xc), "s"(x_in + 80 * jj), "s"(b2 + 80 * jj), "v"(2 * hc), "s"(b1 + 48 * g) : "memory");
    SB();
    tile(IC<RT + 5>{}, IC<0>{});
    pace();
    BAR0();
    if (w == TW) ereq((const char*)(G + G_F) + (b & 128) * 8 + lane * 16);
    SB();
    tile(IC<RT + 6>{}, IC<0>{});
    asm volatile("s_waitcnt vmcnt(%3)" : "+v"(xv), "+v"(bv2), "+v"(bv1) : "n"(nreq(RT + 5, RT + 6)) : "memory");
    *(LAS f32x4*)(uintptr_t)(uint32_t)(L_TAB + 4 * NT * 16 + t16) = (f32x4){lo2f(xv), lo2f(bv2), lo2f(bv1), 0.f};
    pace();
    BAR0();
    {
      const int v0 = min(128 * w + 2 * lane, RP - 2);
      const char* src = (const char*)(G + G_R + g * RP + v0);
      if (w < 4) ereq(src);
      SB();
      tile(IC<RT + 7>{}, IC<0>{});
    if (w == TW && SC(PB)) {
      asm volatile("s_waitcnt vmcnt(%1)" : "+v"(er) : "n"(nreq(RT + 6, RT + 7)) : "memory");
      const bool v0 = er.y == tag, v1 = er.w == tag;
      const float d = (v0 ? (float)((int)(er.x - (tp << 2)) >> 2) : 0.f) + (v1 ? (float)((int)(er.z - (tp << 2)) >> 2) : 0.f);
      const int n = __popcll(__ballot(v0)) + __popcll(__ballot(v1));
      if (n > 16) {
        tp += __builtin_amdgcn_readfirstlane((int)(wsum_bcast(d) * __builtin_amdgcn_rcpf((float)n))) + SC(PA) + 4 * SC(PB);
        pstep = SC(PB);
      }
    }
    tp = __builtin_amdgcn_readfirstlane(tp); pstep = __builtin_amdgcn_readfirstlane(pstep);
      SB();
      pace();
      BAR0();
      tile(IC<RT + 8>{}, IC<0>{});
      if (w < 4) {
        efin(IC<nreq(RT + 7, RT + 8)>{}, src);
        if (v0 < 192) lwr4(sm + S_QB + qpos(v0), pk2(uf(er.x), uf(er.z)));
        else *(LAS u32x2*)(uintptr_t)(uint32_t)(L_CS + 4 * (v0 - 192)) = (u32x2){er.x, er.z};
      }
    }
    SB();
    STAMPV(3);
    pace();
    lds_barrier();
    const char* fsrc = (const char*)(G + G_F) + (w & 1) * 1024 + lane * 16;
    if (w >= 6) ereq(fsrc);
    SB();
    tile(IC<RT + 9>{}, IC<0>{});
    STAMPV(4);
    asm volatile("s_waitcnt vmcnt(%12)"
                 : GRP(2, 9)(kt[0]), GRP(10, 17)(kt[1]), GRP(18, 25)(kt[2]), GRP(26, 33)(kt[3]), GRP(34, 41)(kt[4]), GRP(42, 49)(kt[5]),
                   GRP(50, 57)(kt[6]), GRP(58, 65)(kt[7]), GRP(66, 73)(kt[8]), GRP(74, 81)(kt[9]), GRP(82, 89)(kt[10]), GRP(90, 97)(kt[11])
                 : "n"(nreq(9, RT + 9)) : "memory");
    const u32x4 aT = lrd16(L_TAB + 5 * NT * 16 + t16);
    uint32_t pb;
    f32x4 dv[3];
    {
      float mx, ev;
      pb = qk_softmax(mx, ev, dv, aT.x, 1.f);
      const float ls = wsum63(ev);
      if (lane == 63) *(LAS f32x2*)(uintptr_t)(uint32_t)aT.z = (f32x2){mx, ls};
    }
#define VFIX(v, e0, e1, e2, e3, g0, g1, g2) if constexpr (V == v) \
      asm volatile("s_waitcnt vmcnt(%3)\n\tv_swap_b32 v" #e0 ", v" #e2 "\n\tv_swap_b32 v" #e1 ", v" #e3 "\n\ts_nop 0" \
                   : g0(vt[a]), g1(vt[a + 1]), g2(vt[a + 2]) : "n"(decltype(YN)::value) : "memory")
    auto v_fix = [&](auto Vc, auto YN) {
      constexpr int V = decltype(Vc)::value, a = 3 * (V >> 1);
      SB();
      VFIX(0, 100, 101, 106, 107, GRP(98, 105), GRP(106, 113), GRP(114, 121));
      VFIX(1, 104, 105, 110, 111, GRP(98, 105), GRP(106, 113), GRP(114, 121));
      VFIX(2, 124, 125, 130, 131, GRP(122, 129), GRP(130, 137), GRP(138, 145));
      VFIX(3, 128, 129, 134, 135, GRP(122, 129), GRP(130, 137), GRP(138, 145));
      VFIX(4, 148, 149, 154, 155, GRP(146, 153), GRP(154, 161), GRP(162, 169));
      VFIX(5, 152, 153, 158, 159, GRP(146, 153), GRP(154, 161), GRP(162, 169));
      VFIX(6, 172, 173, 178, 179, GRP(170, 177), GRP(178, 185), GRP(186, 193));
      VFIX(7, 176, 177, 182, 183, GRP(170, 177), GRP(178, 185), GRP(186, 193));
      SB();
    };
    auto mm_step = [&](auto Ic, auto YN) {
      constexpr int i = decltype(Ic)::value;
      u32x4 pa = pset<i>(pb);
      PIN1(pa);
      v_fix(IC<2 * i + 1>{}, YN);
      dv[2] = smf<1>(pa, vt[3 * i + 2], dv[2], ix); SB();
      dv[0] = smf<1>(pa, vt[3 * i], dv[0], ix); dv[1] = smf<1>(pa, vt[3 * i + 1], dv[1], ix);
    };
    SB();
    if constexpr (RT + 10 <= 15) { pace(); BAR0(); tile(IC<RT + 10>{}, IC<0>{}); }
    constexpr int LT = RT + 10;
    v_fix(IC<0>{}, IC<nreq(9, LT)>{}); v_fix(IC<2>{}, IC<nreq(10, LT)>{});
    mm_step(IC<0>{}, IC<nreq(11, LT)>{}); mm_step(IC<1>{}, IC<nreq(12, LT)>{});
    v_fix(IC<4>{}, IC<nreq(13, LT)>{});
    SB();
    if (w >= 6) {
      efin(IC<nreq(RT + 9, LT)>{}, fsrc);
      const uint32_t hx = (uint32_t)__ballot(er.x & 2), hz = (uint32_t)__ballot(er.z & 2);
      lwr4(L_FLAG + 16 + 4 * (w & 1), (__popcll(__ballot(er.x & 1)) + __popcll(__ballot(er.z & 1))) << 8 | (hx & 1) | ((hz & 1) << 1) | ((hx & 2) << 1) | ((hz & 2) << 2));
    }
    if constexpr (RT + 11 <= 15) pace();
    lds_barrier();
    tile(IC<RT + 11>{}, IC<0>{});
    STAMPV(26);
    {
      const u32x2 fv = lrd8(L_FLAG + 16);
      const int f0 = __builtin_amdgcn_readfirstlane(fv.x), votes = (f0 >> 8) + (__builtin_amdgcn_readfirstlane(fv.y) >> 8);
      const int m4 = (f0 & 15) ^ ((SC(CPAR) < 2 ? SC(CPAR) : votes > NWG / 2) ? 15 : 0);
      pm = m4 | ((m4 ^ 15) << 4);
      const int own = rbase(jj), gb = own + (g & 3) * BLK, hb = rbase(jg >> 2) + (jg & 3) * BLK, cw = O_C + (w >> 2) * BLK + (w & 3) * 2048;
      oG = gb + (w < 3 ? 0 : OOR); oD = gb + (w == SW ? O_SG + j * 32 : j * 768);
      rC = own + cw - w * 1024 + (w < 5 ? 0 : OOR); eC = rbase(j) + cw + g * 64;
      rH = gb + (w < 2 ? 0 : OOR); eH = hb + (w == SW ? O_SH + jj * 32 : O_H + (w < 2 ? jj * 128 + w * 64 : 1024 + jj * 64));
      eQ = hb + 4 * BLK + (w == SW ? O_SG + jj * 32 : jj * 768 + w * 128);
      rK = (own & 8192) + O_DB - TW * 1024;
#define UNI(a) a = __builtin_amdgcn_readfirstlane(a)
      UNI(oG); UNI(oD); UNI(rC); UNI(eC); UNI(rH); UNI(eH); UNI(eQ); UNI(rK);
      PINS2(oG, oD); PINS2(rC, eC); PINS2(rH, eH); PINS2(eQ, rK);
    }
    v_fix(IC<6>{}, IC<nreq(14, 15)>{});
    mm_step(IC<2>{}, IC<nreq(15, 15)>{});
    SB();
    u32x4 cT = lrd16(L_TAB + t16);
    PIN1(cT);
    mm_step(IC<3>{}, IC<5>{});
    STAMPV(27);
    lwr4(aT.y + 512, __float_as_uint(dv[2][0])); lwr4(aT.y, __float_as_uint(dv[0][0])); lwr4(aT.y + 256, __float_as_uint(dv[1][0]));
    lds_barrier();
    pv_combine(cT, oD, w == SW);
    s0 = cT.w;
  }

  {
    const u32x4 rs4 = {(uint32_t)(uintptr_t)tail, (uint32_t)((uintptr_t)tail >> 32) & 0xffffu, 128u * BLK, 0x00020000u};
    const u32x4 ro4 = {(uint32_t)(uintptr_t)out, (uint32_t)((uintptr_t)out >> 32) & 0xffffu, 2u * DM, 0x00020000u};
    asm volatile(TAIL_ASM
                 : [s0] "+v"(s0), [tc] "+s"(tcur), [te] "+s"(tE)
                 : [t16] "v"(t16), [bo] "v"(bo), [ss] "v"(sS), [dt] "v"(dT), [ix] "v"(ix), [rs] "s"(rs4), [ro] "s"(ro4), [w] "s"(w0), [far] "s"(min(b0 >> 3, 1)),
                   [oG] "s"(oG), [oD] "s"(oD), [rC] "s"(rC), [eC] "s"(eC), [rH] "s"(rH), [eH] "s"(eH), [eQ] "s"(eQ), [rK] "s"(rK), [cw] "s"(O_DB + b0 * 8), [oo] "s"(160 * b0),
                   [p3e] "s"(SC(P3E)), [p2] "s"(SC(P2)), [p3] "s"(SC(P3)), [p4] "s"(SC(P4)), [p5] "s"(SC(P5)), [p1] "s"(SC(P1)), [rmin] "s"(SC(RMIN)), [rcons] "s"(SC(RCONS)),
                   [eps] "s"(1e-5f), [small] "s"(SMALL), [spin] "n"(SPIN_LIMIT), [tab] "n"(L_TAB), [xp] "n"(S_XP), [ov] "n"(S_OV), [hv] "n"(S_HV),
                   [osg] "n"(O_SG), [osh] "n"(O_SH), [oh] "n"(O_H), [oor] "n"(OOR), [qsw] "n"(L_FLAG + 4),
                   "{v[2:9]}"(kt[0]), "{v[10:17]}"(kt[1]), "{v[18:25]}"(kt[2]), "{v[26:33]}"(kt[3]), "{v[34:41]}"(kt[4]), "{v[42:49]}"(kt[5]),
                   "{v[50:57]}"(kt[6]), "{v[58:65]}"(kt[7]), "{v[66:73]}"(kt[8]), "{v[74:81]}"(kt[9]), "{v[82:89]}"(kt[10]), "{v[90:97]}"(kt[11]),
                   "{v[98:105]}"(vt[0]), "{v[106:113]}"(vt[1]), "{v[114:121]}"(vt[2]), "{v[122:129]}"(vt[3]), "{v[130:137]}"(vt[4]),
                   "{v[138:145]}"(vt[5]), "{v[146:153]}"(vt[6]), "{v[154:161]}"(vt[7]), "{v[162:169]}"(vt[8]), "{v[170:177]}"(vt[9]),
                   "{v[178:185]}"(vt[10]), "{v[186:193]}"(vt[11])
                 : "memory", "scc", "vcc",
                   "v194", "v195", "v196", "v197", "v198", "v199", "v200", "v201", "v202", "v203", "v204", "v205", "v206", "v207", "v208", "v209",
                   "v210", "v211", "v212", "v213", "v214", "v215", "v216", "v217", "v218", "v219", "v220", "v221", "v222", "v223", "v224", "v225",
                   "v226", "v227", "v228", "v229", "v230", "v231", "v232", "v233", "v234", "v235", "v236", "v237", "v238", "v239", "v240", "v241",
                   "v242", "v243", "v244", "v245", "v246", "v247", "s80", "s81", "s82", "s83", "s84", "s85", "s86", "s87", "s88", "s89", "s90", "s91",
                   "s92", "s93", "s94", "s95", "s96", "s97", "s98", "s99"
                 );
  }
  if (b0 == 8 && t16 == SW * 1024) __hip_atomic_store((gu32*)epoch, tag, RLX_AGENT);
}

extern "C" void kernel_launch(void* const* d_in, const int* in_sizes, int n_in, void* d_out, int out_size,
                              void* d_ws, size_t ws_size, hipStream_t stream) {
  (void)in_sizes; (void)n_in; (void)out_size; (void)ws_size;
  (void)hipFuncSetAttribute((const void*)mega_kernel, hipFuncAttributeMaxDynamicSharedMemorySize, L_TOTAL);
  mega_kernel<<<NWG, NT, L_TOTAL, stream>>>(
      (const uint16_t*)d_in[0], (const uint16_t*)d_in[1], (const uint16_t*)d_in[2], (const uint16_t*)d_in[3],
      (const uint16_t*)d_in[4], (const uint16_t*)d_in[6], (const uint16_t*)d_in[7], (const uint16_t*)d_in[8],
      (const uint16_t*)d_in[9], (uint16_t*)d_out, (unsigned*)d_ws
  );
}
```

```cpp
#include <hip/hip_runtime.h>
#include <hip/hip_bf16.h>
#include <stdint.h>

#define GAS __attribute__((address_space(1)))
#define LAS __attribute__((address_space(3)))
typedef GAS unsigned long long gu64;
typedef GAS unsigned gu32;
typedef uint32_t u32x2 __attribute__((ext_vector_type(2)));
typedef uint32_t u32x4 __attribute__((ext_vector_type(4)));
typedef uint32_t u32x8 __attribute__((ext_vector_type(8)));
typedef float f32x2 __attribute__((ext_vector_type(2)));
typedef float f32x4 __attribute__((ext_vector_type(4)));
typedef short s16x4 __attribute__((ext_vector_type(4)));
typedef __attribute__((ext_vector_type(2))) __bf16 bf16x2;
typedef __attribute__((ext_vector_type(8))) __bf16 bf16x8;
typedef __attribute__((ext_vector_type(16))) __bf16 bf16x16;
#define RLX_AGENT __ATOMIC_RELAXED, __HIP_MEMORY_SCOPE_AGENT

constexpr int DM = 640, NH = 32, DH = 192, SK = 32768, WIN = 4096, FF = 1536, QD = NH * DH;
constexpr int NWG = 256, NT = 512, NW = NT / 64;
#define SPINB 20
constexpr unsigned SPIN_LIMIT = 1u << SPINB;
constexpr int TW = 7, SW = 6;
constexpr int CODEPF = 15;
#define TWE 0
constexpr int UP = 3;
constexpr int nreq(int i0, int i1) { int n = 0; for (int i = i0; i <= i1 && i < 16; ++i) n += i == 15 ? 9 : (i == 0 || i > 8) ? 4 : 3; return n; }
#define XOFF 0
constexpr int kord(int n) { return 2 * (n & 3) + (n >> 2); }
constexpr int vord(int n) { return (n & 4) + 2 * (n & 1) + ((n >> 1) & 1); }
constexpr int RT = UP + 1;
constexpr int BKD = 3, BKO = 1;
constexpr float LOG2E = 1.4426950408889634f;
constexpr float SMALL = 40.f;
constexpr uint32_t SENT = 0xffffffffu;
constexpr int OOR = 1 << 22;

constexpr int RP = 27 * 16;
constexpr int G_P = 0, G_R = G_P + NWG * RP, G_F = G_R + NH * RP;
constexpr int BLK = 16384, NB = 14, O_SG = 6144, O_SH = 6400, O_H = 6656, O_C = 32 * BLK, O_S = 48 * BLK, O_DB = O_S + 4 * NB * BLK, O_PR = O_DB + BLK;
constexpr int NVOTE = 32;

constexpr int QS = 400;
constexpr int RS = 800;
constexpr int S_XP = 0, S_OV = 256, S_HV = 768, S_QB = 896, S_ZQ = 1280, S_RED = 1472, S_ML = S_RED + NW * RS, S_B = S_ML + 128, S_END = S_B + 512;
constexpr int L_SM = 0, L_CS = L_SM + S_END, L_ONE = L_CS + 960, L_FLAG = L_ONE + 192, L_SCH = L_FLAG + 64, L_TAB = L_SCH + 64,
              L_WQ = L_TAB + 6 * NT * 16, L_WO = L_WQ + 32768, L_W2 = L_WO + 30720, L_W1 = L_W2 + 8192, L_TOTAL = L_W1 + 8192;
static_assert(L_TOTAL <= 160 * 1024 && L_TOTAL > 80 * 1024 && S_END % 16 == 0, "LDS");

enum { P3E, P2, P3, P4, P5, P1, RMIN, RCONS, CPAR, PO, PT, PA, PB, PC, PD, NSCH };
#define SCHED_INIT {108, 131, 58, 63, 58, 68, 5, 50, 2, 410, 88, 620, 88, 390, 90}
constexpr int sched[NSCH] = SCHED_INIT;
#define SCHED_ARG
#define SC0(i) sched[i]
#define SC(i) sched[i]

__device__ __forceinline__ float lo2f(uint32_t p) { return __uint_as_float(p << 16); }
__device__ __forceinline__ float hi2f(uint32_t p) { return __uint_as_float(p & 0xffff0000u); }
__device__ __forceinline__ uint32_t pk2(float a, float b) {
  f32x2 v = {a, b};
  return __builtin_bit_cast(uint32_t, __builtin_convertvector(v, bf16x2));
}
__device__ __forceinline__ float rbf(float f) { return __uint_as_float(pk2(0.f, f)); }
__device__ __forceinline__ uint32_t pair_lo(uint32_t a, uint32_t b) { return __builtin_amdgcn_perm(b, a, 0x05040100u); }
__device__ __forceinline__ uint32_t pair_hi(uint32_t a, uint32_t b) { return __builtin_amdgcn_perm(b, a, 0x07060302u); }
template <int CTRL> __device__ __forceinline__ float dppv(float v) {
  return __int_as_float(__builtin_amdgcn_update_dpp(0, __float_as_int(v), CTRL, 0xf, 0xf, false));
}
template <int CTRL, int RM> __device__ __forceinline__ float dpp_add(float v) {
  return v + __int_as_float(__builtin_amdgcn_update_dpp(0, __float_as_int(v), CTRL, RM, 0xf, CTRL < 0x140));
}
__device__ __forceinline__ float wsum63(float v) {
  v = dpp_add<0x111, 0xf>(v); v = dpp_add<0x112, 0xf>(v); v = dpp_add<0x114, 0xf>(v); v = dpp_add<0x118, 0xf>(v);
  v = dpp_add<0x142, 0xa>(v); v = dpp_add<0x143, 0xc>(v);
  return v;
}
__device__ __forceinline__ float wsum_bcast(float v) {
  return __int_as_float(__builtin_amdgcn_readlane(__float_as_int(wsum63(v)), 63));
}
__device__ __forceinline__ float swapadd32(float a, float b) {
  auto r = __builtin_amdgcn_permlane32_swap(__float_as_uint(a), __float_as_uint(b), false, false);
  return __uint_as_float(r[0]) + __uint_as_float(r[1]);
}
__device__ __forceinline__ float swapadd16(float a, float b) {
  auto r = __builtin_amdgcn_permlane16_swap(__float_as_uint(a), __float_as_uint(b), false, false);
  return __uint_as_float(r[0]) + __uint_as_float(r[1]);
}
__device__ __forceinline__ float ror8add(float v) { return v + dppv<0x128>(v); }
__device__ __forceinline__ float ror8add2(float a, float b) {
  float r;
  asm("s_nop 1\n\tv_add_f32_dpp %0, %1, %1 row_ror:8 row_mask:0xf bank_mask:0x3\n\tv_add_f32_dpp %0, %2, %2 row_ror:8 row_mask:0xf bank_mask:0xc" : "=&v"(r) : "v"(a), "v"(b));
  return r;
}
__device__ __forceinline__ float red16(float a, float b, float c, float d) { return swapadd16(swapadd32(a, b), swapadd32(c, d)); }
__device__ __forceinline__ float red_cols(const u32x4& a, const u32x4& b) {
  float x, y, z, u;
  asm("v_add_f32 %0, %4, %8\n\tv_add_f32 %1, %5, %9\n\tv_add_f32 %2, %6, %10\n\tv_add_f32 %3, %7, %11\n\t"
      "v_permlane32_swap_b32 %0, %1\n\ts_nop 0\n\tv_permlane32_swap_b32 %2, %3\n\t"
      "v_add_f32 %0, %0, %1\n\tv_add_f32 %2, %2, %3\n\ts_nop 1\n\t"
      "v_permlane16_swap_b32 %0, %2\n\tv_add_f32 %0, %0, %2\n\ts_nop 1\n\t"
      "v_add_f32_dpp %0, %0, %0 row_ror:8 row_mask:0xf bank_mask:0xf\n\ts_nop 1\n\t"
      "v_add_f32_dpp %0, %0, %0 row_ror:4 row_mask:0xf bank_mask:0xf"
      : "=&v"(x), "=&v"(y), "=&v"(z), "=&v"(u) : "v"(a.x), "v"(a.y), "v"(a.z), "v"(a.w), "v"(b.x), "v"(b.y), "v"(b.z), "v"(b.w));
  return x;
}
__device__ __forceinline__ float sum_lo3(float v) { v += dppv<0xb1>(v); v += dppv<0x4e>(v); return v + dppv<0x141>(v); }
__device__ __forceinline__ float max_lo3a(float v) {
  float r;
  asm("s_nop 1\n\tv_max_f32_dpp %0, %1, %1 quad_perm:[1,0,3,2] row_mask:0xf bank_mask:0xf\n\ts_nop 1\n\t"
      "v_max_f32_dpp %0, %0, %0 quad_perm:[2,3,0,1] row_mask:0xf bank_mask:0xf\n\ts_nop 1\n\t"
      "v_max_f32_dpp %0, %0, %0 row_half_mirror row_mask:0xf bank_mask:0xf" : "=&v"(r) : "v"(v));
  return r;
}
__device__ __forceinline__ f32x4 scale_sum_lo3(float f, const u32x4& v) {
  float a, b, c, d;
#define DPP4(ctl) "v_add_f32_dpp %0, %0, %0 " ctl " row_mask:0xf bank_mask:0xf\n\tv_add_f32_dpp %1, %1, %1 " ctl " row_mask:0xf bank_mask:0xf\n\t" \
                  "v_add_f32_dpp %2, %2, %2 " ctl " row_mask:0xf bank_mask:0xf\n\tv_add_f32_dpp %3, %3, %3 " ctl " row_mask:0xf bank_mask:0xf\n\t"
  asm("s_nop 0\n\tv_mul_f32 %0, %4, %5\n\tv_mul_f32 %1, %4, %6\n\tv_mul_f32 %2, %4, %7\n\tv_mul_f32 %3, %4, %8\n\t"
      DPP4("quad_perm:[1,0,3,2]") DPP4("quad_perm:[2,3,0,1]") DPP4("row_half_mirror")
      : "=&v"(a), "=&v"(b), "=&v"(c), "=&v"(d) : "v"(f), "v"(v.x), "v"(v.y), "v"(v.z), "v"(v.w));
  return (f32x4){a, b, c, d};
}
__device__ __forceinline__ float sum_b320(const u32x4& v) {
  float a, b, c;
  asm("v_add_f32_dpp %0, %3, %3 row_ror:8 row_mask:0xf bank_mask:0x3\n\tv_add_f32_dpp %1, %4, %4 row_ror:8 row_mask:0xf bank_mask:0x3\n\t"
      "v_add_f32_dpp %0, %5, %5 row_ror:8 row_mask:0xf bank_mask:0xc\n\tv_add_f32_dpp %1, %6, %6 row_ror:8 row_mask:0xf bank_mask:0xc\n\ts_nop 0\n\t"
      "v_add_f32_dpp %2, %0, %0 row_ror:12 row_mask:0xf bank_mask:0x5\n\tv_add_f32_dpp %2, %1, %1 row_ror:4 row_mask:0xf bank_mask:0xa\n\ts_nop 1\n\t"
      "v_add_f32_dpp %2, %2, %2 quad_perm:[1,0,3,2] row_mask:0xf bank_mask:0xf"
      : "=&v"(a), "=&v"(b), "=&v"(c) : "v"(v.x), "v"(v.y), "v"(v.z), "v"(v.w));
  return c;
}
template <int N> __device__ __forceinline__ uint32_t rbc(uint32_t v) { return __builtin_amdgcn_mov_dpp(v, 0x150 + N, 0xf, 0xf, false); }
template <int I> __device__ __forceinline__ u32x4 pset(uint32_t p) { return (u32x4){rbc<4 * I>(p), rbc<4 * I + 1>(p), rbc<4 * I + 2>(p), rbc<4 * I + 3>(p)}; }
#define RQ "+{v[194:197]}"(r), "+{v[198:201]}"(q)
__device__ __forceinline__ float red_o(u32x4 r, u32x4 q, uint32_t l) {
  float v, t;
  asm("v_permlane32_swap_b32 v194, v195\n\tv_permlane32_swap_b32 v196, v197\n\tv_add_f32_dpp %1, %4, %4 quad_perm:[1,0,3,2] row_mask:0xf bank_mask:0xf\n\t"
      "v_permlane32_swap_b32 v198, v199\n\tv_permlane32_swap_b32 v200, v201\n\t"
      "v_add_f32 v194, v194, v195\n\tv_add_f32 v196, v196, v197\n\tv_add_f32_dpp %1, %1, %1 quad_perm:[2,3,0,1] row_mask:0xf bank_mask:0xf\n\t"
      "v_add_f32 v198, v198, v199\n\tv_add_f32 v200, v200, v201\n\tv_permlane16_swap_b32 v194, v196\n\t"
      "v_add_f32_dpp %1, %1, %1 row_half_mirror row_mask:0xf bank_mask:0xf\n\tv_permlane16_swap_b32 v198, v200\n\t"
      "v_add_f32 v194, v194, v196\n\tv_add_f32 v198, v198, v200\n\tv_rcp_f32 %1, %1\n\t"
      "v_add_f32_dpp %0, v194, v194 row_ror:8 row_mask:0xf bank_mask:0x3\n\tv_add_f32_dpp %0, v198, v198 row_ror:8 row_mask:0xf bank_mask:0xc\n\t"
      "v_mul_f32 %0, %0, %1"
      : "=&v"(v), "=&v"(t), RQ : "v"(l));
  return v;
}
__device__ __forceinline__ float red_q(u32x4 r, u32x4 q, uint32_t sx, uint32_t sy, float cs, float& s2, float& mean) {
  float v;
  asm("v_permlane32_swap_b32 v194, v195\n\tv_permlane32_swap_b32 v196, v197\n\t"
      "v_add_f32_dpp %2, %6, %6 quad_perm:[1,0,3,2] row_mask:0xf bank_mask:0xf\n\tv_add_f32_dpp %1, %5, %5 quad_perm:[1,0,3,2] row_mask:0xf bank_mask:0xf\n\t"
      "v_permlane32_swap_b32 v198, v199\n\tv_permlane32_swap_b32 v200, v201\n\tv_add_f32 v194, v194, v195\n\tv_add_f32 v196, v196, v197\n\t"
      "v_add_f32_dpp %2, %2, %2 quad_perm:[2,3,0,1] row_mask:0xf bank_mask:0xf\n\tv_add_f32_dpp %1, %1, %1 quad_perm:[2,3,0,1] row_mask:0xf bank_mask:0xf\n\t"
      "v_add_f32 v198, v198, v199\n\tv_add_f32 v200, v200, v201\n\tv_permlane16_swap_b32 v194, v196\n\t"
      "v_add_f32_dpp %2, %2, %2 row_half_mirror row_mask:0xf bank_mask:0xf\n\tv_permlane16_swap_b32 v198, v200\n\t"
      "v_add_f32 v194, v194, v196\n\tv_add_f32 v198, v198, v200\n\tv_mul_f32 %2, 0x3acccccd, %2\n\t"
      "v_add_f32_dpp %0, v194, v194 row_ror:8 row_mask:0xf bank_mask:0x3\n\tv_add_f32_dpp %0, v198, v198 row_ror:8 row_mask:0xf bank_mask:0xc\n\t"
      "v_cvt_pk_bf16_f32 v195, 0, %2\n\tv_fma_f32 %0, -v195, %7, %0\n\t"
      "v_add_f32_dpp %1, %1, %1 row_half_mirror row_mask:0xf bank_mask:0xf"
      : "=&v"(v), "=&v"(s2), "=&v"(mean), RQ : "v"(sx), "v"(sy), "v"(cs));
  return v;
}
#define ZERO4(d, a, b, c, e) asm volatile("v_mov_b64 v[" #a ":" #b "], 0\n\tv_mov_b64 v[" #c ":" #e "], 0" : "={v[" #a ":" #e "]}"(d))
__device__ __forceinline__ float ln_rinv(float s2, float mean) {
  const float var = rbf(fmaf(-mean, mean, s2 * (1.0f / DM)));
  return __builtin_amdgcn_rcpf(rbf(__builtin_amdgcn_sqrtf(rbf(fabsf(var) + 1e-5f))));
}
__device__ __forceinline__ int opos(int k) { return ((k >> 1) & 1) * 192 + (k >> 6) * 64 + (k & 32) + (k & 8) * 2 + (k & 4) * 2 + ((k >> 4) & 1) * 4 + (k & 1) * 2; }
__device__ __forceinline__ void sum2_lo3(float x, float y, float& a, float& b) {
#define DPP2(ctl) "s_nop 0\n\tv_add_f32_dpp %0, %0, %0 " ctl " row_mask:0xf bank_mask:0xf\n\tv_add_f32_dpp %1, %1, %1 " ctl " row_mask:0xf bank_mask:0xf\n\t"
  asm("s_nop 1\n\tv_add_f32_dpp %0, %2, %2 quad_perm:[1,0,3,2] row_mask:0xf bank_mask:0xf\n\tv_add_f32_dpp %1, %3, %3 quad_perm:[1,0,3,2] row_mask:0xf bank_mask:0xf\n\t"
      DPP2("quad_perm:[2,3,0,1]") DPP2("row_half_mirror") : "=&v"(a), "=&v"(b) : "v"(x), "v"(y));
}
__device__ __forceinline__ void lds_barrier() { asm volatile("s_waitcnt lgkmcnt(0)\n\ts_barrier" ::: "memory"); }
__device__ __forceinline__ uint32_t tagbad(const u32x4& r, uint32_t tag) { return (r.y ^ tag) | (r.w ^ tag); }
__device__ __forceinline__ float uf(uint32_t u) { return __uint_as_float(u); }
__device__ __forceinline__ uint32_t rt32() {
  unsigned long long t;
  asm volatile("s_memrealtime %0\n\ts_waitcnt lgkmcnt(0)" : "=s"(t)::"memory");
  return (uint32_t)t;
}
__device__ __forceinline__ f32x4 mfma32(const u32x4& a, const u32x4& b, const f32x4& c) {
  return __builtin_amdgcn_mfma_f32_16x16x32_bf16(__builtin_bit_cast(bf16x8, a), __builtin_bit_cast(bf16x8, b), c, 0, 0, 0);
}
template <int H> __device__ __forceinline__ f32x4 smf(const u32x4& a, const u32x8& b, const f32x4& c, uint32_t ix) {
  return __builtin_amdgcn_smfmac_f32_16x16x64_bf16(__builtin_bit_cast(bf16x8, a), __builtin_bit_cast(bf16x16, b), c, ix, 0, H);
}
__device__ __forceinline__ u32x8 cat8(const u32x4& a, const u32x4& b) { return __builtin_shufflevector(a, b, 0, 1, 2, 3, 4, 5, 6, 7); }
__device__ __forceinline__ int qpos(int v) { return ((v >> 1) & 1) * 192 + (v >> 2) * 4 + (v & 1) * 2; }
__device__ __forceinline__ u32x4 cat(const u32x2& a, const u32x2& b) { return (u32x4){a.x, a.y, b.x, b.y}; }
__device__ __forceinline__ uint32_t umax4(const u32x4& r) { return max(max(r.x, r.y), max(r.z, r.w)); }
__device__ __forceinline__ int kpos(int k) { return (k & ~31) | ((k & 12) << 1) | ((k & 16) >> 2) | (k & 3); }

#define DUMP(i) do {} while (0)
#define STAMPW(i) do { const uint32_t t_ = rt32(); if ((int)__lane_id() == (i)) stv = t_; } while (0)
#define STAMPV(i) do {} while (0)
#define CSTAMP(on, i) do {} while (0)
#define ESTAMP(i) do {} while (0)
#define PIN1(a) asm volatile("" : "+v"(a))
#define PINS(a) asm volatile("" : "+s"(a))
#define PINS2(a, c) asm volatile("" : "+s"(a), "+s"(c))
#define SB() do { asm volatile("" ::: "memory"); __builtin_amdgcn_sched_barrier(0); } while (0)
#define VM0() __builtin_amdgcn_s_waitcnt(0x0f70)
#define BAR0() asm volatile("s_barrier" ::: "memory")
#define BARX(t) asm volatile("s_barrier ; " t ::: "memory")
#define LBAR(t) asm volatile("s_waitcnt lgkmcnt(0)\n\ts_barrier ; " t ::: "memory")
template <int V> using IC = std::integral_constant<int, V>;

#define TAIL_ASM \
  "s_mov_b32 s80, 0\n\ts_mov_b32 s84, -1\n\ts_getpc_b64 s[98:99]\n\t.LPC%=:\ns_add_u32 s98, s98, .LCLK%=-.LPC%=\n\ts_addc_u32 s99, s99, 0\n\t.LLOOP%=:\n" \
  "s_cmp_gt_u32 %[w], 4\n\ts_cbranch_scc1 .LH2X%=\n\tds_read_b64 v[240:241], %[t16] offset:%[tab]+8192\n\ts_mov_b32 s81, %[oG]\n\t" \
  "s_add_u32 s82, s81, %[osg]\n\ts_mov_b32 s83, %[eC]\n\tv_and_b32 v242, 16, %[t16]\n\tv_mov_b64 v[232:233], 0\n\tv_mov_b64 v[234:235], 0\n\t" \
  "v_mad_u32_u24 v242, v242, 12, %[bo]\n\ts_cmp_eq_u32 s80, 0\n\ts_cbranch_scc1 .LH2A%=\n\ts_waitcnt lgkmcnt(0)\n\t" \
  "ds_read_b64_tr_b16 v[204:205], v241\n\tds_read_b64_tr_b16 v[206:207], v241 offset:2560\n\tds_read_b64_tr_b16 v[208:209], v241 offset:5120\n\t" \
  "ds_read_b64_tr_b16 v[210:211], v241 offset:7680\n\tds_read_b64_tr_b16 v[212:213], v241 offset:10240\n\t" \
  "ds_read_b64_tr_b16 v[214:215], v241 offset:12800\n\tds_read_b64_tr_b16 v[216:217], v241 offset:15360\n\t" \
  "ds_read_b64_tr_b16 v[218:219], v241 offset:17920\n\tds_read_b64_tr_b16 v[220:221], v241 offset:20480\n\t" \
  "ds_read_b64_tr_b16 v[222:223], v241 offset:23040\n\tds_read_b64_tr_b16 v[224:225], v241 offset:25600\n\t" \
  "ds_read_b64_tr_b16 v[226:227], v241 offset:28160\n\t.LH2A%=:\ns_cmp_gt_u32 %[w], 2\n\ts_cbranch_scc1 .LH2M%=\n\ts_barrier\n\t" \
  "buffer_load_dwordx2 v[202:203], %[ss], %[rs], s82 offen sc1\n\tbuffer_load_dwordx4 v[194:197], %[s0], %[rs], s81 offen sc1\n\t" \
  "buffer_load_dwordx4 v[198:201], %[s0], %[rs], s81 offen offset:128 sc1\n\ts_waitcnt vmcnt(2) lgkmcnt(0)\n\t.LRED2%=:\n" \
  "v_cmp_ne_u32 s[90:91], 0, v202\n\ts_waitcnt vmcnt(0)\n\tv_permlane32_swap_b32 v194, v195\n\tv_permlane32_swap_b32 v196, v197\n\t" \
  "v_add_f32_dpp v247, v203, v203 quad_perm:[1,0,3,2] row_mask:0xf bank_mask:0xf\n\tv_permlane32_swap_b32 v198, v199\n\t" \
  "v_permlane32_swap_b32 v200, v201\n\tv_add_f32 v194, v194, v195\n\tv_add_f32 v196, v196, v197\n\t" \
  "v_add_f32_dpp v247, v247, v247 quad_perm:[2,3,0,1] row_mask:0xf bank_mask:0xf\n\tv_add_f32 v198, v198, v199\n\tv_add_f32 v200, v200, v201\n\t" \
  "v_permlane16_swap_b32 v194, v196\n\tv_add_f32_dpp v247, v247, v247 row_half_mirror row_mask:0xf bank_mask:0xf\n\tv_permlane16_swap_b32 v198, v200\n\t" \
  "v_add_f32 v194, v194, v196\n\tv_add_f32 v198, v198, v200\n\tv_rcp_f32 v247, v247\n\t" \
  "v_add_f32_dpp v246, v194, v194 row_ror:8 row_mask:0xf bank_mask:0x3\n\tv_add_f32_dpp v246, v198, v198 row_ror:8 row_mask:0xf bank_mask:0xc\n\t" \
  "v_mul_f32 v246, v246, v247\n\tv_cvt_pk_bf16_f32 v247, v246, v246\n\tds_write_b16 v240, v247\n\tv_cmp_u_f32 s[92:93], v246, v246\n\t" \
  "s_or_b64 s[90:91], s[90:91], s[92:93]\n\ts_cbranch_scc1 .LSL2%=\n\t.LH2B%=:\ns_cmp_eq_u32 s80, 0\n\ts_cbranch_scc1 .LWO0%=\n\ts_waitcnt lgkmcnt(0)\n\t" \
  "s_barrier\n\tds_read_b128 v[194:197], v242 offset:%[ov]\n\tds_read_b128 v[198:201], v242 offset:%[ov]+64\n\t" \
  "ds_read_b128 v[228:231], v242 offset:%[ov]+128\n\ts_waitcnt lgkmcnt(2)\n\tv_smfmac_f32_16x16x64_bf16 v[232:235], v[194:197], v[204:211], %[ix]\n\t" \
  "s_waitcnt lgkmcnt(1)\n\tv_smfmac_f32_16x16x64_bf16 v[232:235], v[198:201], v[212:219], %[ix]\n\ts_waitcnt lgkmcnt(0)\n\t" \
  "v_smfmac_f32_16x16x64_bf16 v[232:235], v[228:231], v[220:227], %[ix]\n\t.LWOE%=:\ns_nop 7\n\tv_add_f32 v232, v232, v233\n\t" \
  "buffer_store_dword v232, %[dt], %[rs], s83 offen sc1\n\tds_read_b64 v[240:241], %[t16] offset:%[tab]+8192+8\n\t" \
  "ds_read_b32 %[s0], %[t16] offset:%[tab]+16384+12\n\tds_read_b32 v242, %[t16] offset:%[tab]+32768\n\ts_mov_b32 s81, %[rC]\n\ts_mov_b32 s83, %[eH]\n\t" \
  "v_mov_b64 v[214:215], 0\n\ts_waitcnt lgkmcnt(2)\n\tds_read_b64_tr_b16 v[204:205], v241\n\tds_read_b64_tr_b16 v[206:207], v241 offset:1536\n\t" \
  "ds_read_b64_tr_b16 v[208:209], v241 offset:3072\n\tds_read_b64_tr_b16 v[210:211], v241 offset:4608\n\t" \
  "ds_read_b64_tr_b16 v[212:213], v241 offset:6144\n\ts_waitcnt lgkmcnt(0)\n\ts_barrier\n\t" \
  "buffer_load_dwordx4 v[194:197], %[t16], %[rs], s81 offen sc1\n\tbuffer_load_dwordx4 v[198:201], %[t16], %[rs], s81 offen offset:1024 sc1\n\t" \
  "s_waitcnt vmcnt(0)\n\t.LRED3%=:\nv_add_f32 v194, v194, v198\n\tv_add_f32 v195, v195, v199\n\tv_add_f32 v196, v196, v200\n\t" \
  "v_add_f32 v197, v197, v201\n\tv_permlane32_swap_b32 v194, v195\n\ts_nop 0\n\tv_permlane32_swap_b32 v196, v197\n\tv_add_f32 v194, v194, v195\n\t" \
  "v_add_f32 v196, v196, v197\n\ts_nop 1\n\tv_permlane16_swap_b32 v194, v196\n\tv_add_f32 v194, v194, v196\n\ts_nop 1\n\t" \
  "v_add_f32_dpp v194, v194, v194 row_ror:8 row_mask:0xf bank_mask:0xf\n\ts_nop 1\n\t" \
  "v_add_f32_dpp v246, v194, v194 row_ror:4 row_mask:0xf bank_mask:0xf\n\tv_cvt_pk_bf16_f32 v247, 0, v246\n\tv_add_f32 v247, v242, v247\n\t" \
  "v_cvt_pk_bf16_f32 v243, v247, 0\n\tds_write_b16 v240, v243\n\tv_cmp_u_f32 vcc, v246, v246\n\ts_cbranch_vccnz .LSL3%=\n\t.LH3B%=:\n" \
  "s_cmp_gt_u32 %[w], 2\n\ts_cbranch_scc1 .LH3M%=\n\ts_waitcnt lgkmcnt(0)\n\ts_barrier\n\tds_read_b128 v[194:197], %[bo] offset:%[xp]\n\t" \
  "ds_read_b128 v[198:201], %[bo] offset:%[xp]+64\n\tds_read_b128 v[228:231], %[bo] offset:%[xp]+128\n\ts_waitcnt lgkmcnt(2)\n\t" \
  "v_mfma_f32_16x16x32_bf16 v[232:235], v[194:197], v[204:207], 0\n\ts_waitcnt lgkmcnt(1)\n\t" \
  "v_mfma_f32_16x16x32_bf16 v[232:235], v[198:201], v[208:211], v[232:235]\n\ts_waitcnt lgkmcnt(0)\n\t" \
  "v_mfma_f32_16x16x32_bf16 v[232:235], v[228:231], v[212:215], v[232:235]\n\ts_nop 7\n\tbuffer_store_dword v232, %[dt], %[rs], s83 offen sc1\n\t" \
  ".LH3E%=:\nv_lshlrev_b32 v243, 16, v243\n\tds_write_b32 %[t16], v243 offset:%[tab]+32768\n\tds_read_b128 v[240:243], %[t16] offset:%[tab]+16384\n\t" \
  "ds_read_b32 v244, %[t16] offset:%[tab]+32768+8\n\ts_add_u32 s82, %[oG], %[osh]\n\ts_add_u32 s81, %[oG], %[oh]\n\ts_sub_u32 s85, %[dE], 0x28000\n\t" \
  "s_cmp_eq_u32 s80, 0\n\ts_cselect_b32 s85, s85, 0x8000\n\ts_add_u32 s83, %[eC], s85\n\tv_mov_b64 v[210:211], 0\n\tv_mov_b32 v245, 0xc0135761\n\t" \
  "s_waitcnt lgkmcnt(1)\n\tds_read_b64_tr_b16 v[204:205], v241\n\tds_read_b64_tr_b16 v[206:207], v241 offset:2560\n\t" \
  "ds_read_b64_tr_b16 v[208:209], v241 offset:5120\n\tds_read_b32 v242, v242\n\ts_cmp_gt_u32 %[w], 1\n\ts_cbranch_scc1 .LH4M%=\n\t" \
  "s_waitcnt lgkmcnt(0)\n\ts_barrier\n\tbuffer_load_dwordx2 v[202:203], %[ss], %[rs], s82 offen sc1\n\t" \
  "buffer_load_dwordx4 v[194:197], %[s0], %[rs], s81 offen sc1\n\ts_waitcnt vmcnt(1)\n\t.LRED4%=:\n" \
  "v_add_f32_dpp v198, v202, v202 quad_perm:[1,0,3,2] row_mask:0xf bank_mask:0xf\n\t" \
  "v_add_f32_dpp v199, v203, v203 quad_perm:[1,0,3,2] row_mask:0xf bank_mask:0xf\n\ts_waitcnt vmcnt(0)\n\tv_permlane32_swap_b32 v194, v195\n\t" \
  "v_add_f32_dpp v198, v198, v198 quad_perm:[2,3,0,1] row_mask:0xf bank_mask:0xf\n\t" \
  "v_add_f32_dpp v199, v199, v199 quad_perm:[2,3,0,1] row_mask:0xf bank_mask:0xf\n\tv_permlane32_swap_b32 v196, v197\n\tv_add_f32 v194, v194, v195\n\t" \
  "v_add_f32_dpp v198, v198, v198 row_half_mirror row_mask:0xf bank_mask:0xf\n\t" \
  "v_add_f32_dpp v199, v199, v199 row_half_mirror row_mask:0xf bank_mask:0xf\n\tv_add_f32 v196, v196, v197\n\tv_mul_f32 v199, 0x3acccccd, v199\n\t" \
  "v_mul_f32 v198, 0x3acccccd, v198\n\tv_permlane16_swap_b32 v194, v196\n\tv_fma_f32 v198, -v199, v199, v198\n\tv_add_f32 v194, v194, v196\n\t" \
  "v_cvt_pk_bf16_f32 v198, 0, v198\n\tv_cvt_pk_bf16_f32 v199, 0, v199\n\tv_add_f32 v198, |v198|, %[eps]\n\t" \
  "v_add_f32_dpp v194, v194, v194 row_ror:8 row_mask:0xf bank_mask:0xf\n\tv_cvt_pk_bf16_f32 v198, 0, v198\n\tv_sqrt_f32 v198, v198\n\t" \
  "v_fma_f32 v194, -v199, v242, v194\n\tv_cvt_pk_bf16_f32 v198, 0, v198\n\tv_rcp_f32 v198, v198\n\ts_nop 0\n\tv_mul_f32 v246, v194, v198\n\t" \
  "v_cvt_pk_bf16_f32 v247, 0, v246\n\tv_add_f32 v247, v244, v247\n\tv_cvt_pk_bf16_f32 v247, 0, v247\n\tv_mul_f32 v198, v247, v247\n\t" \
  "v_fmamk_f32 v198, v198, 0xbdd2d3e7, v245\n\tv_mul_f32 v198, v198, v247\n\tv_exp_f32 v198, v198\n\tv_cmp_u_f32 vcc, v246, v246\n\t" \
  "v_add_f32 v198, 1.0, v198\n\tv_rcp_f32 v198, v198\n\ts_nop 0\n\tv_mul_f32 v198, v198, v247\n\tv_cvt_pk_bf16_f32 v198, v198, v198\n\t" \
  "ds_write_b16 v240, v198\n\ts_cbranch_vccnz .LSL4%=\n\t.LH4B%=:\ns_waitcnt lgkmcnt(0)\n\ts_barrier\n\tds_read_b128 v[194:197], %[bo] offset:%[hv]\n\t" \
  "ds_read_b128 v[198:201], %[bo] offset:%[hv]+64\n\ts_waitcnt lgkmcnt(1)\n\tv_mfma_f32_16x16x32_bf16 v[232:235], v[194:197], v[204:207], 0\n\t" \
  "s_waitcnt lgkmcnt(0)\n\tv_mfma_f32_16x16x32_bf16 v[232:235], v[198:201], v[208:211], v[232:235]\n\ts_nop 7\n\t" \
  "buffer_store_dword v232, %[dt], %[rs], s83 offen sc1\n\t.LH4E%=:\ns_and_b32 s88, s80, %[far]\n\ts_cbranch_scc1 .LEND%=\n\t" \
  "ds_read_b64 v[240:241], %[t16] offset:%[tab]+24576+8\n\tds_read_b32 v242, %[t16] offset:%[tab]+8192+8\n\t" \
  "ds_read_b64 v[244:245], %[t16] offset:%[tab]+32768\n\ts_sub_u32 s85, %[dR], 0x28000\n\ts_cmp_eq_u32 s80, 0\n\ts_cselect_b32 s85, s85, 0x8000\n\t" \
  "s_add_u32 s81, %[rC], s85\n\ts_mov_b32 s83, %[eQ]\n\tv_mov_b64 v[214:215], 0\n\tv_mov_b64 v[226:227], 0\n\ts_waitcnt lgkmcnt(2)\n\t" \
  "ds_read_b64_tr_b16 v[204:205], v240\n\tds_read_b64_tr_b16 v[216:217], v240 offset:32\n\tds_read_b64_tr_b16 v[206:207], v240 offset:6400\n\t" \
  "ds_read_b64_tr_b16 v[218:219], v240 offset:6432\n\tds_read_b64_tr_b16 v[208:209], v240 offset:12800\n\t" \
  "ds_read_b64_tr_b16 v[220:221], v240 offset:12832\n\tds_read_b64_tr_b16 v[210:211], v240 offset:19200\n\t" \
  "ds_read_b64_tr_b16 v[222:223], v240 offset:19232\n\tds_read_b64_tr_b16 v[212:213], v240 offset:25600\n\t" \
  "ds_read_b64_tr_b16 v[224:225], v240 offset:25632\n\ts_cmp_gt_u32 %[w], 4\n\ts_cbranch_scc1 .LH5M%=\n\ts_waitcnt lgkmcnt(0)\n\ts_barrier\n\t" \
  "buffer_load_dwordx4 v[194:197], %[t16], %[rs], s81 offen sc1\n\tbuffer_load_dwordx4 v[198:201], %[t16], %[rs], s81 offen offset:1024 sc1\n\t" \
  "s_waitcnt vmcnt(0)\n\t.LRED5%=:\nv_add_f32 v194, v194, v198\n\tv_add_f32 v195, v195, v199\n\tv_add_f32 v196, v196, v200\n\t" \
  "v_add_f32 v197, v197, v201\n\tv_permlane32_swap_b32 v194, v195\n\ts_nop 0\n\tv_permlane32_swap_b32 v196, v197\n\tv_add_f32 v194, v194, v195\n\t" \
  "v_add_f32 v196, v196, v197\n\ts_nop 1\n\tv_permlane16_swap_b32 v194, v196\n\tv_add_f32 v194, v194, v196\n\ts_nop 1\n\t" \
  "v_add_f32_dpp v194, v194, v194 row_ror:8 row_mask:0xf bank_mask:0xf\n\ts_nop 1\n\t" \
  "v_add_f32_dpp v246, v194, v194 row_ror:4 row_mask:0xf bank_mask:0xf\n\tv_cvt_pk_bf16_f32 v247, 0, v246\n\tv_cmp_u_f32 vcc, v246, v246\n\t" \
  "v_add_f32 v247, v245, v247\n\tv_cvt_pk_bf16_f32 v247, 0, v247\n\tv_add_f32 v247, v244, v247\n\tv_cvt_pk_bf16_f32 v243, v247, 0\n\t" \
  "ds_write_b16 v242, v243\n\ts_cbranch_vccnz .LSL5%=\n\t.LH5B%=:\ns_cmp_lg_u32 s80, 0\n\ts_cbranch_scc1 .LFIN%=\n\t.LH5C%=:\ns_waitcnt lgkmcnt(0)\n\t" \
  "s_barrier\n\tds_read_b128 v[194:197], %[bo] offset:%[xp]\n\tds_read_b128 v[198:201], %[bo] offset:%[xp]+64\n\t" \
  "ds_read_b128 v[228:231], %[bo] offset:%[xp]+128\n\ts_waitcnt lgkmcnt(2)\n\tv_mfma_f32_16x16x32_bf16 v[232:235], v[194:197], v[204:207], 0\n\t" \
  "v_mfma_f32_16x16x32_bf16 v[236:239], v[194:197], v[216:219], 0\n\ts_waitcnt lgkmcnt(1)\n\t" \
  "v_mfma_f32_16x16x32_bf16 v[232:235], v[198:201], v[208:211], v[232:235]\n\tv_mfma_f32_16x16x32_bf16 v[236:239], v[198:201], v[220:223], v[236:239]\n\t" \
  "s_waitcnt lgkmcnt(0)\n\tv_mfma_f32_16x16x32_bf16 v[232:235], v[228:231], v[212:215], v[232:235]\n\t" \
  "v_mfma_f32_16x16x32_bf16 v[236:239], v[228:231], v[224:227], v[236:239]\n\ts_nop 6\n\tbuffer_store_dword v232, %[dt], %[rs], s83 offen sc1\n\t" \
  "buffer_store_dword v236, %[dt], %[rs], s83 offen offset:64 sc1\n\tv_lshlrev_b32 v243, 16, v243\n\tds_write_b32 %[t16], v243 offset:%[tab]+32768\n\t" \
  ".LH1%=:\nds_read_b64 v[204:205], %[t16] offset:%[tab]+24576\n\tds_read_b128 v[240:243], %[t16] offset:%[tab]\n\t" \
  "ds_read_b128 v[236:239], %[t16] offset:%[tab]+40960\n\ts_add_u32 %[oG], %[oG], %[dG]\n\ts_add_u32 %[eH], %[eH], %[dH]\n\t" \
  "s_add_u32 %[rC], %[rC], %[dR]\n\ts_add_u32 %[eC], %[eC], %[dE]\n\ts_sub_u32 s81, %[oG], 0x10000\n\ts_add_u32 s82, s81, %[osg]\n\t" \
  "s_mov_b32 s92, 0x00ff00ff\n\ts_mov_b32 s93, 0xff00ff00\n\tv_mov_b64 v[208:209], 0\n\tv_mov_b64 v[210:211], 0\n\tv_mov_b64 v[212:213], 0\n\t" \
  "v_mov_b64 v[214:215], 0\n\tv_mov_b64 v[216:217], 0\n\tv_mov_b64 v[218:219], 0\n\tv_mov_b64 v[220:221], 0\n\tv_mov_b64 v[222:223], 0\n\t" \
  "v_mov_b64 v[224:225], 0\n\tv_mov_b64 v[226:227], 0\n\tv_mov_b64 v[228:229], 0\n\tv_mov_b64 v[230:231], 0\n\tv_mov_b64 v[232:233], 0\n\t" \
  "v_mov_b64 v[234:235], 0\n\ts_waitcnt lgkmcnt(2)\n\tds_read_b32 v205, v205\n\ts_cmp_gt_u32 %[w], 2\n\ts_cbranch_scc1 .LH1X%=\n\t" \
  "s_waitcnt lgkmcnt(0)\n\ts_barrier\n\tbuffer_load_dwordx2 v[202:203], %[ss], %[rs], s82 offen sc1\n\t" \
  "buffer_load_dwordx4 v[194:197], v243, %[rs], s81 offen sc1\n\tbuffer_load_dwordx4 v[198:201], v243, %[rs], s81 offen offset:128 sc1\n\t" \
  "s_waitcnt vmcnt(0)\n\t.LRED1%=:\nv_permlane32_swap_b32 v194, v195\n\tv_permlane32_swap_b32 v196, v197\n\t" \
  "v_add_f32_dpp v206, v203, v203 quad_perm:[1,0,3,2] row_mask:0xf bank_mask:0xf\n\tv_permlane32_swap_b32 v198, v199\n\t" \
  "v_permlane32_swap_b32 v200, v201\n\tv_add_f32 v194, v194, v195\n\tv_add_f32 v196, v196, v197\n\t" \
  "v_add_f32_dpp v206, v206, v206 quad_perm:[2,3,0,1] row_mask:0xf bank_mask:0xf\n\tv_add_f32 v198, v198, v199\n\tv_add_f32 v200, v200, v201\n\t" \
  "v_permlane16_swap_b32 v194, v196\n\tv_add_f32_dpp v206, v206, v206 row_half_mirror row_mask:0xf bank_mask:0xf\n\tv_permlane16_swap_b32 v198, v200\n\t" \
  "v_add_f32 v194, v194, v196\n\tv_add_f32 v198, v198, v200\n\tv_mul_f32 v206, 0x3acccccd, v206\n\t" \
  "v_add_f32_dpp v246, v194, v194 row_ror:8 row_mask:0xf bank_mask:0x3\n\tv_add_f32_dpp v246, v198, v198 row_ror:8 row_mask:0xf bank_mask:0xc\n\t" \
  "v_cvt_pk_bf16_f32 v247, 0, v206\n\tv_fma_f32 v246, -v247, v205, v246\n\tv_cvt_pk_bf16_f32 v247, v246, v246\n\tds_write_b16 v204, v247\n\t" \
  "v_cmp_u_f32 vcc, v246, v246\n\ts_cbranch_vccnz .LSL1%=\n\t.LH1B%=:\ns_waitcnt lgkmcnt(0)\n\ts_barrier\n\tds_read_b128 v[194:197], v236\n\t" \
  "ds_read_b128 v[198:201], v236 offset:64\n\tds_read_b128 v[204:207], v236 offset:128\n\tds_read_b32 v202, v239 offset:%[qsw]\n\t" \
  "s_waitcnt lgkmcnt(3)\n\tv_smfmac_f32_16x16x64_bf16 v[208:211], v[194:197], v[2:9], %[ix]\n\t" \
  "v_smfmac_f32_16x16x64_bf16 v[212:215], v[194:197], v[26:33], %[ix]\n\tv_smfmac_f32_16x16x64_bf16 v[216:219], v[194:197], v[50:57], %[ix]\n\t" \
  "v_smfmac_f32_16x16x64_bf16 v[220:223], v[194:197], v[74:81], %[ix]\n\ts_waitcnt lgkmcnt(2)\n\t" \
  "v_smfmac_f32_16x16x64_bf16 v[208:211], v[198:201], v[10:17], %[ix]\n\tv_smfmac_f32_16x16x64_bf16 v[212:215], v[198:201], v[34:41], %[ix]\n\t" \
  "v_smfmac_f32_16x16x64_bf16 v[216:219], v[198:201], v[58:65], %[ix]\n\tv_smfmac_f32_16x16x64_bf16 v[220:223], v[198:201], v[82:89], %[ix]\n\t" \
  "s_waitcnt lgkmcnt(1)\n\tv_smfmac_f32_16x16x64_bf16 v[208:211], v[204:207], v[18:25], %[ix]\n\t" \
  "v_smfmac_f32_16x16x64_bf16 v[212:215], v[204:207], v[42:49], %[ix]\n\tv_smfmac_f32_16x16x64_bf16 v[216:219], v[204:207], v[66:73], %[ix]\n\t" \
  "v_smfmac_f32_16x16x64_bf16 v[220:223], v[204:207], v[90:97], %[ix]\n\ts_setprio 1\n\ts_nop 3\n\tv_add_f32 v246, v208, v209\n\t" \
  "v_add_f32 v203, v212, v213\n\tv_cndmask_b32 v246, v203, v246, s[92:93]\n\tv_add_f32 v247, v216, v217\n\tv_add_f32 v203, v220, v221\n\t" \
  "v_cndmask_b32 v247, v203, v247, s[92:93]\n\ts_waitcnt lgkmcnt(0)\n\ts_nop 0\n\tv_permlane32_swap_b32 v246, v247\n\ts_nop 1\n\t" \
  "v_add_f32_dpp v246, v247, v246 row_ror:8 row_mask:0xf bank_mask:0xf\n\tv_mul_f32 v246, v246, v202\n\tv_cvt_pk_bf16_f32 v246, 0, v246\n\t" \
  "v_mul_f32 v246, 0x3d93cd3a, v246\n\tv_cvt_pk_bf16_f32 v246, 0, v246\n\tv_mul_f32 v246, 0x3fb8aa3b, v246\n\tv_exp_f32 v203, v246\n\t" \
  "v_cmp_nle_f32 vcc, |v246|, %[small]\n\tv_cvt_pk_bf16_f32 v244, v203, 0\n\tv_add_f32_dpp v203, v203, v203 row_ror:1 row_mask:0xf bank_mask:0xf\n\t" \
  "s_nop 0\n\tv_mov_b32_dpp v245, v244 row_shl:1 row_mask:0xf bank_mask:0xf\n\ts_cbranch_vccnz .LSLM%=\n\t.LPV%=:\ns_setprio 0\n\t" \
  "v_mov_b64_dpp v[208:209], v[244:245] row_newbcast:0 row_mask:0xf bank_mask:0xf\n\t" \
  "v_mov_b64_dpp v[210:211], v[244:245] row_newbcast:2 row_mask:0xf bank_mask:0xf\n\t" \
  "v_add_f32_dpp v203, v203, v203 row_ror:2 row_mask:0xf bank_mask:0xf\n\t" \
  "v_mov_b64_dpp v[212:213], v[244:245] row_newbcast:4 row_mask:0xf bank_mask:0xf\n\t" \
  "v_smfmac_f32_16x16x64_bf16 v[224:227], v[208:211], v[98:105], %[ix] abid:1\n\t" \
  "v_mov_b64_dpp v[214:215], v[244:245] row_newbcast:6 row_mask:0xf bank_mask:0xf\n\t" \
  "v_smfmac_f32_16x16x64_bf16 v[228:231], v[208:211], v[106:113], %[ix] abid:1\n\tv_add_f32_dpp v203, v203, v203 row_ror:4 row_mask:0xf bank_mask:0xf\n\t" \
  "v_smfmac_f32_16x16x64_bf16 v[232:235], v[208:211], v[114:121], %[ix] abid:1\n\t" \
  "v_smfmac_f32_16x16x64_bf16 v[224:227], v[212:215], v[122:129], %[ix] abid:1\n\t" \
  "v_mov_b64_dpp v[208:209], v[244:245] row_newbcast:8 row_mask:0xf bank_mask:0xf\n\t" \
  "v_mov_b64_dpp v[210:211], v[244:245] row_newbcast:10 row_mask:0xf bank_mask:0xf\n\t" \
  "v_smfmac_f32_16x16x64_bf16 v[228:231], v[212:215], v[130:137], %[ix] abid:1\n\tv_add_f32_dpp v203, v203, v203 row_ror:8 row_mask:0xf bank_mask:0xf\n\t" \
  "v_smfmac_f32_16x16x64_bf16 v[232:235], v[212:215], v[138:145], %[ix] abid:1\n\t" \
  "v_smfmac_f32_16x16x64_bf16 v[224:227], v[208:211], v[146:153], %[ix] abid:1\n\t" \
  "v_mov_b64_dpp v[212:213], v[244:245] row_newbcast:12 row_mask:0xf bank_mask:0xf\n\t" \
  "v_mov_b64_dpp v[214:215], v[244:245] row_newbcast:14 row_mask:0xf bank_mask:0xf\n\t" \
  "v_smfmac_f32_16x16x64_bf16 v[228:231], v[208:211], v[154:161], %[ix] abid:1\n\t" \
  "v_add_f32_dpp v203, v203, v203 row_bcast:15 row_mask:0xa bank_mask:0xf\n\t" \
  "v_smfmac_f32_16x16x64_bf16 v[232:235], v[208:211], v[162:169], %[ix] abid:1\n\t" \
  "v_smfmac_f32_16x16x64_bf16 v[224:227], v[212:215], v[170:177], %[ix] abid:1\n\t" \
  "v_smfmac_f32_16x16x64_bf16 v[228:231], v[212:215], v[178:185], %[ix] abid:1\n\t" \
  "v_smfmac_f32_16x16x64_bf16 v[232:235], v[212:215], v[186:193], %[ix] abid:1\n\t" \
  "v_add_f32_dpp v203, v203, v203 row_bcast:31 row_mask:0xc bank_mask:0xf\n\ts_lshl_b64 exec, 1, 63\n\tds_write2_b32 v238, v239, v203 offset1:1\n\t" \
  "s_mov_b64 exec, -1\n\ts_mov_b32 s80, 1\n\ts_mov_b32 s83, %[oD]\n\ts_nop 0\n\tds_write2st64_b32 v237, v224, v228 offset1:1\n\t" \
  "ds_write_b32 v237, v232 offset:512\n\ts_waitcnt lgkmcnt(0)\n\ts_barrier\n\tds_read_b128 v[194:197], v241\n\tds_read_b32 v204, v240\n\t" \
  "v_mov_b32 %[s0], v243\n\ts_waitcnt lgkmcnt(1)\n\t.LCMB%=:\nv_add_f32_dpp v205, v194, v194 row_ror:8 row_mask:0xf bank_mask:0x3\n\t" \
  "v_add_f32_dpp v206, v195, v195 row_ror:8 row_mask:0xf bank_mask:0x3\n\tv_add_f32_dpp v205, v196, v196 row_ror:8 row_mask:0xf bank_mask:0xc\n\t" \
  "v_add_f32_dpp v206, v197, v197 row_ror:8 row_mask:0xf bank_mask:0xc\n\ts_waitcnt lgkmcnt(0)\n\tv_cmp_ne_u32 vcc, 0, v204\n\t" \
  "v_add_f32_dpp v246, v205, v205 row_ror:12 row_mask:0xf bank_mask:0x5\n\tv_add_f32_dpp v246, v206, v206 row_ror:4 row_mask:0xf bank_mask:0xa\n\t" \
  "s_nop 1\n\tv_add_f32_dpp v246, v246, v246 quad_perm:[1,0,3,2] row_mask:0xf bank_mask:0xf\n\ts_cbranch_vccnz .LSLC%=\n\t.LCMS%=:\n" \
  "buffer_store_dword v246, v242, %[rs], s83 offen sc1\n\ts_branch .LLOOP%=\n\t.LWO0%=:\ns_waitcnt lgkmcnt(0)\n\ts_barrier\n\t" \
  "ds_read_b128 v[194:197], v242 offset:%[ov]+0\n\tds_read_b64_tr_b16 v[204:205], v241\n\tds_read_b64_tr_b16 v[206:207], v241 offset:2560\n\t" \
  "ds_read_b64_tr_b16 v[208:209], v241 offset:5120\n\tds_read_b64_tr_b16 v[210:211], v241 offset:7680\n\t" \
  "ds_read_b128 v[198:201], v242 offset:%[ov]+64\n\tds_read_b64_tr_b16 v[212:213], v241 offset:10240\n\t" \
  "ds_read_b64_tr_b16 v[214:215], v241 offset:12800\n\tds_read_b64_tr_b16 v[216:217], v241 offset:15360\n\t" \
  "ds_read_b64_tr_b16 v[218:219], v241 offset:17920\n\tds_read_b128 v[228:231], v242 offset:%[ov]+128\n\t" \
  "ds_read_b64_tr_b16 v[220:221], v241 offset:20480\n\tds_read_b64_tr_b16 v[222:223], v241 offset:23040\n\t" \
  "ds_read_b64_tr_b16 v[224:225], v241 offset:25600\n\tds_read_b64_tr_b16 v[226:227], v241 offset:28160\n\ts_waitcnt lgkmcnt(10)\n\t" \
  "v_smfmac_f32_16x16x64_bf16 v[232:235], v[194:197], v[204:211], %[ix]\n\ts_waitcnt lgkmcnt(5)\n\t" \
  "v_smfmac_f32_16x16x64_bf16 v[232:235], v[198:201], v[212:219], %[ix]\n\ts_waitcnt lgkmcnt(0)\n\t" \
  "v_smfmac_f32_16x16x64_bf16 v[232:235], v[228:231], v[220:227], %[ix]\n\ts_branch .LWOE%=\n\t.LH1X%=:\ns_cmp_eq_u32 %[w], 3\n\t" \
  "s_cbranch_scc1 .LH1S%=\n\ts_cmp_lg_u32 %[w], 7\n\ts_cbranch_scc1 .LH1Y%=\n\ts_mov_b32 s88, %[p1]\n\ts_swappc_b64 s[96:97], s[98:99]\n\t.LH1Y%=:\n" \
  "s_barrier\n\ts_branch .LH1B%=\n\t.LH1S%=:\ns_waitcnt lgkmcnt(0)\n\ts_barrier\n\tbuffer_load_dwordx2 v[202:203], %[ss], %[rs], s82 offen sc1\n\t" \
  "s_waitcnt vmcnt(0)\n\t.LREDS%=:\nv_add_f32_dpp v194, v202, v202 quad_perm:[1,0,3,2] row_mask:0xf bank_mask:0xf\n\t" \
  "v_add_f32_dpp v195, v203, v203 quad_perm:[1,0,3,2] row_mask:0xf bank_mask:0xf\n\ts_nop 0\n\t" \
  "v_add_f32_dpp v194, v194, v194 quad_perm:[2,3,0,1] row_mask:0xf bank_mask:0xf\n\t" \
  "v_add_f32_dpp v195, v195, v195 quad_perm:[2,3,0,1] row_mask:0xf bank_mask:0xf\n\ts_nop 0\n\t" \
  "v_add_f32_dpp v194, v194, v194 row_half_mirror row_mask:0xf bank_mask:0xf\n\t" \
  "v_add_f32_dpp v195, v195, v195 row_half_mirror row_mask:0xf bank_mask:0xf\n\tv_mul_f32 v194, 0x3acccccd, v194\n\tv_mul_f32 v195, 0x3acccccd, v195\n\t" \
  "v_fma_f32 v194, -v195, v195, v194\n\tv_cvt_pk_bf16_f32 v194, 0, v194\n\tv_add_f32 v194, |v194|, %[eps]\n\tv_cvt_pk_bf16_f32 v194, 0, v194\n\t" \
  "v_sqrt_f32 v194, v194\n\ts_nop 0\n\tv_cvt_pk_bf16_f32 v194, 0, v194\n\tv_rcp_f32 v194, v194\n\ts_nop 0\n\tds_write_b32 v239, v194 offset:%[qsw]\n\t" \
  "v_cmp_u_f32 vcc, v194, v194\n\ts_cbranch_vccz .LH1B%=\n\ts_branch .LSLS%=\n\t.LH2M%=:\ns_barrier\n\ts_waitcnt vmcnt(0)\n\ts_branch .LH2B%=\n\t" \
  ".LH3M%=:\ns_waitcnt lgkmcnt(0)\n\ts_barrier\n\ts_branch .LH3E%=\n\t.LH4M%=:\ns_barrier\n\ts_branch .LH4B%=\n\t.LH5M%=:\ns_barrier\n\t" \
  "s_cmp_eq_u32 s80, 0\n\ts_cbranch_scc1 .LH5C%=\n\ts_branch .LFE%=\n\t.LH2X%=:\ns_cmp_eq_u32 %[w], 7\n\ts_cbranch_scc1 .LTW%=\n\t" \
  "ds_read_b64 v[240:241], %[t16] offset:%[tab]+16384\n\tv_mov_b64 v[198:199], -1\n\tv_mov_b64 v[200:201], -1\n\ts_cmp_lg_u32 s80, 0\n\t" \
  "s_waitcnt lgkmcnt(0)\n\ts_barrier\n\ts_cbranch_scc1 .LH2Y%=\n\tbuffer_store_dwordx4 v[198:201], v240, %[rs], 0 offen sc1\n\t.LH2Y%=:\n" \
  "s_waitcnt vmcnt(0)\n\ts_barrier\n\ts_mov_b32 s83, %[eH]\n\ts_cmp_eq_u32 %[w], 6\n\ts_cbranch_scc1 .LSW3%=\n\ts_movk_i32 s88, 0x2000\n\t" \
  "s_cmp_lg_u32 s80, 0\n\ts_barrier\n\ts_barrier\n\ts_cbranch_scc1 .LH3Y%=\n\tbuffer_store_dwordx4 v[198:201], v241, %[rs], 0 offen sc1\n\t" \
  "buffer_store_dwordx4 v[198:201], v241, %[rs], s88 offen sc1\n\t.LH3Y%=:\ns_barrier\n\ts_waitcnt vmcnt(0)\n\ts_barrier\n\ts_branch .LH4E%=\n\t" \
  ".LSW3%=:\ns_barrier\n\ts_barrier\n\tds_read_b128 v[194:197], %[bo] offset:%[xp]\n\tds_read_b128 v[204:207], %[ss]\n\t" \
  "ds_read_b128 v[198:201], %[bo] offset:%[xp]+64\n\tds_read_b128 v[208:211], %[ss] offset:64\n\tds_read_b128 v[228:231], %[bo] offset:%[xp]+128\n\t" \
  "ds_read_b128 v[212:215], %[ss] offset:128\n\ts_waitcnt lgkmcnt(4)\n\tv_mfma_f32_16x16x32_bf16 v[232:235], v[194:197], v[204:207], 0\n\t" \
  "s_waitcnt lgkmcnt(2)\n\tv_mfma_f32_16x16x32_bf16 v[232:235], v[198:201], v[208:211], v[232:235]\n\ts_waitcnt lgkmcnt(0)\n\t" \
  "v_mfma_f32_16x16x32_bf16 v[232:235], v[228:231], v[212:215], v[232:235]\n\ts_nop 7\n\tbuffer_store_dword v232, %[dt], %[rs], s83 offen sc1\n\t" \
  "s_barrier\n\ts_barrier\n\ts_and_b32 s88, s80, %[far]\n\ts_cbranch_scc1 .LEND%=\n\ts_mov_b32 s83, %[eQ]\n\ts_barrier\n\ts_cmp_lg_u32 s80, 0\n\t" \
  "s_cbranch_scc1 .LFE%=\n\ts_barrier\n\tds_read_b128 v[194:197], %[bo] offset:%[xp]\n\tds_read_b128 v[204:207], %[ss]\n\t" \
  "ds_read_b128 v[198:201], %[bo] offset:%[xp]+64\n\tds_read_b128 v[208:211], %[ss] offset:64\n\tds_read_b128 v[228:231], %[bo] offset:%[xp]+128\n\t" \
  "ds_read_b128 v[212:215], %[ss] offset:128\n\ts_waitcnt lgkmcnt(4)\n\tv_mfma_f32_16x16x32_bf16 v[232:235], v[194:197], v[204:207], 0\n\t" \
  "s_waitcnt lgkmcnt(2)\n\tv_mfma_f32_16x16x32_bf16 v[232:235], v[198:201], v[208:211], v[232:235]\n\ts_waitcnt lgkmcnt(0)\n\t" \
  "v_mfma_f32_16x16x32_bf16 v[232:235], v[228:231], v[212:215], v[232:235]\n\ts_nop 7\n\tbuffer_store_dword v232, %[dt], %[rs], s83 offen sc1\n\t" \
  "s_branch .LH1%=\n\t.LTW%=:\ns_cmp_lg_u32 s80, 0\n\ts_cbranch_scc1 .LTW1%=\n\ts_memrealtime s[94:95]\n\tv_and_b32 v240, 0x3e0, %[t16]\n\t" \
  "v_lshlrev_b32 v241, 9, %[t16]\n\tv_and_b32 v241, 0x2000, v241\n\tv_cmp_eq_u32 vcc, 0, v240\n\tv_mov_b32 v240, %[oor]\n\t" \
  "v_cndmask_b32 v240, v240, v241, vcc\n\tv_mov_b32 v243, %[tag]\n\ts_waitcnt lgkmcnt(0)\n\ts_mov_b32 %[tc], s94\n\tv_mov_b32 v242, s94\n\t" \
  "buffer_store_dwordx2 v[242:243], v240, %[rs], %[cw] offen sc1\n\ts_barrier\n\ts_waitcnt vmcnt(1)\n\ts_barrier\n\ts_mov_b32 s87, %[spin]\n\t.LTWP%=:\n" \
  "buffer_load_dwordx4 v[194:197], %[t16], %[rs], %[rK] offen sc1\n\tbuffer_load_dwordx4 v[198:201], %[t16], %[rs], %[rK] offen offset:1024 sc1\n\t" \
  "s_waitcnt vmcnt(0)\n\tv_xor_b32 v247, v195, v243\n\tv_xor_b32 v246, v197, v243\n\tv_xor_b32 v199, v199, v243\n\tv_xor_b32 v201, v201, v243\n\t" \
  "v_or3_b32 v247, v247, v246, v199\n\tv_or_b32 v247, v247, v201\n\tv_cmp_ne_u32 vcc, 0, v247\n\ts_cbranch_vccz .LTWQ%=\n\ts_sleep 3\n\t" \
  "s_sub_u32 s87, s87, 1\n\ts_cbranch_scc0 .LTWP%=\n\t.LTWQ%=:\nv_subrev_u32 v194, %[tc], v194\n\tv_subrev_u32 v196, %[tc], v196\n\t" \
  "v_subrev_u32 v198, %[tc], v198\n\tv_subrev_u32 v200, %[tc], v200\n\tv_max_i32 v247, v194, v196\n\tv_max3_i32 v247, v198, v200, v247\n\ts_nop 1\n\t" \
  "v_max_i32_dpp v247, v247, v247 row_ror:1 row_mask:0xf bank_mask:0xf\n\ts_nop 1\n\t" \
  "v_max_i32_dpp v247, v247, v247 row_ror:2 row_mask:0xf bank_mask:0xf\n\ts_nop 1\n\t" \
  "v_max_i32_dpp v247, v247, v247 row_ror:4 row_mask:0xf bank_mask:0xf\n\ts_nop 1\n\t" \
  "v_max_i32_dpp v247, v247, v247 row_ror:8 row_mask:0xf bank_mask:0xf\n\ts_nop 1\n\t" \
  "v_max_i32_dpp v247, v247, v247 row_bcast:15 row_mask:0xa bank_mask:0xf\n\ts_nop 1\n\t" \
  "v_max_i32_dpp v247, v247, v247 row_bcast:31 row_mask:0xc bank_mask:0xf\n\ts_nop 1\n\tv_readlane_b32 s88, v247, 63\n\t" \
  "s_add_u32 %[tc], %[tc], %[p3e]\n\ts_add_u32 %[tc], %[tc], s88\n\ts_mov_b32 %[te], %[tc]\n\ts_add_u32 s94, s98, .LUNT%=-.LCLK%=\n\t" \
  "s_addc_u32 s95, s99, 0\n\ts_swappc_b64 s[96:97], s[94:95]\n\ts_branch .LTW3%=\n\t.LTW1%=:\ns_mov_b32 s88, %[p2]\n\ts_swappc_b64 s[96:97], s[98:99]\n\t" \
  "s_barrier\n\ts_barrier\n\ts_mov_b32 s88, %[p3]\n\ts_swappc_b64 s[96:97], s[98:99]\n\t.LTW3%=:\ns_barrier\n\ts_barrier\n\ts_mov_b32 s88, %[p4]\n\t" \
  "s_swappc_b64 s[96:97], s[98:99]\n\ts_barrier\n\ts_barrier\n\ts_and_b32 s88, s80, %[far]\n\ts_cbranch_scc1 .LEND%=\n\ts_mov_b32 s88, %[p5]\n\t" \
  "s_swappc_b64 s[96:97], s[98:99]\n\ts_barrier\n\ts_cmp_lg_u32 s80, 0\n\ts_cbranch_scc1 .LFE%=\n\ts_barrier\n\ts_branch .LH1%=\n\t.LCLK%=:\n" \
  "s_memrealtime s[94:95]\n\ts_add_u32 %[tc], %[tc], s88\n\ts_movk_i32 s87, 64\n\ts_waitcnt lgkmcnt(0)\n\t.LCL1%=:\ns_sub_i32 s88, %[tc], s94\n\t" \
  "s_cmp_ge_i32 s88, %[rmin]\n\ts_cbranch_scc1 .LUNT%=\n\ts_add_u32 %[tc], %[tc], %[rcons]\n\ts_sub_u32 s87, s87, 1\n\ts_cbranch_scc0 .LCL1%=\n\t" \
  ".LUNT%=:\ns_movk_i32 s87, 0x1000\n\t.LUN1%=:\ns_memrealtime s[94:95]\n\ts_sub_u32 s87, s87, 1\n\ts_cbranch_scc1 .LUN2%=\n\ts_waitcnt lgkmcnt(0)\n\t" \
  "s_sub_i32 s88, %[tc], s94\n\ts_cmp_gt_i32 s88, 3\n\ts_cbranch_scc1 .LUN1%=\n\ts_cmp_lt_i32 s88, 1\n\ts_cbranch_scc1 .LUN2%=\n\ts_nop 3\n\t" \
  "s_cmp_lt_i32 s88, 2\n\ts_cbranch_scc1 .LUN2%=\n\ts_nop 3\n\ts_cmp_lt_i32 s88, 3\n\ts_cbranch_scc1 .LUN2%=\n\ts_nop 9\n\t.LUN2%=:\n" \
  "s_waitcnt lgkmcnt(0)\n\ts_setpc_b64 s[96:97]\n\t.LSL2%=:\ns_add_u32 s88, s80, 2\n\ts_cmp_eq_u32 s84, s88\n\ts_cbranch_scc1 .LH2B%=\n\t" \
  "s_mov_b32 s84, s88\n\ts_mov_b32 s87, %[spin]\n\t.LSL2a%=:\nbuffer_load_dwordx2 v[202:203], %[ss], %[rs], s82 offen sc1\n\t" \
  "buffer_load_dwordx4 v[194:197], %[s0], %[rs], s81 offen sc1\n\tbuffer_load_dwordx4 v[198:201], %[s0], %[rs], s81 offen offset:128 sc1\n\t" \
  "s_waitcnt vmcnt(0)\n\tv_max3_u32 v247, v194, v195, v196\n\tv_max3_u32 v247, v247, v197, v198\n\tv_max3_u32 v247, v247, v199, v200\n\t" \
  "v_max3_u32 v247, v247, v201, v202\n\tv_max_u32 v247, v247, v203\n\tv_cmp_eq_u32 vcc, -1, v247\n\ts_cbranch_vccz .LSL2b%=\n\ts_sub_u32 s87, s87, 1\n\t" \
  "s_cbranch_scc0 .LSL2a%=\n\t.LSL2b%=:\nv_cmp_ne_u32 vcc, 0, v202\n\ts_cbranch_vccz .LRED2%=\n\tv_lshrrev_b32 v247, 5, %[t16]\n\t" \
  "v_and_b32 v247, 28, v247\n\tds_bpermute_b32 v247, v247, v202\n\tv_max_f32_dpp v246, v202, v202 quad_perm:[1,0,3,2] row_mask:0xf bank_mask:0xf\n\t" \
  "s_nop 1\n\tv_max_f32_dpp v246, v246, v246 quad_perm:[2,3,0,1] row_mask:0xf bank_mask:0xf\n\ts_nop 1\n\t" \
  "v_max_f32_dpp v246, v246, v246 row_half_mirror row_mask:0xf bank_mask:0xf\n\ts_waitcnt lgkmcnt(0)\n\tv_sub_f32 v247, v247, v246\n\t" \
  "v_sub_f32 v202, v202, v246\n\tv_exp_f32 v247, v247\n\tv_exp_f32 v202, v202\n\ts_nop 0\n\tv_mul_f32 v194, v247, v194\n\tv_mul_f32 v195, v247, v195\n\t" \
  "v_mul_f32 v196, v247, v196\n\tv_mul_f32 v197, v247, v197\n\tv_mul_f32 v198, v247, v198\n\tv_mul_f32 v199, v247, v199\n\tv_mul_f32 v200, v247, v200\n\t" \
  "v_mul_f32 v201, v247, v201\n\tv_mul_f32 v203, v202, v203\n\tv_mov_b32 v202, 0\n\ts_nop 1\n\ts_branch .LRED2%=\n\t.LSL3%=:\ns_add_u32 s88, s80, 4\n\t" \
  "s_cmp_eq_u32 s84, s88\n\ts_cbranch_scc1 .LH3B%=\n\ts_mov_b32 s84, s88\n\ts_mov_b32 s87, %[spin]\n\t.LSL3a%=:\n" \
  "buffer_load_dwordx4 v[194:197], %[t16], %[rs], s81 offen sc1\n\tbuffer_load_dwordx4 v[198:201], %[t16], %[rs], s81 offen offset:1024 sc1\n\t" \
  "s_waitcnt vmcnt(0)\n\tv_max3_u32 v247, v194, v195, v196\n\tv_max3_u32 v247, v247, v197, v198\n\tv_max3_u32 v247, v247, v199, v200\n\t" \
  "v_max_u32 v247, v247, v201\n\tv_cmp_eq_u32 vcc, -1, v247\n\ts_cbranch_vccz .LSL3b%=\n\ts_sleep 1\n\ts_sub_u32 s87, s87, 1\n\t" \
  "s_cbranch_scc0 .LSL3a%=\n\t.LSL3b%=:\ns_branch .LRED3%=\n\t.LSL4%=:\ns_add_u32 s88, s80, 6\n\ts_cmp_eq_u32 s84, s88\n\ts_cbranch_scc1 .LH4B%=\n\t" \
  "s_mov_b32 s84, s88\n\ts_mov_b32 s87, %[spin]\n\t.LSL4a%=:\nbuffer_load_dwordx2 v[202:203], %[ss], %[rs], s82 offen sc1\n\t" \
  "buffer_load_dwordx4 v[194:197], %[s0], %[rs], s81 offen sc1\n\ts_waitcnt vmcnt(0)\n\tv_max3_u32 v247, v194, v195, v196\n\t" \
  "v_max3_u32 v247, v247, v197, v202\n\tv_max_u32 v247, v247, v203\n\tv_cmp_eq_u32 vcc, -1, v247\n\ts_cbranch_vccz .LSL4b%=\n\ts_sleep 1\n\t" \
  "s_sub_u32 s87, s87, 1\n\ts_cbranch_scc0 .LSL4a%=\n\t.LSL4b%=:\ns_branch .LRED4%=\n\t.LSL5%=:\ns_add_u32 s88, s80, 8\n\ts_cmp_eq_u32 s84, s88\n\t" \
  "s_cbranch_scc1 .LH5B%=\n\ts_mov_b32 s84, s88\n\ts_mov_b32 s87, %[spin]\n\t.LSL5a%=:\nbuffer_load_dwordx4 v[194:197], %[t16], %[rs], s81 offen sc1\n\t" \
  "buffer_load_dwordx4 v[198:201], %[t16], %[rs], s81 offen offset:1024 sc1\n\ts_waitcnt vmcnt(0)\n\tv_max3_u32 v247, v194, v195, v196\n\t" \
  "v_max3_u32 v247, v247, v197, v198\n\tv_max3_u32 v247, v247, v199, v200\n\tv_max_u32 v247, v247, v201\n\tv_cmp_eq_u32 vcc, -1, v247\n\t" \
  "s_cbranch_vccz .LSL5b%=\n\ts_sleep 1\n\ts_sub_u32 s87, s87, 1\n\ts_cbranch_scc0 .LSL5a%=\n\t.LSL5b%=:\ns_branch .LRED5%=\n\t.LSL1%=:\n" \
  "s_add_u32 s88, s80, 10\n\ts_cmp_eq_u32 s84, s88\n\ts_cbranch_scc1 .LH1B%=\n\ts_mov_b32 s84, s88\n\ts_mov_b32 s87, %[spin]\n\t.LSL1a%=:\n" \
  "buffer_load_dwordx2 v[202:203], %[ss], %[rs], s82 offen sc1\n\tbuffer_load_dwordx4 v[194:197], v243, %[rs], s81 offen sc1\n\t" \
  "buffer_load_dwordx4 v[198:201], v243, %[rs], s81 offen offset:128 sc1\n\ts_waitcnt vmcnt(0)\n\tv_max3_u32 v247, v194, v195, v196\n\t" \
  "v_max3_u32 v247, v247, v197, v198\n\tv_max3_u32 v247, v247, v199, v200\n\tv_max3_u32 v247, v247, v201, v202\n\tv_max_u32 v247, v247, v203\n\t" \
  "v_cmp_eq_u32 vcc, -1, v247\n\ts_cbranch_vccz .LSL1b%=\n\ts_sleep 1\n\ts_sub_u32 s87, s87, 1\n\ts_cbranch_scc0 .LSL1a%=\n\t.LSL1b%=:\n" \
  "s_branch .LRED1%=\n\t.LSLS%=:\ns_add_u32 s88, s80, 10\n\ts_cmp_eq_u32 s84, s88\n\ts_cbranch_scc1 .LH1B%=\n\ts_mov_b32 s84, s88\n\t" \
  "s_mov_b32 s87, %[spin]\n\t.LSLSa%=:\nbuffer_load_dwordx2 v[202:203], %[ss], %[rs], s82 offen sc1\n\ts_waitcnt vmcnt(0)\n\t" \
  "v_max3_u32 v247, v202, v203, v203\n\tv_cmp_eq_u32 vcc, -1, v247\n\ts_cbranch_vccz .LSLSb%=\n\ts_sleep 1\n\ts_sub_u32 s87, s87, 1\n\t" \
  "s_cbranch_scc0 .LSLSa%=\n\t.LSLSb%=:\ns_branch .LREDS%=\n\t.LSLM%=:\nv_max_f32_dpp v247, v246, v246 row_ror:1 row_mask:0xf bank_mask:0xf\n\t" \
  "s_nop 1\n\tv_max_f32_dpp v247, v247, v247 row_ror:2 row_mask:0xf bank_mask:0xf\n\ts_nop 1\n\t" \
  "v_max_f32_dpp v247, v247, v247 row_ror:4 row_mask:0xf bank_mask:0xf\n\ts_nop 1\n\t" \
  "v_max_f32_dpp v247, v247, v247 row_ror:8 row_mask:0xf bank_mask:0xf\n\ts_nop 1\n\t" \
  "v_max_f32_dpp v247, v247, v247 row_bcast:15 row_mask:0xa bank_mask:0xf\n\ts_nop 1\n\t" \
  "v_max_f32_dpp v247, v247, v247 row_bcast:31 row_mask:0xc bank_mask:0xf\n\ts_nop 1\n\tv_readlane_b32 s88, v247, 63\n\ts_nop 3\n\t" \
  "v_mov_b32 v239, s88\n\tv_sub_f32 v246, v246, v239\n\tv_exp_f32 v203, v246\n\ts_nop 0\n\tv_cvt_pk_bf16_f32 v244, v203, 0\n\t" \
  "v_add_f32_dpp v203, v203, v203 row_ror:1 row_mask:0xf bank_mask:0xf\n\ts_nop 0\n\tv_mov_b32_dpp v245, v244 row_shl:1 row_mask:0xf bank_mask:0xf\n\t" \
  "s_branch .LPV%=\n\t.LSLC%=:\nv_max_f32_dpp v247, v204, v204 row_ror:8 row_mask:0xf bank_mask:0xf\n\ts_nop 1\n\t" \
  "v_max_f32_dpp v247, v247, v247 row_ror:4 row_mask:0xf bank_mask:0xf\n\ts_nop 1\n\t" \
  "v_max_f32_dpp v247, v247, v247 quad_perm:[1,0,3,2] row_mask:0xf bank_mask:0xf\n\tv_sub_f32 v204, v204, v247\n\tv_exp_f32 v204, v204\n\ts_nop 0\n\t" \
  "v_mul_f32 v194, v204, v194\n\tv_mul_f32 v195, v204, v195\n\tv_mul_f32 v196, v204, v196\n\tv_mul_f32 v197, v204, v197\n\ts_nop 0\n\t" \
  "v_add_f32_dpp v205, v194, v194 row_ror:8 row_mask:0xf bank_mask:0x3\n\tv_add_f32_dpp v206, v195, v195 row_ror:8 row_mask:0xf bank_mask:0x3\n\t" \
  "v_add_f32_dpp v205, v196, v196 row_ror:8 row_mask:0xf bank_mask:0xc\n\tv_add_f32_dpp v206, v197, v197 row_ror:8 row_mask:0xf bank_mask:0xc\n\t" \
  "s_nop 0\n\tv_add_f32_dpp v246, v205, v205 row_ror:12 row_mask:0xf bank_mask:0x5\n\t" \
  "v_add_f32_dpp v246, v206, v206 row_ror:4 row_mask:0xf bank_mask:0xa\n\ts_nop 1\n\t" \
  "v_add_f32_dpp v246, v246, v246 quad_perm:[1,0,3,2] row_mask:0xf bank_mask:0xf\n\ts_cmp_lg_u32 %[w], 6\n\ts_cbranch_scc1 .LCMS%=\n\t" \
  "v_cmp_eq_u32 vcc, 0, v242\n\tv_cndmask_b32 v246, v246, v247, vcc\n\ts_branch .LCMS%=\n\t.LFIN%=:\n" \
  "buffer_store_short v243, v241, %[ro], %[oo] offen\n\t.LFE%=:\n.LEND%=:\n"
static_assert(BLK == 16384 && NT == 512 && QS == 400 && L_SM == 0 && TW == 7 && SW == 6, "constants of the hand-written part");

__global__ void __launch_bounds__(NT, 2)
mega_kernel(const uint16_t* __restrict__ x_in, const uint16_t* __restrict__ kc, const uint16_t* __restrict__ vc,
            const uint16_t* __restrict__ wq, const uint16_t* __restrict__ wo, const uint16_t* __restrict__ w1,
            const uint16_t* __restrict__ b1, const uint16_t* __restrict__ w2, const uint16_t* __restrict__ b2,
            uint16_t* __restrict__ out, unsigned* epoch SCHED_ARG) {
  extern __shared__ __attribute__((aligned(16))) char lds[];
  uint32_t cp;
  const uint64_t code0 = __builtin_amdgcn_s_getpc() & ~(uint64_t)127;
  asm volatile("global_load_dword %0, %1, %2" : "=v"(cp) : "v"(min((int)((blockIdx.x >> 3) * 1024 + (threadIdx.x >> 6) * 128), CODEPF * 1024 - 128)),
               "s"(code0) : "memory");
  __builtin_amdgcn_sched_barrier(0);
  const int tid0 = threadIdx.x, w0 = __builtin_amdgcn_readfirstlane(tid0 >> 6);
  const int b0 = blockIdx.x;
  unsigned long long* gran_base = (unsigned long long*)epoch + 512;
  char* tail = (char*)(((uintptr_t)epoch + (4u << 20)) & ~(uintptr_t)((2u << 20) - 1));
  const auto rs = __builtin_amdgcn_make_buffer_rsrc(tail, 0, 128 * BLK, 0x00020000);
  auto ld16 = [&](int voff, int so) -> u32x4 { return __builtin_amdgcn_raw_buffer_load_b128(rs, voff, so, 16); };
  auto ld8 = [&](int voff, int so) -> u32x2 { return __builtin_amdgcn_raw_buffer_load_b64(rs, voff, so, 16); };
  auto ld4 = [&](int voff, int so) -> uint32_t { return __builtin_amdgcn_raw_buffer_load_b32(rs, voff, so, 16); };
  auto st16 = [&](int voff, int so, const f32x4& v) { __builtin_amdgcn_raw_buffer_store_b128(__builtin_bit_cast(u32x4, v), rs, voff, so, 16); };
  auto st4 = [&](int voff, int so, float a) { __builtin_amdgcn_raw_buffer_store_b32(__float_as_uint(a), rs, voff, so, 16); };
  auto trd = [&](int off) -> u32x2 {
    return __builtin_bit_cast(u32x2, __builtin_amdgcn_ds_read_tr16_b64_v4i16((LAS s16x4*)(uintptr_t)(uint32_t)off));
  };
  auto lrd16 = [&](int off) -> u32x4 { return *(const LAS u32x4*)(uintptr_t)(uint32_t)off; };
  auto lrd8 = [&](int off) -> u32x2 { return *(const LAS u32x2*)(uintptr_t)(uint32_t)off; };
  auto lrd4 = [&](int off) -> uint32_t { return *(const LAS uint32_t*)(uintptr_t)(uint32_t)off; };
  auto lwr2 = [&](int off, uint32_t v) { *(LAS uint16_t*)(uintptr_t)(uint32_t)off = (uint16_t)v; };
  auto lwr4 = [&](int off, uint32_t v) { *(LAS uint32_t*)(uintptr_t)(uint32_t)off = v; };
  constexpr int sm = L_SM;

  uint32_t tag;
  uint32_t tE = 0;
  uint32_t tcur = 0;
  u32x8 kt[12], vt[12];
  uint32_t ix;
  int t16, bo, sS, dT;

  auto qk_softmax = [&](float& mx, float& ev, f32x4 (&dv)[3], int qb, float qs, bool cs = false) -> uint32_t {
    u32x4 qa[3];
    f32x4 d[4];
#pragma unroll
    for (int u = 0; u < 3; ++u) qa[u] = lrd16(qb + 64 * u);
#pragma unroll
    for (int a = 0; a < 1; ++a) { ZERO4(d[0], 204, 205, 206, 207); ZERO4(d[1], 208, 209, 210, 211); ZERO4(d[2], 212, 213, 214, 215); ZERO4(d[3], 216, 217, 218, 219); }
    SB();
#pragma unroll
    for (int u = 0; u < 3; ++u)
#pragma unroll
      for (int a = 0; a < 4; ++a) {
        d[a] = smf<0>(qa[u], kt[3 * a + u], d[a], ix);
        SB();
        if (u == 1 && a == 0) ZERO4(dv[0], 220, 221, 222, 223);
        if (u == 1 && a == 2) ZERO4(dv[1], 224, 225, 226, 227);
        if (u == 2 && a == 0) ZERO4(dv[2], 228, 229, 230, 231);
        SB();
      }
    __builtin_amdgcn_s_setprio(1);
    const bool same = __builtin_amdgcn_inverse_ballot_w64(0xff00ff0000ff00ffull);
    const float f0 = same ? d[0][0] + d[0][1] : d[1][0] + d[1][1], f1 = same ? d[2][0] + d[2][1] : d[3][0] + d[3][1];
    CSTAMP(cs, 41);
    auto r = __builtin_amdgcn_permlane32_swap(__float_as_uint(f0), __float_as_uint(f1), false, false);
    const float sraw = uf(r[0]) + dppv<0x128>(uf(r[1]));
    const float sv = rbf(rbf(sraw) * 0.07216878364870322f) * LOG2E;
    uint64_t big = __builtin_amdgcn_ballot_w64(!(fabsf(sv) <= SMALL));
    PINS(big);
    SB();
    mx = 0.f;
    ev = __builtin_amdgcn_exp2f(sv);
    uint32_t pb = pk2(ev, 0.f);
    PIN1(pb);
    SB();
    if (__builtin_expect(big != 0, 0)) {
      float mxv;
      asm volatile("s_nop 1\n\t"
                   "v_max_f32_dpp %0, %1, %1 row_ror:1 row_mask:0xf bank_mask:0xf\n\ts_nop 1\n\t"
                   "v_max_f32_dpp %0, %0, %0 row_ror:2 row_mask:0xf bank_mask:0xf\n\ts_nop 1\n\t"
                   "v_max_f32_dpp %0, %0, %0 row_ror:4 row_mask:0xf bank_mask:0xf\n\ts_nop 1\n\t"
                   "v_max_f32_dpp %0, %0, %0 row_ror:8 row_mask:0xf bank_mask:0xf\n\ts_nop 1\n\t"
                   "v_max_f32_dpp %0, %0, %0 row_bcast:15 row_mask:0xa bank_mask:0xf\n\ts_nop 1\n\t"
                   "v_max_f32_dpp %0, %0, %0 row_bcast:31 row_mask:0xc bank_mask:0xf\n\ts_nop 0"
                   : "=&v"(mxv) : "v"(sv));
      mx = __int_as_float(__builtin_amdgcn_readlane(__float_as_int(mxv), 63));
      ev = __builtin_amdgcn_exp2f(sv - mx);
      pb = pk2(ev, 0.f);
    }
    __builtin_amdgcn_s_setprio(0);
    CSTAMP(cs, 42);
    return pb;
  };
  auto pv_combine = [&](const u32x4& cT, int od, bool sw, bool cs = false) {
    CSTAMP(cs, 45);
    const uint32_t m = lrd4(cT.x);
    u32x4 rv = lrd16(cT.y);
    SB();
    uint64_t shifted = __builtin_amdgcn_ballot_w64(m != 0);
    PINS(shifted);
    SB();
    float o = sum_b320(rv);
    PIN1(o);
    SB();
    if (__builtin_expect(shifted != 0, 0)) {
      float M = fmaxf(uf(m), dppv<0x128>(uf(m)));
      M = fmaxf(M, dppv<0x124>(M));
      M = fmaxf(M, dppv<0xb1>(M));
      const float f = __builtin_amdgcn_exp2f(uf(m) - M);
      rv = __builtin_bit_cast(u32x4, __builtin_bit_cast(f32x4, rv) * f);
      asm volatile("s_nop 1" : "+v"(rv));
      o = sum_b320(rv);
      if (sw && (t16 & 0xc0) == 0) o = M;
    }
    st4(cT.z, od, o);
    CSTAMP(cs, 46);
  };
  int s0;
  int oG, oD, rC, eC, eH, eQ, rK;
  int dG, dH, dR, dE;

  {
    const int w = w0, b = b0, lane = tid0 & 63;
    const int g = (b & 7) * 4 + (b >> 6), j = (b >> 3) & 7, jg = b >> 3, jj = b & 7;
    const int ck = lane & 7, jp = lane >> 3, n16 = lane & 15, kq = lane >> 4;
    const int sub = (lane >> 5) + 2 * ((lane >> 4) & 1), trl = 4 * kq + (n16 >> 2), trc = 8 * (lane & 3);
    const int xc = min(16 * w + 4 * (lane & 3) + sub, 79);
    const int gc = 64 * (w < 3 ? w : 0) + 4 * (lane & 8) + 4 * ck + sub, hc = (w & 1) ? 32 + 4 * (lane & 3) + sub : 4 * ck + sub;
    gu64* G = (gu64*)gran_base;
    STAMPV(0);
    const size_t row0 = (size_t)g * SK + (SK - WIN) + j * 64 + w * 8;
    const uint16_t *kp = kc + row0 * DH, *vp = vc + row0 * DH;
    const int rot = g, hx = (g >> 2) & 1;
    const int vo = ((kq >> 1) ^ hx) * (2048 * 384) + (kq & 1) * 384 + n16 * 16, vo2 = vo + 256 - n16 * 8;
    const int ko = ((lane & 4) + ((lane & 1) << 1) + ((lane >> 1) & 1)) * 384 + (lane & 8) * 8 + kq * 16, ko2 = ko ^ 64;
#define GRP(lo, hi) "+{v[" #lo ":" #hi "]}"
#define LOADK(t, r0, r1, r2, g0, g1, g2) if constexpr (T == t) \
      asm volatile("global_load_dwordx4 " r0 ", %3, %4 nt\n\tglobal_load_dwordx4 " r1 ", %3, %4 offset:128 nt\n\t" \
                   "global_load_dwordx4 " r2 ", %3, %4 offset:256 nt" \
                   : g0(kt[a]), g1(kt[a + 1]), g2(kt[a + 2]) : "v"(o), "s"(p) : "memory")
    auto load_k = [&](auto Tc) {
      constexpr int T = decltype(Tc)::value, a = 3 * (T >> 1);
      const int o = (T & 2) ? ko2 : ko;
      const uint16_t* p = kp + ((((T >> 1) + rot) & 3) * 512 + ((T & 1) ^ hx) * 2048) * DH;
      LOADK(0, "v[2:5]", "v[10:13]", "v[18:21]", GRP(2, 9), GRP(10, 17), GRP(18, 25));
      LOADK(1, "v[6:9]", "v[14:17]", "v[22:25]", GRP(2, 9), GRP(10, 17), GRP(18, 25));
      LOADK(2, "v[26:29]", "v[34:37]", "v[42:45]", GRP(26, 33), GRP(34, 41), GRP(42, 49));
      LOADK(3, "v[30:33]", "v[38:41]", "v[46:49]", GRP(26, 33), GRP(34, 41), GRP(42, 49));
      LOADK(4, "v[50:53]", "v[58:61]", "v[66:69]", GRP(50, 57), GRP(58, 65), GRP(66, 73));
      LOADK(5, "v[54:57]", "v[62:65]", "v[70:73]", GRP(50, 57), GRP(58, 65), GRP(66, 73));
      LOADK(6, "v[74:77]", "v[82:85]", "v[90:93]", GRP(74, 81), GRP(82, 89), GRP(90, 97));
      LOADK(7, "v[78:81]", "v[86:89]", "v[94:97]", GRP(74, 81), GRP(82, 89), GRP(90, 97));
      SB();
    };
#define LOADV(v, x0, y0, x1, y1, g0, g1, g2) if constexpr (V == v) \
      asm volatile("global_load_dwordx4 " x0 ", %3, %5 nt\n\tglobal_load_dwordx2 " y0 ", %4, %5 nt\n\t" \
                   "global_load_dwordx4 " x1 ", %3, %5 offset:768 nt\n\tglobal_load_dwordx2 " y1 ", %4, %5 offset:768 nt" \
                   : g0(vt[a]), g1(vt[a + 1]), g2(vt[a + 2]) : "v"(vo), "v"(vo2), "s"(p) : "memory")
    auto load_v = [&](auto Vc) {
      constexpr int V = decltype(Vc)::value, a = 3 * (V >> 1);
      const uint16_t* p = vp + (((2 * (V & 1) + (V >> 2) + rot) & 3) * 512 + ((V >> 1) & 1) * 4) * DH;
      LOADV(0, "v[98:101]", "v[114:115]", "v[106:109]", "v[116:117]", GRP(98, 105), GRP(106, 113), GRP(114, 121));
      LOADV(1, "v[102:105]", "v[118:119]", "v[110:113]", "v[120:121]", GRP(98, 105), GRP(106, 113), GRP(114, 121));
      LOADV(2, "v[122:125]", "v[138:139]", "v[130:133]", "v[140:141]", GRP(122, 129), GRP(130, 137), GRP(138, 145));
      LOADV(3, "v[126:129]", "v[142:143]", "v[134:137]", "v[144:145]", GRP(122, 129), GRP(130, 137), GRP(138, 145));
      LOADV(4, "v[146:149]", "v[162:163]", "v[154:157]", "v[164:165]", GRP(146, 153), GRP(154, 161), GRP(162, 169));
      LOADV(5, "v[150:153]", "v[166:167]", "v[158:161]", "v[168:169]", GRP(146, 153), GRP(154, 161), GRP(162, 169));
      LOADV(6, "v[170:173]", "v[186:187]", "v[178:181]", "v[188:189]", GRP(170, 177), GRP(178, 185), GRP(186, 193));
      LOADV(7, "v[174:177]", "v[190:191]", "v[182:185]", "v[192:193]", GRP(170, 177), GRP(178, 185), GRP(186, 193));
      SB();
    };
    int go[5];
    auto wo_prep = [&]() {
      int l = lane;
      PIN1(l);
#pragma unroll
      for (int i = 0; i < 5; ++i) { const int c = min(64 * i + l, w < 6 ? 319 : 239), row = (c * 205) >> 11; go[i] = row * (2 * DM) + (c - 10 * row) * 16; PIN1(go[i]); }
    };
    auto load_wo = [&]() {
      const uint16_t* src = (w < 6 ? wo + (size_t)(192 * g + 32 * w) * DM : w2 + (size_t)(48 * g + 24 * (w - 6)) * DM) + 80 * j;
      unsigned keep;
      asm volatile("s_mov_b32 %0, m0\n\ts_mov_b32 m0, %7\n\ts_nop 0\n\tglobal_load_lds_dwordx4 %1, %6 nt\n\t"
                   "s_add_u32 m0, m0, 0x400\n\ts_nop 0\n\tglobal_load_lds_dwordx4 %2, %6 nt\n\t"
                   "s_add_u32 m0, m0, 0x400\n\ts_nop 0\n\tglobal_load_lds_dwordx4 %3, %6 nt\n\t"
                   "s_add_u32 m0, m0, 0x400\n\ts_mov_b64 exec, %8\n\tglobal_load_lds_dwordx4 %4, %6 nt\n\t"
                   "s_add_u32 m0, m0, %10\n\ts_mov_b64 exec, %9\n\tglobal_load_lds_dwordx4 %5, %6 nt\n\t"
                   "s_mov_b64 exec, -1\n\ts_mov_b32 m0, %0"
                   : "=&s"(keep) : "v"(go[0]), "v"(go[1]), "v"(go[2]), "v"(go[3]), "v"(go[4]), "s"(src), "s"(w < 7 ? L_WO + w * 5120 : L_W2 + 3840),
                     "s"(w < 6 ? ~0ull : (1ull << 48) - 1), "s"(w < 6 ? ~0ull : 1ull), "s"(w < 6 ? 1024 : 752) : "memory", "scc");
      SB();
    };
    asm volatile("" : "=v"(vt[0]), "=v"(vt[1]), "=v"(vt[2]));
    load_v(IC<0>{});
    ESTAMP(56);
    uint32_t tp = rt32() + SC0(PO);
    int pstep = SC0(PT);
    if (TWE && w == TW) __builtin_amdgcn_s_setprio(1);
    int rix = 40;
    auto pace = [&]() {
      int rem;
      if (w == TW) asm volatile("1:\n\ts_memrealtime vcc\n\ts_waitcnt lgkmcnt(0)\n\ts_sub_i32 %0, %1, vcc_lo\n\ts_cmp_lt_u32 %0, 0x800\n\ts_cbranch_scc1 1b\n\ts_add_u32 %1, %1, %2"
                   : "=&s"(rem), "+s"(tp) : "s"(pstep) : "memory", "scc", "vcc");
    };
    const bool early = w < 4 || (TWE && w == TW);
    auto first = [&](auto Ic) {
      constexpr int i = decltype(Ic)::value, x = i <= 8 ? kord(i - 1) : vord(i - 8);
      u32x8* p = (i <= 8 ? kt : vt) + 3 * (x >> 1);
      if (!(x & 1)) asm volatile("" : "=v"(p[0]), "=v"(p[1]), "=v"(p[2]));
    };
    auto tile0 = [&](auto Ic) {
      constexpr int i = decltype(Ic)::value;
      if constexpr (i <= 8) load_k(IC<kord(i - 1)>{}); else load_v(IC<vord(i - 8)>{});
    };
    auto tile = [&](auto Ic) { first(Ic); tile0(Ic); };
    auto tile_e = [&](auto Ic) { first(Ic); if (early) tile0(Ic); SB(); };
    auto tile_l = [&](auto Ic) { SB(); if (!early) tile0(Ic); };
    auto piece = [&](int k, int n) { const int o = n * (b >> 2) + lane * 16; return lane * 16 < n ? O_S + ((b & 3) * NB + k + (o >> 13)) * BLK + (o & 8191) : OOR; };
    const int prb = O_PR + (b >> 3) * BLK + (w >> 2) * 8192 + (b & 7) * 1024 + (w & 3) * 256;
    uint32_t tv = 0;
    PINS(tv);
    if (b < NVOTE && w == SW) {
      __builtin_amdgcn_raw_buffer_store_b128((u32x4){0, 0, 0, 0}, rs, (lane & 12) != 12 && lane < 32 ? prb - 8192 - 512 + (lane >> 4) * 8192 + (lane & 12) * 64 + (lane & 3) * 16 : OOR, 0, 16);
      asm volatile("s_load_dword %0, %1, 0x80" : "+s"(tv) : "s"(tail + prb) : "memory");
    }
    SB();
    uint32_t ep = 0, xf[5] = {0, 0, 0, 0, 0}, xs = 0;
    asm volatile("" : "+v"(xf[0]), "+v"(xf[1]), "+v"(xf[2]), "+v"(xf[3]), "+v"(xf[4]), "+v"(xs), "+v"(ep));
    if (w == 1) asm volatile("global_load_dword %0, %1, off" : "+v"(xs) : "v"(lane < 40 ? b2 + 80 * jj + 2 * lane : b1 + 48 * g + 2 * lane - 80) : "memory");
    if (w == 0)
      asm volatile("global_load_dword %0, %7, %8\n\tglobal_load_dword %1, %7, %8 offset:256\n\tglobal_load_dword %2, %7, %8 offset:512\n\t"
                   "global_load_dword %3, %7, %8 offset:768\n\tglobal_load_dword %4, %7, %8 offset:1024\n\tglobal_load_dword %5, %9, %8\n\tglobal_load_dword %6, %10, %11 sc1"
                   : "+v"(xf[0]), "+v"(xf[1]), "+v"(xf[2]), "+v"(xf[3]), "+v"(xf[4]), "+v"(xs), "+v"(ep)
                   : "v"(4 * lane), "s"(x_in), "v"(160 * jj + 4 * min(lane, 39)), "v"(0), "s"(epoch) : "memory");
    SB();
    {
      int go[4];
#pragma unroll
      for (int r = 0; r < 4; ++r) { const int c = min(64 * r + lane, 249), row = (c * 41) >> 10; go[r] = row * (2 * QD) + min(c - 25 * row, 23) * 16; }
      unsigned keep;
      asm volatile("s_mov_b32 %0, m0\n\ts_mov_b32 m0, %6\n\ts_nop 0\n\tglobal_load_lds_dwordx4 %1, %5 nt\n\t"
                   "s_add_u32 m0, m0, 0x400\n\ts_nop 0\n\tglobal_load_lds_dwordx4 %2, %5 nt\n\t"
                   "s_add_u32 m0, m0, 0x400\n\ts_nop 0\n\tglobal_load_lds_dwordx4 %3, %5 nt\n\t"
                   "s_add_u32 m0, m0, 0x400\n\ts_mov_b64 exec, %7\n\tglobal_load_lds_dwordx4 %4, %5 nt\n\t"
                   "s_mov_b64 exec, -1\n\ts_mov_b32 m0, %0"
                   : "=&s"(keep) : "v"(go[0]), "v"(go[1]), "v"(go[2]), "v"(go[3]),
                     "s"(wq + (size_t)(80 * jj + 10 * w) * QD + 192 * jg), "s"(L_WQ + w * 10 * QS), "s"((1ull << 58) - 1) : "memory", "scc");
    }
    SB();
    {
      const int c = min(lane, 59), row = (c * 43) >> 8;
      unsigned keep;
      asm volatile("s_mov_b32 %0, m0\n\ts_mov_b32 m0, %3\n\ts_mov_b64 exec, %4\n\tglobal_load_lds_dwordx4 %1, %2 nt\n\ts_mov_b64 exec, -1\n\ts_mov_b32 m0, %0"
                   : "=&s"(keep) : "v"(row * (2 * FF) + (c - 6 * row) * 16), "s"(w1 + (size_t)(80 * jj + 10 * w) * FF + 48 * jg), "s"(L_W1 + w * 960), "s"((1ull << 60) - 1) : "memory");
    }
    SB();
    ESTAMP(57);
    BAR0();
    ESTAMP(58);
    tile_e(IC<1>{});
    ESTAMP(59);
    auto probe = [&](auto Oc) -> uint32_t {
      unsigned long long c0, c1; uint32_t v;
      asm volatile("s_memtime %0\n\ts_waitcnt lgkmcnt(0)\n\ts_load_dword %2, %3, %4\n\ts_waitcnt lgkmcnt(0)\n\ts_memtime %1\n\ts_waitcnt lgkmcnt(0)"
                   : "=&s"(c0), "=&s"(c1), "=&s"(v) : "s"(tail + prb), "n"(decltype(Oc)::value) : "memory");
      return (uint32_t)(c1 - c0);
    };
    {
      t16 = w * 1024 + lane * 16; bo = sm + kq * 16;
      sS = w == SW ? (n16 == 0 ? sm + S_XP + kq * 16 : L_ONE) : ck * 32;
      dT = lane < (w == SW ? 2 : 16) ? lane * 4 : OOR;
      LAS u32x4* T = (LAS u32x4*)(uintptr_t)(uint32_t)(L_TAB + t16);
      const int pk = ((lane >> 1) & 6) | (lane & 1), c4 = 8 * w + ((lane >> 3) & 6) + ((lane >> 1) & 1);
      T[0] = (u32x4){(uint32_t)(sm + S_ML + pk * 16), (uint32_t)(w < 6 ? sm + S_RED + pk * RS + c4 * 16 : sm + S_ML + pk * 16),
                     (uint32_t)(w == SW ? ((lane & ~4) == 0 ? lane : OOR) : (!(lane & 1) && w < 6) ? c4 * 16 + (lane & 12) : OOR), (uint32_t)(jp * 768 + w * 256 + ck * 16)};
      T[5 * NT] = (u32x4){(uint32_t)((kq >> 1) == ((n16 >> 2) & 1) ? sm + S_QB + (n16 & 1) * 192 + (n16 & 8) * 4 + (kq & 1) * 16 : sm + S_ZQ),
                          (uint32_t)(sm + S_RED + w * RS + lane * 4), (uint32_t)(sm + S_ML + w * 16), 0u};
      ix = ((n16 & 1) ? 0xeeeeu : 0x4444u) | (n16 >> 2) * 0x55550000u;
      const int gd = (gc >> 4) & 3, dimo = gd + (gc < 128 ? 8 * (gc & 15) + 4 * (gc >> 6) : 128 + 4 * (gc & 15));
      if (w == 1 && lane < 12) *(LAS u32x4*)(uintptr_t)(uint32_t)(sm + S_ZQ + lane * 16) = (u32x4){0, 0, 0, 0};
      T[NT] = (u32x4){(uint32_t)(sm + S_OV + opos(dimo)), (uint32_t)(L_WO + (w < 5 ? w : 0) * 32 + trl * 160 + trc),
                      (uint32_t)(sm + S_XP + 2 * kpos(xc)), (uint32_t)(L_W1 + (w < 3 ? w : 0) * 32 + trl * 96 + trc)};
      T[2 * NT] = (u32x4){(uint32_t)(w < 5 ? sm + S_HV + 2 * kpos(hc) : piece(0, 768) + (w & 1) * 8192), (uint32_t)(w < 5 ? L_W2 + w * 32 + trl * 160 + trc : piece(6, 1024)),
                          (uint32_t)(L_CS + 768 + 4 * hc), (uint32_t)((w & 1) ? 1024 + jp * 64 + (ck & 3) * 16 : lane * 16)};
      T[3 * NT] = (u32x4){(uint32_t)(sm + S_QB + qpos(gc)), (uint32_t)(L_CS + 4 * gc), (uint32_t)(L_WQ + (w < 6 ? w : 0) * 64 + trl * QS + trc), (uint32_t)(2 * xc)};
    }
    ESTAMP(60);
    tile_l(IC<1>{});
    BAR0();
    tile(IC<2>{});
    ESTAMP(61);
    asm volatile("s_waitcnt vmcnt(%8)" : "+v"(ep), "+v"(xf[0]), "+v"(xf[1]), "+v"(xf[2]), "+v"(xf[3]), "+v"(xf[4]), "+v"(xs), "+v"(cp) : "n"(3 * UP + 2) : "memory");
    if (w < 2) lwr4(sm + S_B + w * 256 + lane * 4, xs);
    if (w == 0) {
      float s1 = 0.f;
#pragma unroll
      for (int i = 0; i < 5; ++i) s1 += lo2f(xf[i]) + hi2f(xf[i]);
      const float mean = wsum_bcast(s1) * (1.0f / DM);
      float q0 = 0.f;
#pragma unroll
      for (int i = 0; i < 5; ++i) { const float d0 = lo2f(xf[i]) - mean, d1 = hi2f(xf[i]) - mean; q0 = fmaf(d0, d0, q0); q0 = fmaf(d1, d1, q0); }
      const float var = wsum_bcast(q0) * (1.0f / DM);
      const uint32_t mv = pk2(mean, var);
      const float rinv = __builtin_amdgcn_rcpf(rbf(__builtin_amdgcn_sqrtf(rbf(hi2f(mv) + 1e-5f))));
      if (lane < 48) lwr4(sm + S_XP + 2 * kpos(2 * lane), lane < 40 ? pk2(rbf(lo2f(xs) - lo2f(mv)) * rinv, rbf(hi2f(xs) - lo2f(mv)) * rinv) : 0u);
      if (lane < 48) lwr4(L_ONE + lane * 4, 0x3f803f80u);
      if (lane < 16) lwr2(sm + S_HV + 2 * kpos(48 + lane), 0);
      lwr4(L_FLAG, ep * 1664525u + 1013904223u);
    }
    if (b < NVOTE && (w & 3) != 3) lwr4(L_FLAG + 32 + 4 * w, probe(IC<0>{}));
    SB();
    asm volatile("s_waitcnt vmcnt(%0)" :: "n"(6) : "memory");
    STAMPV(1);
    lds_barrier();
    PINS(tv);
    tile_e(IC<3>{});
    tag = __builtin_amdgcn_readfirstlane(lrd4(L_FLAG));
    float pv[2];
    {
      const int xb = n16 >= 4 ? L_ONE : sm + S_XP + kq * 16;
      const u32x4 x0 = lrd16(xb), x1 = lrd16(xb + 64), x2 = lrd16(xb + 128);
#pragma unroll
      for (int r = 0; r < 2; ++r) {
        const int tl = min(2 * w + r, 14), rstr = tl < 12 ? QS : 96;
        const int a0 = (tl < 12 ? L_WQ : L_W1 - 12 * 32) + tl * 32 + trl * rstr + trc;
        f32x4 d = {0.f, 0.f, 0.f, 0.f};
        d = mfma32(x0, cat(trd(a0), trd(a0 + 16 * rstr)), d);
        d = mfma32(x1, cat(trd(a0 + 32 * rstr), trd(a0 + 48 * rstr)), d);
        d = mfma32(x2, cat(trd(a0 + 64 * rstr), (u32x2){0, 0}), d);
        pv[r] = d[0];
      }
    }
    gu64* dst = G + G_P + b * RP + (kq == 0 ? 0 : 192) + n16 + 32 * w;
    const int cu = (lane >> 2) & 7;
    int so = (w & 3) == 0 ? g * BLK + (w >> 2) * 8192 + j * 1024 + lane * 16
             : w == 1 && cu < 5 ? O_C + (2 * j + (cu >> 2)) * BLK + (lane >> 5) * 8192 + (cu & 3) * 2048 + ((g + 7 * cu) & 31) * 64 + (lane & 3) * 16 : OOR;
    if (w == TW) {
      unsigned xcc;
      asm volatile("s_getreg_b32 %0, hwreg(HW_REG_XCC_ID)" : "=s"(xcc));
      const u32x4 l0 = lrd16(L_FLAG + 32), l1 = lrd16(L_FLAG + 48);
      const uint32_t vote = ((min(min(l1.x, l1.y), l1.z) < min(min(l0.x, l0.y), l0.z) ? 1u : 0u) ^ (xcc >> 2)) & 1u;
      pv[1] = uf(vote | ((xcc >> 1) & 2) | (rt32() << 2));
    }
    PIN1(pv[0]); PIN1(pv[1]); PIN1(dst); PIN1(so);
    tile_l(IC<3>{});
    STAMPV(2);
    pace();
    BAR0();
    if (kq == 1 || (kq == 0 && w < 6)) {
      __hip_atomic_store(dst, ((unsigned long long)tag << 32) | __float_as_uint(pv[0]), RLX_AGENT);
      if (w < 7) __hip_atomic_store(dst + 16, ((unsigned long long)tag << 32) | __float_as_uint(pv[1]), RLX_AGENT);
    }
    if (w == TW && b < NVOTE && lane == 0) __hip_atomic_store(G + G_F + b, ((unsigned long long)tag << 32) | __float_as_uint(pv[1]), RLX_AGENT);
    if ((w & 2) == 0 && w != 5) __builtin_amdgcn_raw_buffer_store_b128((u32x4){SENT, SENT, SENT, SENT}, rs, so, 0, 16);
    SB();
    tile(IC<RT>{});
    pace();
    BAR0();
    tile_e(IC<RT + 1>{});
    {
      auto rd = [&](int off) { return lo2f(*(const LAS uint16_t*)(uintptr_t)(uint32_t)(sm + S_B + off)); };
      *(LAS f32x4*)(uintptr_t)(uint32_t)(L_TAB + 4 * NT * 16 + t16) = (f32x4){rd(2 * xc), rd(256 + 2 * xc), rd(416 + 2 * hc), 0.f};
    }
    tile_l(IC<RT + 1>{});
    pace();
    BAR0();
    tile(IC<RT + 2>{});
    pace();
    BAR0();
    u32x4 er = {0, 0, 0, 0};
    asm volatile("" : "+v"(er));
    auto ereq = [&](const char* src) { asm volatile("global_load_dwordx4 %0, %1, off sc1" : "+v"(er) : "v"(src) : "memory"); };
    auto efin = [&](auto Nc, const char* src) {
      asm volatile("s_waitcnt vmcnt(%1)" : "+v"(er) : "n"(decltype(Nc)::value) : "memory");
      if (XOFF & 4) return;
#pragma unroll 1
      for (unsigned sp = 0; sp < SPIN_LIMIT; ++sp) {
        if (__all(tagbad(er, tag) == 0)) break;
        __builtin_amdgcn_s_sleep(4);
        asm volatile("global_load_dwordx4 %0, %1, off sc1\n\ts_waitcnt vmcnt(0)" : "+v"(er) : "v"(src) : "memory");
      }
    };
    {
      const int un = j + 8 * w;
      const bool mine = un < 27;
      const char* src = mine ? (const char*)(G + G_P + (8 * g + jp) * RP) + un * 128 + ck * 16 : (const char*)(G + G_F) + n16 * 16;
      if (mine || w == TW) ereq(src);
      SB();
      tile(IC<RT + 3>{});
      pace();
      BAR0();
      tile_e(IC<RT + 4>{});
      asm volatile("s_waitcnt vmcnt(%0)" :: "n"(nreq(RT, RT + 3)) : "memory");
      if (mine) efin(IC<nreq(RT + 3, RT + 4)>{}, src);
      const float t = swapadd32(uf(er.x), uf(er.z));
      uint32_t sv = __float_as_uint(ror8add(swapadd16(t, t)));
      gu64* sd = G + G_R + g * RP + un * 16 + 2 * ck + (lane >> 5);
      PIN1(sv); PIN1(sd);
      tile_l(IC<RT + 4>{});
      pace();
      BAR0();
      if (mine && !(lane & 24)) __hip_atomic_store(sd, ((unsigned long long)tag << 32) | sv, RLX_AGENT);
      if (w == TW) {
        if (b < NVOTE && lane == 0) __hip_atomic_store(G + G_F + NVOTE + b, ((unsigned long long)tag << 32) | rt32(), RLX_AGENT);
        efin(IC<nreq(RT + 3, RT + 4)>{}, src);
        const uint32_t hx = (uint32_t)__ballot(er.x & 2), hz = (uint32_t)__ballot(er.z & 2);
        lwr4(L_FLAG + 16, (__popcll(__ballot(er.x & 1)) + __popcll(__ballot(er.z & 1))) << 8 | (hx & 1) | ((hz & 1) << 1) | ((hx & 2) << 1) | ((hz & 2) << 2));
        if (SC(PB)) {
          const float d = (float)((int)(er.x - (tp << 2)) >> 2) + (float)((int)(er.z - (tp << 2)) >> 2);
          tp += __builtin_amdgcn_readfirstlane((int)(wsum_bcast(d) * (1.f / 128.f))) + SC(PA);
          pstep = SC(PB);
        }
      }
      tp = __builtin_amdgcn_readfirstlane(tp); pstep = __builtin_amdgcn_readfirstlane(pstep);
      SB();
    }
    tile(IC<RT + 5>{});
    pace();
    BAR0();
    tile(IC<RT + 6>{});
    pace();
    BAR0();
    {
      const int v0 = min(128 * w + 2 * lane, RP - 2);
      const char* src = w < 4 ? (const char*)(G + G_R + g * RP + v0) : (const char*)(G + G_F + NVOTE) + n16 * 16;
      if (w < 4 || w == TW) ereq(src);
      SB();
      tile(IC<RT + 7>{});
      pace();
      BAR0();
      tile(IC<RT + 8>{});
      if (w == TW && SC(PD)) {
        asm volatile("s_waitcnt vmcnt(%1)" : "+v"(er) : "n"(nreq(RT + 7, RT + 8)) : "memory");
        const bool v0 = er.y == tag, v1 = er.w == tag;
        const float d = (v0 ? (float)(int)(er.x - tp) : 0.f) + (v1 ? (float)(int)(er.z - tp) : 0.f);
        const int n = __popcll(__ballot(v0)) + __popcll(__ballot(v1));
        if (n > 32) {
          tp += __builtin_amdgcn_readfirstlane((int)(wsum_bcast(d) * __builtin_amdgcn_rcpf((float)n))) + SC(PC);
          pstep = SC(PD);
        }
      }
      tp = __builtin_amdgcn_readfirstlane(tp); pstep = __builtin_amdgcn_readfirstlane(pstep);
      pace();
      BAR0();
      tile(IC<RT + 9>{});
      if (w < 4) {
        efin(IC<nreq(RT + 7, RT + 9)>{}, src);
        if (v0 < 192) lwr4(sm + S_QB + qpos(v0), pk2(uf(er.x), uf(er.z)));
        else *(LAS u32x2*)(uintptr_t)(uint32_t)(L_CS + 4 * (v0 - 192)) = (u32x2){er.x, er.z};
      }
    }
    SB();
    STAMPV(3);
    pace();
    lds_barrier();
    tile_e(IC<RT + 10>{});
    STAMPV(4);
    asm volatile("s_waitcnt vmcnt(%12)"
                 : GRP(2, 9)(kt[0]), GRP(10, 17)(kt[1]), GRP(18, 25)(kt[2]), GRP(26, 33)(kt[3]), GRP(34, 41)(kt[4]), GRP(42, 49)(kt[5]),
                   GRP(50, 57)(kt[6]), GRP(58, 65)(kt[7]), GRP(66, 73)(kt[8]), GRP(74, 81)(kt[9]), GRP(82, 89)(kt[10]), GRP(90, 97)(kt[11])
                 : "n"(nreq(9, RT + 9)) : "memory");
    const u32x4 aT = lrd16(L_TAB + 5 * NT * 16 + t16);
    uint32_t pb;
    f32x4 dv[3];
    {
      float mx, ev;
      pb = qk_softmax(mx, ev, dv, aT.x, 1.f);
      const float ls = wsum63(ev);
      if (lane == 63) *(LAS f32x2*)(uintptr_t)(uint32_t)aT.z = (f32x2){mx, ls};
    }
#define VFIX(v, e0, e1, e2, e3, g0, g1, g2) if constexpr (V == v) \
      asm volatile("s_waitcnt vmcnt(%3)\n\tv_swap_b32 v" #e0 ", v" #e2 "\n\tv_swap_b32 v" #e1 ", v" #e3 "\n\ts_nop 0" \
                   : g0(vt[a]), g1(vt[a + 1]), g2(vt[a + 2]) : "n"(decltype(YN)::value) : "memory")
    auto v_fix = [&](auto Vc, auto YN) {
      constexpr int V = decltype(Vc)::value, a = 3 * (V >> 1);
      SB();
      VFIX(0, 100, 101, 106, 107, GRP(98, 105), GRP(106, 113), GRP(114, 121));
      VFIX(1, 104, 105, 110, 111, GRP(98, 105), GRP(106, 113), GRP(114, 121));
      VFIX(2, 124, 125, 130, 131, GRP(122, 129), GRP(130, 137), GRP(138, 145));
      VFIX(3, 128, 129, 134, 135, GRP(122, 129), GRP(130, 137), GRP(138, 145));
      VFIX(4, 148, 149, 154, 155, GRP(146, 153), GRP(154, 161), GRP(162, 169));
      VFIX(5, 152, 153, 158, 159, GRP(146, 153), GRP(154, 161), GRP(162, 169));
      VFIX(6, 172, 173, 178, 179, GRP(170, 177), GRP(178, 185), GRP(186, 193));
      VFIX(7, 176, 177, 182, 183, GRP(170, 177), GRP(178, 185), GRP(186, 193));
      SB();
    };
    auto mm_step = [&](auto Ic, auto YN) {
      constexpr int i = decltype(Ic)::value;
      u32x4 pa = pset<i>(pb);
      PIN1(pa);
      v_fix(IC<2 * i + 1>{}, YN);
      dv[2] = smf<1>(pa, vt[3 * i + 2], dv[2], ix); SB();
      dv[0] = smf<1>(pa, vt[3 * i], dv[0], ix); dv[1] = smf<1>(pa, vt[3 * i + 1], dv[1], ix);
    };
    SB();
    wo_prep();
    tile_l(IC<RT + 10>{});
    pace();
    lds_barrier();
    tile_e(IC<15>{});
    v_fix(IC<0>{}, IC<16>{}); v_fix(IC<2>{}, IC<16>{});
    mm_step(IC<0>{}, IC<16>{}); mm_step(IC<1>{}, IC<12>{});
    tile_l(IC<15>{});
    BAR0();
    load_wo();
    STAMPV(26);
    {
      const int f0 = __builtin_amdgcn_readfirstlane(lrd4(L_FLAG + 16)), votes = f0 >> 8;
      const int m4 = (f0 & 15) ^ ((SC(CPAR) < 2 ? SC(CPAR) : votes > 2 * NVOTE) ? 15 : 0);
      const int pm = m4 | ((m4 ^ 15) << 4);
      auto par = [&](int x) { return ((pm >> x) & 1) * 8192; };
      const int cw = O_C + (w >> 2) * BLK + (w & 3) * 2048;
      oG = g * BLK + par(jj); oD = oG + (w == SW ? O_SG + j * 32 : j * 768);
      rC = 2 * jj * BLK + par(jj) + cw - w * 1024; eC = 2 * j * BLK + par(j) + cw + ((g + 7 * w) & 31) * 64;
      eH = jg * BLK + par(jg >> 2) + (w == SW ? O_SH + jj * 32 : O_H + (w < 2 ? jj * 128 + w * 64 : 1024 + jj * 64));
      rK = par(jj) + O_DB - TW * 1024;
      dG = O_S + ((jj & 3) * NB + 6 + (g & 3) - g) * BLK; dH = O_S + (((jg >> 2) & 3) * NB + 6 + (jg & 3) - jg) * BLK;
      dR = O_S - O_C + ((jj & 3) * NB + 10 - 2 * jj) * BLK; dE = O_S - O_C + ((j & 3) * NB + 10 - 2 * j) * BLK;
      eQ = jg * BLK + dH - 4 * BLK + par(jg >> 2) + (w == SW ? O_SG + jj * 32 : jj * 768 + w * 128);
#define UNI(a) a = __builtin_amdgcn_readfirstlane(a)
      UNI(oG); UNI(oD); UNI(rC); UNI(eC); UNI(eH); UNI(eQ); UNI(rK);
      PINS2(oG, oD); PINS2(rC, eC); PINS2(eH, eQ); PINS(rK);
    }
    v_fix(IC<4>{}, IC<nreq(13, 15)>{}); v_fix(IC<6>{}, IC<nreq(14, 15)>{});
    mm_step(IC<2>{}, IC<nreq(15, 15)>{});
    SB();
    u32x4 cT = lrd16(L_TAB + t16);
    PIN1(cT);
    mm_step(IC<3>{}, IC<5>{});
    STAMPV(27);
    lwr4(aT.y + 512, __float_as_uint(dv[2][0])); lwr4(aT.y, __float_as_uint(dv[0][0])); lwr4(aT.y + 256, __float_as_uint(dv[1][0]));
    lds_barrier();
    pv_combine(cT, oD, w == SW);
    s0 = cT.w;
  }

  if (TWE) __builtin_amdgcn_s_setprio(0);
  {
    const u32x4 rs4 = {(uint32_t)(uintptr_t)tail, (uint32_t)((uintptr_t)tail >> 32) & 0xffffu, 128u * BLK, 0x00020000u};
    const u32x4 ro4 = {(uint32_t)(uintptr_t)out, (uint32_t)((uintptr_t)out >> 32) & 0xffffu, 2u * DM, 0x00020000u};
    asm volatile(TAIL_ASM
                 : [s0] "+v"(s0), [tc] "+s"(tcur), [te] "+s"(tE), [oG] "+s"(oG), [rC] "+s"(rC), [eC] "+s"(eC), [eH] "+s"(eH)
                 : [t16] "v"(t16), [bo] "v"(bo), [ss] "v"(sS), [dt] "v"(dT), [ix] "v"(ix), [rs] "s"(rs4), [ro] "s"(ro4), [w] "s"(w0), [far] "s"(min(b0 >> 3, 1)),
                   [oD] "s"(oD + dG), [dG] "s"(dG), [dH] "s"(dH), [dR] "s"(dR), [dE] "s"(dE), [eQ] "s"(eQ), [rK] "s"(rK), [tag] "s"(tag), [cw] "s"(O_DB + b0 * 8), [oo] "s"(160 * b0),
                   [p3e] "s"(SC(P3E)), [p2] "s"(SC(P2)), [p3] "s"(SC(P3)), [p4] "s"(SC(P4)), [p5] "s"(SC(P5)), [p1] "s"(SC(P1)), [rmin] "s"(SC(RMIN)), [rcons] "s"(SC(RCONS)),
                   [eps] "s"(1e-5f), [small] "s"(SMALL), [spin] "n"(SPIN_LIMIT), [tab] "n"(L_TAB), [xp] "n"(S_XP), [ov] "n"(S_OV), [hv] "n"(S_HV),
                   [osg] "n"(O_SG), [osh] "n"(O_SH), [oh] "n"(O_H), [oor] "n"(OOR), [qsw] "n"(L_FLAG + 4),
                   "{v[2:9]}"(kt[0]), "{v[10:17]}"(kt[1]), "{v[18:25]}"(kt[2]), "{v[26:33]}"(kt[3]), "{v[34:41]}"(kt[4]), "{v[42:49]}"(kt[5]),
                   "{v[50:57]}"(kt[6]), "{v[58:65]}"(kt[7]), "{v[66:73]}"(kt[8]), "{v[74:81]}"(kt[9]), "{v[82:89]}"(kt[10]), "{v[90:97]}"(kt[11]),
                   "{v[98:105]}"(vt[0]), "{v[106:113]}"(vt[1]), "{v[114:121]}"(vt[2]), "{v[122:129]}"(vt[3]), "{v[130:137]}"(vt[4]),
                   "{v[138:145]}"(vt[5]), "{v[146:153]}"(vt[6]), "{v[154:161]}"(vt[7]), "{v[162:169]}"(vt[8]), "{v[170:177]}"(vt[9]),
                   "{v[178:185]}"(vt[10]), "{v[186:193]}"(vt[11])
                 : "memory", "scc", "vcc",
                   "v194", "v195", "v196", "v197", "v198", "v199", "v200", "v201", "v202", "v203", "v204", "v205", "v206", "v207", "v208", "v209",
                   "v210", "v211", "v212", "v213", "v214", "v215", "v216", "v217", "v218", "v219", "v220", "v221", "v222", "v223", "v224", "v225",
                   "v226", "v227", "v228", "v229", "v230", "v231", "v232", "v233", "v234", "v235", "v236", "v237", "v238", "v239", "v240", "v241",
                   "v242", "v243", "v244", "v245", "v246", "v247", "s80", "s81", "s82", "s83", "s84", "s85", "s86", "s87", "s88", "s89", "s90", "s91",
                   "s92", "s93", "s94", "s95", "s96", "s97", "s98", "s99"
                 );
  }
  if (b0 == 8 && t16 == SW * 1024) __hip_atomic_store((gu32*)epoch, tag, RLX_AGENT);
}

extern "C" void kernel_launch(void* const* d_in, const int* in_sizes, int n_in, void* d_out, int out_size,
                              void* d_ws, size_t ws_size, hipStream_t stream) {
  (void)in_sizes; (void)n_in; (void)out_size; (void)ws_size;
  (void)hipFuncSetAttribute((const void*)mega_kernel, hipFuncAttributeMaxDynamicSharedMemorySize, L_TOTAL);
  mega_kernel<<<NWG, NT, L_TOTAL, stream>>>(
      (const uint16_t*)d_in[0], (const uint16_t*)d_in[1], (const uint16_t*)d_in[2], (const uint16_t*)d_in[3],
      (const uint16_t*)d_in[4], (const uint16_t*)d_in[6], (const uint16_t*)d_in[7], (const uint16_t*)d_in[8],
      (const uint16_t*)d_in[9], (uint16_t*)d_out, (unsigned*)d_ws
  );
}
```

```cpp
#include <hip/hip_runtime.h>
#include <hip/hip_bf16.h>
#include <stdint.h>

#define GAS __attribute__((address_space(1)))
#define LAS __attribute__((address_space(3)))
typedef GAS unsigned long long gu64;
typedef GAS unsigned gu32;
typedef uint32_t u32x2 __attribute__((ext_vector_type(2)));
typedef uint32_t u32x4 __attribute__((ext_vector_type(4)));
typedef uint32_t u32x8 __attribute__((ext_vector_type(8)));
typedef float f32x2 __attribute__((ext_vector_type(2)));
typedef float f32x4 __attribute__((ext_vector_type(4)));
typedef short s16x4 __attribute__((ext_vector_type(4)));
typedef __attribute__((ext_vector_type(2))) __bf16 bf16x2;
typedef __attribute__((ext_vector_type(8))) __bf16 bf16x8;
typedef __attribute__((ext_vector_type(16))) __bf16 bf16x16;
#define RLX_AGENT __ATOMIC_RELAXED, __HIP_MEMORY_SCOPE_AGENT

constexpr int DM = 640, NH = 32, DH = 192, SK = 32768, WIN = 4096, FF = 1536, QD = NH * DH;
constexpr int NWG = 256, NT = 512, NW = NT / 64;
#define SPINB 20
constexpr unsigned SPIN_LIMIT = 1u << SPINB;
constexpr int TW = 7, SW = 6;
constexpr int CODEPF = 15;
#define TWE 0
constexpr int UP = 3;
constexpr int nreq(int i0, int i1) { int n = 0; for (int i = i0; i <= i1 && i < 16; ++i) n += i == 15 ? 9 : (i == 0 || i > 8) ? 4 : 3; return n; }
#define XOFF 0
constexpr int kord(int n) { return 2 * (n & 3) + (n >> 2); }
constexpr int vord(int n) { return (n & 4) + 2 * (n & 1) + ((n >> 1) & 1); }
constexpr int RT = UP + 1;
constexpr int BKD = 3, BKO = 1;
constexpr float LOG2E = 1.4426950408889634f;
constexpr float SMALL = 40.f;
constexpr uint32_t SENT = 0xffffffffu;
constexpr int OOR = 1 << 22;

constexpr int RP = 27 * 16;
constexpr int G_P = 0, G_R = G_P + NWG * RP, G_F = G_R + NH * RP;
constexpr int BLK = 16384, NB = 14, O_SG = 6144, O_SH = 6400, O_H = 6656, O_C = 32 * BLK, O_S = 48 * BLK, O_DB = O_S + 4 * NB * BLK, O_PR = O_DB + BLK;
constexpr int NVOTE = 32;

constexpr int QS = 400;
constexpr int RS = 800;
constexpr int S_XP = 0, S_OV = 256, S_HV = 768, S_QB = 896, S_ZQ = 1280, S_RED = 1472, S_ML = S_RED + NW * RS, S_B = S_ML + 128, S_END = S_B + 512;
constexpr int L_SM = 0, L_CS = L_SM + S_END, L_ONE = L_CS + 960, L_FLAG = L_ONE + 192, L_SCH = L_FLAG + 64, L_TAB = L_SCH + 64,
              L_WQ = L_TAB + 6 * NT * 16, L_WO = L_WQ + 32768, L_W2 = L_WO + 30720, L_W1 = L_W2 + 8192, L_TOTAL = L_W1 + 8192;
static_assert(L_TOTAL <= 160 * 1024 && L_TOTAL > 80 * 1024 && S_END % 16 == 0, "LDS");

enum { P3E, P2, P3, P4, P5, P1, RMIN, RCONS, CPAR, PO, PT, PA, PB, PC, PD, PW, NSCH };
#define SCHED_INIT {120, 124, 58, 62, 57, 67, 5, 50, 2, 410, 88, 620, 88, 470, 90, 6}
constexpr int sched[NSCH] = SCHED_INIT;
#define SCHED_ARG
#define SC0(i) sched[i]
#define SC(i) sched[i]

__device__ __forceinline__ float lo2f(uint32_t p) { return __uint_as_float(p << 16); }
__device__ __forceinline__ float hi2f(uint32_t p) { return __uint_as_float(p & 0xffff0000u); }
__device__ __forceinline__ uint32_t pk2(float a, float b) {
  f32x2 v = {a, b};
  return __builtin_bit_cast(uint32_t, __builtin_convertvector(v, bf16x2));
}
__device__ __forceinline__ float rbf(float f) { return __uint_as_float(pk2(0.f, f)); }
__device__ __forceinline__ uint32_t pair_lo(uint32_t a, uint32_t b) { return __builtin_amdgcn_perm(b, a, 0x05040100u); }
__device__ __forceinline__ uint32_t pair_hi(uint32_t a, uint32_t b) { return __builtin_amdgcn_perm(b, a, 0x07060302u); }
template <int CTRL> __device__ __forceinline__ float dppv(float v) {
  return __int_as_float(__builtin_amdgcn_update_dpp(0, __float_as_int(v), CTRL, 0xf, 0xf, false));
}
template <int CTRL, int RM> __device__ __forceinline__ float dpp_add(float v) {
  return v + __int_as_float(__builtin_amdgcn_update_dpp(0, __float_as_int(v), CTRL, RM, 0xf, CTRL < 0x140));
}
__device__ __forceinline__ float wsum63(float v) {
  v = dpp_add<0x111, 0xf>(v); v = dpp_add<0x112, 0xf>(v); v = dpp_add<0x114, 0xf>(v); v = dpp_add<0x118, 0xf>(v);
  v = dpp_add<0x142, 0xa>(v); v = dpp_add<0x143, 0xc>(v);
  return v;
}
__device__ __forceinline__ float wsum_bcast(float v) {
  return __int_as_float(__builtin_amdgcn_readlane(__float_as_int(wsum63(v)), 63));
}
__device__ __forceinline__ float swapadd32(float a, float b) {
  auto r = __builtin_amdgcn_permlane32_swap(__float_as_uint(a), __float_as_uint(b), false, false);
  return __uint_as_float(r[0]) + __uint_as_float(r[1]);
}
__device__ __forceinline__ float swapadd16(float a, float b) {
  auto r = __builtin_amdgcn_permlane16_swap(__float_as_uint(a), __float_as_uint(b), false, false);
  return __uint_as_float(r[0]) + __uint_as_float(r[1]);
}
__device__ __forceinline__ float ror8add(float v) { return v + dppv<0x128>(v); }
__device__ __forceinline__ float ror8add2(float a, float b) {
  float r;
  asm("s_nop 1\n\tv_add_f32_dpp %0, %1, %1 row_ror:8 row_mask:0xf bank_mask:0x3\n\tv_add_f32_dpp %0, %2, %2 row_ror:8 row_mask:0xf bank_mask:0xc" : "=&v"(r) : "v"(a), "v"(b));
  return r;
}
__device__ __forceinline__ float red16(float a, float b, float c, float d) { return swapadd16(swapadd32(a, b), swapadd32(c, d)); }
__device__ __forceinline__ float red_cols(const u32x4& a, const u32x4& b) {
  float x, y, z, u;
  asm("v_add_f32 %0, %4, %8\n\tv_add_f32 %1, %5, %9\n\tv_add_f32 %2, %6, %10\n\tv_add_f32 %3, %7, %11\n\t"
      "v_permlane32_swap_b32 %0, %1\n\ts_nop 0\n\tv_permlane32_swap_b32 %2, %3\n\t"
      "v_add_f32 %0, %0, %1\n\tv_add_f32 %2, %2, %3\n\ts_nop 1\n\t"
      "v_permlane16_swap_b32 %0, %2\n\tv_add_f32 %0, %0, %2\n\ts_nop 1\n\t"
      "v_add_f32_dpp %0, %0, %0 row_ror:8 row_mask:0xf bank_mask:0xf\n\ts_nop 1\n\t"
      "v_add_f32_dpp %0, %0, %0 row_ror:4 row_mask:0xf bank_mask:0xf"
      : "=&v"(x), "=&v"(y), "=&v"(z), "=&v"(u) : "v"(a.x), "v"(a.y), "v"(a.z), "v"(a.w), "v"(b.x), "v"(b.y), "v"(b.z), "v"(b.w));
  return x;
}
__device__ __forceinline__ float sum_lo3(float v) { v += dppv<0xb1>(v); v += dppv<0x4e>(v); return v + dppv<0x141>(v); }
__device__ __forceinline__ float max_lo3a(float v) {
  float r;
  asm("s_nop 1\n\tv_max_f32_dpp %0, %1, %1 quad_perm:[1,0,3,2] row_mask:0xf bank_mask:0xf\n\ts_nop 1\n\t"
      "v_max_f32_dpp %0, %0, %0 quad_perm:[2,3,0,1] row_mask:0xf bank_mask:0xf\n\ts_nop 1\n\t"
      "v_max_f32_dpp %0, %0, %0 row_half_mirror row_mask:0xf bank_mask:0xf" : "=&v"(r) : "v"(v));
  return r;
}
__device__ __forceinline__ f32x4 scale_sum_lo3(float f, const u32x4& v) {
  float a, b, c, d;
#define DPP4(ctl) "v_add_f32_dpp %0, %0, %0 " ctl " row_mask:0xf bank_mask:0xf\n\tv_add_f32_dpp %1, %1, %1 " ctl " row_mask:0xf bank_mask:0xf\n\t" \
                  "v_add_f32_dpp %2, %2, %2 " ctl " row_mask:0xf bank_mask:0xf\n\tv_add_f32_dpp %3, %3, %3 " ctl " row_mask:0xf bank_mask:0xf\n\t"
  asm("s_nop 0\n\tv_mul_f32 %0, %4, %5\n\tv_mul_f32 %1, %4, %6\n\tv_mul_f32 %2, %4, %7\n\tv_mul_f32 %3, %4, %8\n\t"
      DPP4("quad_perm:[1,0,3,2]") DPP4("quad_perm:[2,3,0,1]") DPP4("row_half_mirror")
      : "=&v"(a), "=&v"(b), "=&v"(c), "=&v"(d) : "v"(f), "v"(v.x), "v"(v.y), "v"(v.z), "v"(v.w));
  return (f32x4){a, b, c, d};
}
__device__ __forceinline__ float sum_b320(const u32x4& v) {
  float a, b, c;
  asm("v_add_f32_dpp %0, %3, %3 row_ror:8 row_mask:0xf bank_mask:0x3\n\tv_add_f32_dpp %1, %4, %4 row_ror:8 row_mask:0xf bank_mask:0x3\n\t"
      "v_add_f32_dpp %0, %5, %5 row_ror:8 row_mask:0xf bank_mask:0xc\n\tv_add_f32_dpp %1, %6, %6 row_ror:8 row_mask:0xf bank_mask:0xc\n\ts_nop 0\n\t"
      "v_add_f32_dpp %2, %0, %0 row_ror:12 row_mask:0xf bank_mask:0x5\n\tv_add_f32_dpp %2, %1, %1 row_ror:4 row_mask:0xf bank_mask:0xa\n\ts_nop 1\n\t"
      "v_add_f32_dpp %2, %2, %2 quad_perm:[1,0,3,2] row_mask:0xf bank_mask:0xf"
      : "=&v"(a), "=&v"(b), "=&v"(c) : "v"(v.x), "v"(v.y), "v"(v.z), "v"(v.w));
  return c;
}
template <int N> __device__ __forceinline__ uint32_t rbc(uint32_t v) { return __builtin_amdgcn_mov_dpp(v, 0x150 + N, 0xf, 0xf, false); }
template <int I> __device__ __forceinline__ u32x4 pset(uint32_t p) { return (u32x4){rbc<4 * I>(p), rbc<4 * I + 1>(p), rbc<4 * I + 2>(p), rbc<4 * I + 3>(p)}; }
#define RQ "+{v[194:197]}"(r), "+{v[198:201]}"(q)
__device__ __forceinline__ float red_o(u32x4 r, u32x4 q, uint32_t l) {
  float v, t;
  asm("v_permlane32_swap_b32 v194, v195\n\tv_permlane32_swap_b32 v196, v197\n\tv_add_f32_dpp %1, %4, %4 quad_perm:[1,0,3,2] row_mask:0xf bank_mask:0xf\n\t"
      "v_permlane32_swap_b32 v198, v199\n\tv_permlane32_swap_b32 v200, v201\n\t"
      "v_add_f32 v194, v194, v195\n\tv_add_f32 v196, v196, v197\n\tv_add_f32_dpp %1, %1, %1 quad_perm:[2,3,0,1] row_mask:0xf bank_mask:0xf\n\t"
      "v_add_f32 v198, v198, v199\n\tv_add_f32 v200, v200, v201\n\tv_permlane16_swap_b32 v194, v196\n\t"
      "v_add_f32_dpp %1, %1, %1 row_half_mirror row_mask:0xf bank_mask:0xf\n\tv_permlane16_swap_b32 v198, v200\n\t"
      "v_add_f32 v194, v194, v196\n\tv_add_f32 v198, v198, v200\n\tv_rcp_f32 %1, %1\n\t"
      "v_add_f32_dpp %0, v194, v194 row_ror:8 row_mask:0xf bank_mask:0x3\n\tv_add_f32_dpp %0, v198, v198 row_ror:8 row_mask:0xf bank_mask:0xc\n\t"
      "v_mul_f32 %0, %0, %1"
      : "=&v"(v), "=&v"(t), RQ : "v"(l));
  return v;
}
__device__ __forceinline__ float red_q(u32x4 r, u32x4 q, uint32_t sx, uint32_t sy, float cs, float& s2, float& mean) {
  float v;
  asm("v_permlane32_swap_b32 v194, v195\n\tv_permlane32_swap_b32 v196, v197\n\t"
      "v_add_f32_dpp %2, %6, %6 quad_perm:[1,0,3,2] row_mask:0xf bank_mask:0xf\n\tv_add_f32_dpp %1, %5, %5 quad_perm:[1,0,3,2] row_mask:0xf bank_mask:0xf\n\t"
      "v_permlane32_swap_b32 v198, v199\n\tv_permlane32_swap_b32 v200, v201\n\tv_add_f32 v194, v194, v195\n\tv_add_f32 v196, v196, v197\n\t"
      "v_add_f32_dpp %2, %2, %2 quad_perm:[2,3,0,1] row_mask:0xf bank_mask:0xf\n\tv_add_f32_dpp %1, %1, %1 quad_perm:[2,3,0,1] row_mask:0xf bank_mask:0xf\n\t"
      "v_add_f32 v198, v198, v199\n\tv_add_f32 v200, v200, v201\n\tv_permlane16_swap_b32 v194, v196\n\t"
      "v_add_f32_dpp %2, %2, %2 row_half_mirror row_mask:0xf bank_mask:0xf\n\tv_permlane16_swap_b32 v198, v200\n\t"
      "v_add_f32 v194, v194, v196\n\tv_add_f32 v198, v198, v200\n\tv_mul_f32 %2, 0x3acccccd, %2\n\t"
      "v_add_f32_dpp %0, v194, v194 row_ror:8 row_mask:0xf bank_mask:0x3\n\tv_add_f32_dpp %0, v198, v198 row_ror:8 row_mask:0xf bank_mask:0xc\n\t"
      "v_cvt_pk_bf16_f32 v195, 0, %2\n\tv_fma_f32 %0, -v195, %7, %0\n\t"
      "v_add_f32_dpp %1, %1, %1 row_half_mirror row_mask:0xf bank_mask:0xf"
      : "=&v"(v), "=&v"(s2), "=&v"(mean), RQ : "v"(sx), "v"(sy), "v"(cs));
  return v;
}
#define ZERO4(d, a, b, c, e) asm volatile("v_mov_b64 v[" #a ":" #b "], 0\n\tv_mov_b64 v[" #c ":" #e "], 0" : "={v[" #a ":" #e "]}"(d))
__device__ __forceinline__ float ln_rinv(float s2, float mean) {
  const float var = rbf(fmaf(-mean, mean, s2 * (1.0f / DM)));
  return __builtin_amdgcn_rcpf(rbf(__builtin_amdgcn_sqrtf(rbf(fabsf(var) + 1e-5f))));
}
__device__ __forceinline__ int opos(int k) { return ((k >> 1) & 1) * 192 + (k >> 6) * 64 + (k & 32) + (k & 8) * 2 + (k & 4) * 2 + ((k >> 4) & 1) * 4 + (k & 1) * 2; }
__device__ __forceinline__ void sum2_lo3(float x, float y, float& a, float& b) {
#define DPP2(ctl) "s_nop 0\n\tv_add_f32_dpp %0, %0, %0 " ctl " row_mask:0xf bank_mask:0xf\n\tv_add_f32_dpp %1, %1, %1 " ctl " row_mask:0xf bank_mask:0xf\n\t"
  asm("s_nop 1\n\tv_add_f32_dpp %0, %2, %2 quad_perm:[1,0,3,2] row_mask:0xf bank_mask:0xf\n\tv_add_f32_dpp %1, %3, %3 quad_perm:[1,0,3,2] row_mask:0xf bank_mask:0xf\n\t"
      DPP2("quad_perm:[2,3,0,1]") DPP2("row_half_mirror") : "=&v"(a), "=&v"(b) : "v"(x), "v"(y));
}
__device__ __forceinline__ void lds_barrier() { asm volatile("s_waitcnt lgkmcnt(0)\n\ts_barrier" ::: "memory"); }
__device__ __forceinline__ uint32_t tagbad(const u32x4& r, uint32_t tag) { return (r.y ^ tag) | (r.w ^ tag); }
__device__ __forceinline__ float uf(uint32_t u) { return __uint_as_float(u); }
__device__ __forceinline__ uint32_t rt32() {
  unsigned long long t;
  asm volatile("s_memrealtime %0\n\ts_waitcnt lgkmcnt(0)" : "=s"(t)::"memory");
  return (uint32_t)t;
}
__device__ __forceinline__ f32x4 mfma32(const u32x4& a, const u32x4& b, const f32x4& c) {
  return __builtin_amdgcn_mfma_f32_16x16x32_bf16(__builtin_bit_cast(bf16x8, a), __builtin_bit_cast(bf16x8, b), c, 0, 0, 0);
}
template <int H> __device__ __forceinline__ f32x4 smf(const u32x4& a, const u32x8& b, const f32x4& c, uint32_t ix) {
  return __builtin_amdgcn_smfmac_f32_16x16x64_bf16(__builtin_bit_cast(bf16x8, a), __builtin_bit_cast(bf16x16, b), c, ix, 0, H);
}
__device__ __forceinline__ u32x8 cat8(const u32x4& a, const u32x4& b) { return __builtin_shufflevector(a, b, 0, 1, 2, 3, 4, 5, 6, 7); }
__device__ __forceinline__ int qpos(int v) { return ((v >> 1) & 1) * 192 + (v >> 2) * 4 + (v & 1) * 2; }
__device__ __forceinline__ u32x4 cat(const u32x2& a, const u32x2& b) { return (u32x4){a.x, a.y, b.x, b.y}; }
__device__ __forceinline__ uint32_t umax4(const u32x4& r) { return max(max(r.x, r.y), max(r.z, r.w)); }
__device__ __forceinline__ int kpos(int k) { return (k & ~31) | ((k & 12) << 1) | ((k & 16) >> 2) | (k & 3); }

#define DUMP(i) do {} while (0)
#define STAMPW(i) do { const uint32_t t_ = rt32(); if ((int)__lane_id() == (i)) stv = t_; } while (0)
#define STAMPV(i) do {} while (0)
#define CSTAMP(on, i) do {} while (0)
#define ESTAMP(i) do {} while (0)
#define PIN1(a) asm volatile("" : "+v"(a))
#define PINS(a) asm volatile("" : "+s"(a))
#define PINS2(a, c) asm volatile("" : "+s"(a), "+s"(c))
#define SB() do { asm volatile("" ::: "memory"); __builtin_amdgcn_sched_barrier(0); } while (0)
#define VM0() __builtin_amdgcn_s_waitcnt(0x0f70)
#define BAR0() asm volatile("s_barrier" ::: "memory")
#define BARX(t) asm volatile("s_barrier ; " t ::: "memory")
#define LBAR(t) asm volatile("s_waitcnt lgkmcnt(0)\n\ts_barrier ; " t ::: "memory")
template <int V> using IC = std::integral_constant<int, V>;

#define TAIL_ASM \
  "s_mov_b32 s80, 0\n\ts_mov_b32 s84, -1\n\ts_getpc_b64 s[98:99]\n\t.LPC%=:\ns_add_u32 s98, s98, .LCLK%=-.LPC%=\n\ts_addc_u32 s99, s99, 0\n\t.LLOOP%=:\n" \
  "s_cmp_gt_u32 %[w], 4\n\ts_cbranch_scc1 .LH2X%=\n\tds_read_b64 v[240:241], %[t16] offset:%[tab]+8192\n\ts_mov_b32 s81, %[oG]\n\t" \
  "s_add_u32 s82, s81, %[osg]\n\ts_mov_b32 s83, %[eC]\n\tv_and_b32 v242, 16, %[t16]\n\tv_mov_b64 v[232:233], 0\n\tv_mov_b64 v[234:235], 0\n\t" \
  "v_mad_u32_u24 v242, v242, 12, %[bo]\n\ts_cmp_eq_u32 s80, 0\n\ts_cbranch_scc1 .LH2A%=\n\ts_waitcnt lgkmcnt(0)\n\t" \
  "ds_read_b64_tr_b16 v[204:205], v241\n\tds_read_b64_tr_b16 v[206:207], v241 offset:2560\n\tds_read_b64_tr_b16 v[208:209], v241 offset:5120\n\t" \
  "ds_read_b64_tr_b16 v[210:211], v241 offset:7680\n\tds_read_b64_tr_b16 v[212:213], v241 offset:10240\n\t" \
  "ds_read_b64_tr_b16 v[214:215], v241 offset:12800\n\tds_read_b64_tr_b16 v[216:217], v241 offset:15360\n\t" \
  "ds_read_b64_tr_b16 v[218:219], v241 offset:17920\n\tds_read_b64_tr_b16 v[220:221], v241 offset:20480\n\t" \
  "ds_read_b64_tr_b16 v[222:223], v241 offset:23040\n\tds_read_b64_tr_b16 v[224:225], v241 offset:25600\n\t" \
  "ds_read_b64_tr_b16 v[226:227], v241 offset:28160\n\t.LH2A%=:\ns_cmp_gt_u32 %[w], 2\n\ts_cbranch_scc1 .LH2M%=\n\ts_barrier\n\t" \
  "buffer_load_dwordx2 v[202:203], %[ss], %[rs], s82 offen sc1\n\tbuffer_load_dwordx4 v[194:197], %[s0], %[rs], s81 offen sc1\n\t" \
  "buffer_load_dwordx4 v[198:201], %[s0], %[rs], s81 offen offset:128 sc1\n\ts_waitcnt vmcnt(2) lgkmcnt(0)\n\t.LRED2%=:\n" \
  "v_cmp_ne_u32 s[90:91], 0, v202\n\ts_waitcnt vmcnt(0)\n\tv_permlane32_swap_b32 v194, v195\n\tv_permlane32_swap_b32 v196, v197\n\t" \
  "v_add_f32_dpp v247, v203, v203 quad_perm:[1,0,3,2] row_mask:0xf bank_mask:0xf\n\tv_permlane32_swap_b32 v198, v199\n\t" \
  "v_permlane32_swap_b32 v200, v201\n\tv_add_f32 v194, v194, v195\n\tv_add_f32 v196, v196, v197\n\t" \
  "v_add_f32_dpp v247, v247, v247 quad_perm:[2,3,0,1] row_mask:0xf bank_mask:0xf\n\tv_add_f32 v198, v198, v199\n\tv_add_f32 v200, v200, v201\n\t" \
  "v_permlane16_swap_b32 v194, v196\n\tv_add_f32_dpp v247, v247, v247 row_half_mirror row_mask:0xf bank_mask:0xf\n\tv_permlane16_swap_b32 v198, v200\n\t" \
  "v_add_f32 v194, v194, v196\n\tv_add_f32 v198, v198, v200\n\tv_rcp_f32 v247, v247\n\t" \
  "v_add_f32_dpp v246, v194, v194 row_ror:8 row_mask:0xf bank_mask:0x3\n\tv_add_f32_dpp v246, v198, v198 row_ror:8 row_mask:0xf bank_mask:0xc\n\t" \
  "v_mul_f32 v246, v246, v247\n\tv_cvt_pk_bf16_f32 v247, v246, v246\n\tds_write_b16 v240, v247\n\tv_cmp_u_f32 s[92:93], v246, v246\n\t" \
  "s_or_b64 s[90:91], s[90:91], s[92:93]\n\ts_cbranch_scc1 .LSL2%=\n\t.LH2B%=:\ns_cmp_eq_u32 s80, 0\n\ts_cbranch_scc1 .LWO0%=\n\ts_waitcnt lgkmcnt(0)\n\t" \
  "s_barrier\n\tds_read_b128 v[194:197], v242 offset:%[ov]\n\tds_read_b128 v[198:201], v242 offset:%[ov]+64\n\t" \
  "ds_read_b128 v[228:231], v242 offset:%[ov]+128\n\ts_waitcnt lgkmcnt(2)\n\tv_smfmac_f32_16x16x64_bf16 v[232:235], v[194:197], v[204:211], %[ix]\n\t" \
  "s_waitcnt lgkmcnt(1)\n\tv_smfmac_f32_16x16x64_bf16 v[232:235], v[198:201], v[212:219], %[ix]\n\ts_waitcnt lgkmcnt(0)\n\t" \
  "v_smfmac_f32_16x16x64_bf16 v[232:235], v[228:231], v[220:227], %[ix]\n\t.LWOE%=:\ns_nop 7\n\tv_add_f32 v232, v232, v233\n\t" \
  "buffer_store_dword v232, %[dt], %[rs], s83 offen sc1\n\tds_read_b64 v[240:241], %[t16] offset:%[tab]+8192+8\n\t" \
  "ds_read_b32 %[s0], %[t16] offset:%[tab]+16384+12\n\tds_read_b32 v242, %[t16] offset:%[tab]+32768\n\ts_mov_b32 s81, %[rC]\n\ts_mov_b32 s83, %[eH]\n\t" \
  "v_mov_b64 v[214:215], 0\n\ts_waitcnt lgkmcnt(2)\n\tds_read_b64_tr_b16 v[204:205], v241\n\tds_read_b64_tr_b16 v[206:207], v241 offset:1536\n\t" \
  "ds_read_b64_tr_b16 v[208:209], v241 offset:3072\n\tds_read_b64_tr_b16 v[210:211], v241 offset:4608\n\t" \
  "ds_read_b64_tr_b16 v[212:213], v241 offset:6144\n\ts_waitcnt lgkmcnt(0)\n\ts_barrier\n\t" \
  "buffer_load_dwordx4 v[194:197], %[t16], %[rs], s81 offen sc1\n\tbuffer_load_dwordx4 v[198:201], %[t16], %[rs], s81 offen offset:1024 sc1\n\t" \
  "s_waitcnt vmcnt(0)\n\t.LRED3%=:\nv_add_f32 v194, v194, v198\n\tv_add_f32 v195, v195, v199\n\tv_add_f32 v196, v196, v200\n\t" \
  "v_add_f32 v197, v197, v201\n\tv_permlane32_swap_b32 v194, v195\n\ts_nop 0\n\tv_permlane32_swap_b32 v196, v197\n\tv_add_f32 v194, v194, v195\n\t" \
  "v_add_f32 v196, v196, v197\n\ts_nop 1\n\tv_permlane16_swap_b32 v194, v196\n\tv_add_f32 v194, v194, v196\n\ts_nop 1\n\t" \
  "v_add_f32_dpp v194, v194, v194 row_ror:8 row_mask:0xf bank_mask:0xf\n\ts_nop 1\n\t" \
  "v_add_f32_dpp v246, v194, v194 row_ror:4 row_mask:0xf bank_mask:0xf\n\tv_cvt_pk_bf16_f32 v247, 0, v246\n\tv_add_f32 v247, v242, v247\n\t" \
  "v_cvt_pk_bf16_f32 v243, v247, 0\n\tds_write_b16 v240, v243\n\tv_cmp_u_f32 vcc, v246, v246\n\ts_cbranch_vccnz .LSL3%=\n\t.LH3B%=:\n" \
  "s_cmp_gt_u32 %[w], 2\n\ts_cbranch_scc1 .LH3M%=\n\ts_waitcnt lgkmcnt(0)\n\ts_barrier\n\tds_read_b128 v[194:197], %[bo] offset:%[xp]\n\t" \
  "ds_read_b128 v[198:201], %[bo] offset:%[xp]+64\n\tds_read_b128 v[228:231], %[bo] offset:%[xp]+128\n\ts_waitcnt lgkmcnt(2)\n\t" \
  "v_mfma_f32_16x16x32_bf16 v[232:235], v[194:197], v[204:207], 0\n\ts_waitcnt lgkmcnt(1)\n\t" \
  "v_mfma_f32_16x16x32_bf16 v[232:235], v[198:201], v[208:211], v[232:235]\n\ts_waitcnt lgkmcnt(0)\n\t" \
  "v_mfma_f32_16x16x32_bf16 v[232:235], v[228:231], v[212:215], v[232:235]\n\ts_nop 7\n\tbuffer_store_dword v232, %[dt], %[rs], s83 offen sc1\n\t" \
  ".LH3E%=:\nv_lshlrev_b32 v243, 16, v243\n\tds_write_b32 %[t16], v243 offset:%[tab]+32768\n\tds_read_b128 v[240:243], %[t16] offset:%[tab]+16384\n\t" \
  "ds_read_b32 v244, %[t16] offset:%[tab]+32768+8\n\ts_add_u32 s82, %[oG], %[osh]\n\ts_add_u32 s81, %[oG], %[oh]\n\ts_sub_u32 s85, %[dE], 0x28000\n\t" \
  "s_cmp_eq_u32 s80, 0\n\ts_cselect_b32 s85, s85, 0x8000\n\ts_add_u32 s83, %[eC], s85\n\tv_mov_b64 v[210:211], 0\n\tv_mov_b32 v245, 0xc0135761\n\t" \
  "s_waitcnt lgkmcnt(1)\n\tds_read_b64_tr_b16 v[204:205], v241\n\tds_read_b64_tr_b16 v[206:207], v241 offset:2560\n\t" \
  "ds_read_b64_tr_b16 v[208:209], v241 offset:5120\n\tds_read_b32 v242, v242\n\ts_cmp_gt_u32 %[w], 1\n\ts_cbranch_scc1 .LH4M%=\n\t" \
  "s_waitcnt lgkmcnt(0)\n\ts_barrier\n\tbuffer_load_dwordx2 v[202:203], %[ss], %[rs], s82 offen sc1\n\t" \
  "buffer_load_dwordx4 v[194:197], %[s0], %[rs], s81 offen sc1\n\ts_waitcnt vmcnt(1)\n\t.LRED4%=:\n" \
  "v_add_f32_dpp v198, v202, v202 quad_perm:[1,0,3,2] row_mask:0xf bank_mask:0xf\n\t" \
  "v_add_f32_dpp v199, v203, v203 quad_perm:[1,0,3,2] row_mask:0xf bank_mask:0xf\n\ts_waitcnt vmcnt(0)\n\tv_permlane32_swap_b32 v194, v195\n\t" \
  "v_add_f32_dpp v198, v198, v198 quad_perm:[2,3,0,1] row_mask:0xf bank_mask:0xf\n\t" \
  "v_add_f32_dpp v199, v199, v199 quad_perm:[2,3,0,1] row_mask:0xf bank_mask:0xf\n\tv_permlane32_swap_b32 v196, v197\n\tv_add_f32 v194, v194, v195\n\t" \
  "v_add_f32_dpp v198, v198, v198 row_half_mirror row_mask:0xf bank_mask:0xf\n\t" \
  "v_add_f32_dpp v199, v199, v199 row_half_mirror row_mask:0xf bank_mask:0xf\n\tv_add_f32 v196, v196, v197\n\tv_mul_f32 v199, 0x3acccccd, v199\n\t" \
  "v_mul_f32 v198, 0x3acccccd, v198\n\tv_permlane16_swap_b32 v194, v196\n\tv_fma_f32 v198, -v199, v199, v198\n\tv_add_f32 v194, v194, v196\n\t" \
  "v_cvt_pk_bf16_f32 v198, 0, v198\n\tv_cvt_pk_bf16_f32 v199, 0, v199\n\tv_add_f32 v198, |v198|, %[eps]\n\t" \
  "v_add_f32_dpp v194, v194, v194 row_ror:8 row_mask:0xf bank_mask:0xf\n\tv_cvt_pk_bf16_f32 v198, 0, v198\n\tv_sqrt_f32 v198, v198\n\t" \
  "v_fma_f32 v194, -v199, v242, v194\n\tv_cvt_pk_bf16_f32 v198, 0, v198\n\tv_rcp_f32 v198, v198\n\ts_nop 0\n\tv_mul_f32 v246, v194, v198\n\t" \
  "v_cvt_pk_bf16_f32 v247, 0, v246\n\tv_add_f32 v247, v244, v247\n\tv_cvt_pk_bf16_f32 v247, 0, v247\n\tv_mul_f32 v198, v247, v247\n\t" \
  "v_fmamk_f32 v198, v198, 0xbdd2d3e7, v245\n\tv_mul_f32 v198, v198, v247\n\tv_exp_f32 v198, v198\n\tv_cmp_u_f32 vcc, v246, v246\n\t" \
  "v_add_f32 v198, 1.0, v198\n\tv_rcp_f32 v198, v198\n\ts_nop 0\n\tv_mul_f32 v198, v198, v247\n\tv_cvt_pk_bf16_f32 v198, v198, v198\n\t" \
  "ds_write_b16 v240, v198\n\ts_cbranch_vccnz .LSL4%=\n\t.LH4B%=:\ns_waitcnt lgkmcnt(0)\n\ts_barrier\n\tds_read_b128 v[194:197], %[bo] offset:%[hv]\n\t" \
  "ds_read_b128 v[198:201], %[bo] offset:%[hv]+64\n\ts_waitcnt lgkmcnt(1)\n\tv_mfma_f32_16x16x32_bf16 v[232:235], v[194:197], v[204:207], 0\n\t" \
  "s_waitcnt lgkmcnt(0)\n\tv_mfma_f32_16x16x32_bf16 v[232:235], v[198:201], v[208:211], v[232:235]\n\ts_nop 7\n\t" \
  "buffer_store_dword v232, %[dt], %[rs], s83 offen sc1\n\t.LH4E%=:\ns_and_b32 s88, s80, %[far]\n\ts_cbranch_scc1 .LEND%=\n\t" \
  "ds_read_b64 v[240:241], %[t16] offset:%[tab]+24576+8\n\tds_read_b32 v242, %[t16] offset:%[tab]+8192+8\n\t" \
  "ds_read_b64 v[244:245], %[t16] offset:%[tab]+32768\n\ts_sub_u32 s85, %[dR], 0x28000\n\ts_cmp_eq_u32 s80, 0\n\ts_cselect_b32 s85, s85, 0x8000\n\t" \
  "s_add_u32 s81, %[rC], s85\n\ts_mov_b32 s83, %[eQ]\n\tv_mov_b64 v[214:215], 0\n\tv_mov_b64 v[226:227], 0\n\ts_waitcnt lgkmcnt(2)\n\t" \
  "ds_read_b64_tr_b16 v[204:205], v240\n\tds_read_b64_tr_b16 v[216:217], v240 offset:32\n\tds_read_b64_tr_b16 v[206:207], v240 offset:6400\n\t" \
  "ds_read_b64_tr_b16 v[218:219], v240 offset:6432\n\tds_read_b64_tr_b16 v[208:209], v240 offset:12800\n\t" \
  "ds_read_b64_tr_b16 v[220:221], v240 offset:12832\n\tds_read_b64_tr_b16 v[210:211], v240 offset:19200\n\t" \
  "ds_read_b64_tr_b16 v[222:223], v240 offset:19232\n\tds_read_b64_tr_b16 v[212:213], v240 offset:25600\n\t" \
  "ds_read_b64_tr_b16 v[224:225], v240 offset:25632\n\ts_cmp_gt_u32 %[w], 4\n\ts_cbranch_scc1 .LH5M%=\n\ts_waitcnt lgkmcnt(0)\n\ts_barrier\n\t" \
  "buffer_load_dwordx4 v[194:197], %[t16], %[rs], s81 offen sc1\n\tbuffer_load_dwordx4 v[198:201], %[t16], %[rs], s81 offen offset:1024 sc1\n\t" \
  "s_waitcnt vmcnt(0)\n\t.LRED5%=:\nv_add_f32 v194, v194, v198\n\tv_add_f32 v195, v195, v199\n\tv_add_f32 v196, v196, v200\n\t" \
  "v_add_f32 v197, v197, v201\n\tv_permlane32_swap_b32 v194, v195\n\ts_nop 0\n\tv_permlane32_swap_b32 v196, v197\n\tv_add_f32 v194, v194, v195\n\t" \
  "v_add_f32 v196, v196, v197\n\ts_nop 1\n\tv_permlane16_swap_b32 v194, v196\n\tv_add_f32 v194, v194, v196\n\ts_nop 1\n\t" \
  "v_add_f32_dpp v194, v194, v194 row_ror:8 row_mask:0xf bank_mask:0xf\n\ts_nop 1\n\t" \
  "v_add_f32_dpp v246, v194, v194 row_ror:4 row_mask:0xf bank_mask:0xf\n\tv_cvt_pk_bf16_f32 v247, 0, v246\n\tv_cmp_u_f32 vcc, v246, v246\n\t" \
  "v_add_f32 v247, v245, v247\n\tv_cvt_pk_bf16_f32 v247, 0, v247\n\tv_add_f32 v247, v244, v247\n\tv_cvt_pk_bf16_f32 v243, v247, 0\n\t" \
  "ds_write_b16 v242, v243\n\ts_cbranch_vccnz .LSL5%=\n\t.LH5B%=:\ns_cmp_lg_u32 s80, 0\n\ts_cbranch_scc1 .LFIN%=\n\t.LH5C%=:\ns_waitcnt lgkmcnt(0)\n\t" \
  "s_barrier\n\tds_read_b128 v[194:197], %[bo] offset:%[xp]\n\tds_read_b128 v[198:201], %[bo] offset:%[xp]+64\n\t" \
  "ds_read_b128 v[228:231], %[bo] offset:%[xp]+128\n\ts_waitcnt lgkmcnt(2)\n\tv_mfma_f32_16x16x32_bf16 v[232:235], v[194:197], v[204:207], 0\n\t" \
  "v_mfma_f32_16x16x32_bf16 v[236:239], v[194:197], v[216:219], 0\n\ts_waitcnt lgkmcnt(1)\n\t" \
  "v_mfma_f32_16x16x32_bf16 v[232:235], v[198:201], v[208:211], v[232:235]\n\tv_mfma_f32_16x16x32_bf16 v[236:239], v[198:201], v[220:223], v[236:239]\n\t" \
  "s_waitcnt lgkmcnt(0)\n\tv_mfma_f32_16x16x32_bf16 v[232:235], v[228:231], v[212:215], v[232:235]\n\t" \
  "v_mfma_f32_16x16x32_bf16 v[236:239], v[228:231], v[224:227], v[236:239]\n\ts_nop 6\n\tbuffer_store_dword v232, %[dt], %[rs], s83 offen sc1\n\t" \
  "buffer_store_dword v236, %[dt], %[rs], s83 offen offset:64 sc1\n\tv_lshlrev_b32 v243, 16, v243\n\tds_write_b32 %[t16], v243 offset:%[tab]+32768\n\t" \
  ".LH1%=:\nds_read_b64 v[204:205], %[t16] offset:%[tab]+24576\n\tds_read_b128 v[240:243], %[t16] offset:%[tab]\n\t" \
  "ds_read_b128 v[236:239], %[t16] offset:%[tab]+40960\n\ts_add_u32 %[oG], %[oG], %[dG]\n\ts_add_u32 %[eH], %[eH], %[dH]\n\t" \
  "s_add_u32 %[rC], %[rC], %[dR]\n\ts_add_u32 %[eC], %[eC], %[dE]\n\ts_sub_u32 s81, %[oG], 0x10000\n\ts_add_u32 s82, s81, %[osg]\n\t" \
  "s_mov_b32 s92, 0x00ff00ff\n\ts_mov_b32 s93, 0xff00ff00\n\tv_mov_b64 v[208:209], 0\n\tv_mov_b64 v[210:211], 0\n\tv_mov_b64 v[212:213], 0\n\t" \
  "v_mov_b64 v[214:215], 0\n\tv_mov_b64 v[216:217], 0\n\tv_mov_b64 v[218:219], 0\n\tv_mov_b64 v[220:221], 0\n\tv_mov_b64 v[222:223], 0\n\t" \
  "v_mov_b64 v[224:225], 0\n\tv_mov_b64 v[226:227], 0\n\tv_mov_b64 v[228:229], 0\n\tv_mov_b64 v[230:231], 0\n\tv_mov_b64 v[232:233], 0\n\t" \
  "v_mov_b64 v[234:235], 0\n\ts_waitcnt lgkmcnt(2)\n\tds_read_b32 v205, v205\n\ts_cmp_gt_u32 %[w], 2\n\ts_cbranch_scc1 .LH1X%=\n\t" \
  "s_waitcnt lgkmcnt(0)\n\ts_barrier\n\tbuffer_load_dwordx2 v[202:203], %[ss], %[rs], s82 offen sc1\n\t" \
  "buffer_load_dwordx4 v[194:197], v243, %[rs], s81 offen sc1\n\tbuffer_load_dwordx4 v[198:201], v243, %[rs], s81 offen offset:128 sc1\n\t" \
  "s_waitcnt vmcnt(0)\n\t.LRED1%=:\nv_permlane32_swap_b32 v194, v195\n\tv_permlane32_swap_b32 v196, v197\n\t" \
  "v_add_f32_dpp v206, v203, v203 quad_perm:[1,0,3,2] row_mask:0xf bank_mask:0xf\n\tv_permlane32_swap_b32 v198, v199\n\t" \
  "v_permlane32_swap_b32 v200, v201\n\tv_add_f32 v194, v194, v195\n\tv_add_f32 v196, v196, v197\n\t" \
  "v_add_f32_dpp v206, v206, v206 quad_perm:[2,3,0,1] row_mask:0xf bank_mask:0xf\n\tv_add_f32 v198, v198, v199\n\tv_add_f32 v200, v200, v201\n\t" \
  "v_permlane16_swap_b32 v194, v196\n\tv_add_f32_dpp v206, v206, v206 row_half_mirror row_mask:0xf bank_mask:0xf\n\tv_permlane16_swap_b32 v198, v200\n\t" \
  "v_add_f32 v194, v194, v196\n\tv_add_f32 v198, v198, v200\n\tv_mul_f32 v206, 0x3acccccd, v206\n\t" \
  "v_add_f32_dpp v246, v194, v194 row_ror:8 row_mask:0xf bank_mask:0x3\n\tv_add_f32_dpp v246, v198, v198 row_ror:8 row_mask:0xf bank_mask:0xc\n\t" \
  "v_cvt_pk_bf16_f32 v247, 0, v206\n\tv_fma_f32 v246, -v247, v205, v246\n\tv_cvt_pk_bf16_f32 v247, v246, v246\n\tds_write_b16 v204, v247\n\t" \
  "v_cmp_u_f32 vcc, v246, v246\n\ts_cbranch_vccnz .LSL1%=\n\t.LH1B%=:\ns_waitcnt lgkmcnt(0)\n\ts_barrier\n\tds_read_b128 v[194:197], v236\n\t" \
  "ds_read_b128 v[198:201], v236 offset:64\n\tds_read_b128 v[204:207], v236 offset:128\n\tds_read_b32 v202, v239 offset:%[qsw]\n\t" \
  "s_waitcnt lgkmcnt(3)\n\tv_smfmac_f32_16x16x64_bf16 v[208:211], v[194:197], v[2:9], %[ix]\n\t" \
  "v_smfmac_f32_16x16x64_bf16 v[212:215], v[194:197], v[26:33], %[ix]\n\tv_smfmac_f32_16x16x64_bf16 v[216:219], v[194:197], v[50:57], %[ix]\n\t" \
  "v_smfmac_f32_16x16x64_bf16 v[220:223], v[194:197], v[74:81], %[ix]\n\ts_waitcnt lgkmcnt(2)\n\t" \
  "v_smfmac_f32_16x16x64_bf16 v[208:211], v[198:201], v[10:17], %[ix]\n\tv_smfmac_f32_16x16x64_bf16 v[212:215], v[198:201], v[34:41], %[ix]\n\t" \
  "v_smfmac_f32_16x16x64_bf16 v[216:219], v[198:201], v[58:65], %[ix]\n\tv_smfmac_f32_16x16x64_bf16 v[220:223], v[198:201], v[82:89], %[ix]\n\t" \
  "s_waitcnt lgkmcnt(1)\n\tv_smfmac_f32_16x16x64_bf16 v[208:211], v[204:207], v[18:25], %[ix]\n\t" \
  "v_smfmac_f32_16x16x64_bf16 v[212:215], v[204:207], v[42:49], %[ix]\n\tv_smfmac_f32_16x16x64_bf16 v[216:219], v[204:207], v[66:73], %[ix]\n\t" \
  "v_smfmac_f32_16x16x64_bf16 v[220:223], v[204:207], v[90:97], %[ix]\n\ts_setprio 1\n\ts_nop 3\n\tv_add_f32 v246, v208, v209\n\t" \
  "v_add_f32 v203, v212, v213\n\tv_cndmask_b32 v246, v203, v246, s[92:93]\n\tv_add_f32 v247, v216, v217\n\tv_add_f32 v203, v220, v221\n\t" \
  "v_cndmask_b32 v247, v203, v247, s[92:93]\n\ts_waitcnt lgkmcnt(0)\n\ts_nop 0\n\tv_permlane32_swap_b32 v246, v247\n\ts_nop 1\n\t" \
  "v_add_f32_dpp v246, v247, v246 row_ror:8 row_mask:0xf bank_mask:0xf\n\tv_mul_f32 v246, v246, v202\n\tv_cvt_pk_bf16_f32 v246, 0, v246\n\t" \
  "v_mul_f32 v246, 0x3d93cd3a, v246\n\tv_cvt_pk_bf16_f32 v246, 0, v246\n\tv_mul_f32 v246, 0x3fb8aa3b, v246\n\tv_exp_f32 v203, v246\n\t" \
  "v_cmp_nle_f32 vcc, |v246|, %[small]\n\tv_cvt_pk_bf16_f32 v244, v203, 0\n\tv_add_f32_dpp v203, v203, v203 row_ror:1 row_mask:0xf bank_mask:0xf\n\t" \
  "s_nop 0\n\tv_mov_b32_dpp v245, v244 row_shl:1 row_mask:0xf bank_mask:0xf\n\ts_cbranch_vccnz .LSLM%=\n\t.LPV%=:\ns_setprio 0\n\t" \
  "v_mov_b64_dpp v[208:209], v[244:245] row_newbcast:0 row_mask:0xf bank_mask:0xf\n\t" \
  "v_mov_b64_dpp v[210:211], v[244:245] row_newbcast:2 row_mask:0xf bank_mask:0xf\n\t" \
  "v_add_f32_dpp v203, v203, v203 row_ror:2 row_mask:0xf bank_mask:0xf\n\t" \
  "v_mov_b64_dpp v[212:213], v[244:245] row_newbcast:4 row_mask:0xf bank_mask:0xf\n\t" \
  "v_smfmac_f32_16x16x64_bf16 v[224:227], v[208:211], v[98:105], %[ix] abid:1\n\t" \
  "v_mov_b64_dpp v[214:215], v[244:245] row_newbcast:6 row_mask:0xf bank_mask:0xf\n\t" \
  "v_smfmac_f32_16x16x64_bf16 v[228:231], v[208:211], v[106:113], %[ix] abid:1\n\tv_add_f32_dpp v203, v203, v203 row_ror:4 row_mask:0xf bank_mask:0xf\n\t" \
  "v_smfmac_f32_16x16x64_bf16 v[232:235], v[208:211], v[114:121], %[ix] abid:1\n\t" \
  "v_smfmac_f32_16x16x64_bf16 v[224:227], v[212:215], v[122:129], %[ix] abid:1\n\t" \
  "v_mov_b64_dpp v[208:209], v[244:245] row_newbcast:8 row_mask:0xf bank_mask:0xf\n\t" \
  "v_mov_b64_dpp v[210:211], v[244:245] row_newbcast:10 row_mask:0xf bank_mask:0xf\n\t" \
  "v_smfmac_f32_16x16x64_bf16 v[228:231], v[212:215], v[130:137], %[ix] abid:1\n\tv_add_f32_dpp v203, v203, v203 row_ror:8 row_mask:0xf bank_mask:0xf\n\t" \
  "v_smfmac_f32_16x16x64_bf16 v[232:235], v[212:215], v[138:145], %[ix] abid:1\n\t" \
  "v_smfmac_f32_16x16x64_bf16 v[224:227], v[208:211], v[146:153], %[ix] abid:1\n\t" \
  "v_mov_b64_dpp v[212:213], v[244:245] row_newbcast:12 row_mask:0xf bank_mask:0xf\n\t" \
  "v_mov_b64_dpp v[214:215], v[244:245] row_newbcast:14 row_mask:0xf bank_mask:0xf\n\t" \
  "v_smfmac_f32_16x16x64_bf16 v[228:231], v[208:211], v[154:161], %[ix] abid:1\n\t" \
  "v_add_f32_dpp v203, v203, v203 row_bcast:15 row_mask:0xa bank_mask:0xf\n\t" \
  "v_smfmac_f32_16x16x64_bf16 v[232:235], v[208:211], v[162:169], %[ix] abid:1\n\t" \
  "v_smfmac_f32_16x16x64_bf16 v[224:227], v[212:215], v[170:177], %[ix] abid:1\n\t" \
  "v_smfmac_f32_16x16x64_bf16 v[228:231], v[212:215], v[178:185], %[ix] abid:1\n\t" \
  "v_smfmac_f32_16x16x64_bf16 v[232:235], v[212:215], v[186:193], %[ix] abid:1\n\t" \
  "v_add_f32_dpp v203, v203, v203 row_bcast:31 row_mask:0xc bank_mask:0xf\n\ts_lshl_b64 exec, 1, 63\n\tds_write2_b32 v238, v239, v203 offset1:1\n\t" \
  "s_mov_b64 exec, -1\n\ts_mov_b32 s80, 1\n\ts_mov_b32 s83, %[oD]\n\ts_nop 0\n\tds_write2st64_b32 v237, v224, v228 offset1:1\n\t" \
  "ds_write_b32 v237, v232 offset:512\n\ts_waitcnt lgkmcnt(0)\n\ts_barrier\n\tds_read_b128 v[194:197], v241\n\tds_read_b32 v204, v240\n\t" \
  "v_mov_b32 %[s0], v243\n\ts_waitcnt lgkmcnt(1)\n\t.LCMB%=:\nv_add_f32_dpp v205, v194, v194 row_ror:8 row_mask:0xf bank_mask:0x3\n\t" \
  "v_add_f32_dpp v206, v195, v195 row_ror:8 row_mask:0xf bank_mask:0x3\n\tv_add_f32_dpp v205, v196, v196 row_ror:8 row_mask:0xf bank_mask:0xc\n\t" \
  "v_add_f32_dpp v206, v197, v197 row_ror:8 row_mask:0xf bank_mask:0xc\n\ts_waitcnt lgkmcnt(0)\n\tv_cmp_ne_u32 vcc, 0, v204\n\t" \
  "v_add_f32_dpp v246, v205, v205 row_ror:12 row_mask:0xf bank_mask:0x5\n\tv_add_f32_dpp v246, v206, v206 row_ror:4 row_mask:0xf bank_mask:0xa\n\t" \
  "s_nop 1\n\tv_add_f32_dpp v246, v246, v246 quad_perm:[1,0,3,2] row_mask:0xf bank_mask:0xf\n\ts_cbranch_vccnz .LSLC%=\n\t.LCMS%=:\n" \
  "buffer_store_dword v246, v242, %[rs], s83 offen sc1\n\ts_branch .LLOOP%=\n\t.LWO0%=:\ns_waitcnt lgkmcnt(0)\n\ts_barrier\n\t" \
  "ds_read_b128 v[194:197], v242 offset:%[ov]+0\n\tds_read_b64_tr_b16 v[204:205], v241\n\tds_read_b64_tr_b16 v[206:207], v241 offset:2560\n\t" \
  "ds_read_b64_tr_b16 v[208:209], v241 offset:5120\n\tds_read_b64_tr_b16 v[210:211], v241 offset:7680\n\t" \
  "ds_read_b128 v[198:201], v242 offset:%[ov]+64\n\tds_read_b64_tr_b16 v[212:213], v241 offset:10240\n\t" \
  "ds_read_b64_tr_b16 v[214:215], v241 offset:12800\n\tds_read_b64_tr_b16 v[216:217], v241 offset:15360\n\t" \
  "ds_read_b64_tr_b16 v[218:219], v241 offset:17920\n\tds_read_b128 v[228:231], v242 offset:%[ov]+128\n\t" \
  "ds_read_b64_tr_b16 v[220:221], v241 offset:20480\n\tds_read_b64_tr_b16 v[222:223], v241 offset:23040\n\t" \
  "ds_read_b64_tr_b16 v[224:225], v241 offset:25600\n\tds_read_b64_tr_b16 v[226:227], v241 offset:28160\n\ts_waitcnt lgkmcnt(10)\n\t" \
  "v_smfmac_f32_16x16x64_bf16 v[232:235], v[194:197], v[204:211], %[ix]\n\ts_waitcnt lgkmcnt(5)\n\t" \
  "v_smfmac_f32_16x16x64_bf16 v[232:235], v[198:201], v[212:219], %[ix]\n\ts_waitcnt lgkmcnt(0)\n\t" \
  "v_smfmac_f32_16x16x64_bf16 v[232:235], v[228:231], v[220:227], %[ix]\n\ts_branch .LWOE%=\n\t.LH1X%=:\ns_cmp_eq_u32 %[w], 3\n\t" \
  "s_cbranch_scc1 .LH1S%=\n\ts_cmp_lg_u32 %[w], 7\n\ts_cbranch_scc1 .LH1Y%=\n\ts_mov_b32 s88, %[p1]\n\ts_swappc_b64 s[96:97], s[98:99]\n\t.LH1Y%=:\n" \
  "s_barrier\n\ts_branch .LH1B%=\n\t.LH1S%=:\ns_waitcnt lgkmcnt(0)\n\ts_barrier\n\tbuffer_load_dwordx2 v[202:203], %[ss], %[rs], s82 offen sc1\n\t" \
  "s_waitcnt vmcnt(0)\n\t.LREDS%=:\nv_add_f32_dpp v194, v202, v202 quad_perm:[1,0,3,2] row_mask:0xf bank_mask:0xf\n\t" \
  "v_add_f32_dpp v195, v203, v203 quad_perm:[1,0,3,2] row_mask:0xf bank_mask:0xf\n\ts_nop 0\n\t" \
  "v_add_f32_dpp v194, v194, v194 quad_perm:[2,3,0,1] row_mask:0xf bank_mask:0xf\n\t" \
  "v_add_f32_dpp v195, v195, v195 quad_perm:[2,3,0,1] row_mask:0xf bank_mask:0xf\n\ts_nop 0\n\t" \
  "v_add_f32_dpp v194, v194, v194 row_half_mirror row_mask:0xf bank_mask:0xf\n\t" \
  "v_add_f32_dpp v195, v195, v195 row_half_mirror row_mask:0xf bank_mask:0xf\n\tv_mul_f32 v194, 0x3acccccd, v194\n\tv_mul_f32 v195, 0x3acccccd, v195\n\t" \
  "v_fma_f32 v194, -v195, v195, v194\n\tv_cvt_pk_bf16_f32 v194, 0, v194\n\tv_add_f32 v194, |v194|, %[eps]\n\tv_cvt_pk_bf16_f32 v194, 0, v194\n\t" \
  "v_sqrt_f32 v194, v194\n\ts_nop 0\n\tv_cvt_pk_bf16_f32 v194, 0, v194\n\tv_rcp_f32 v194, v194\n\ts_nop 0\n\tds_write_b32 v239, v194 offset:%[qsw]\n\t" \
  "v_cmp_u_f32 vcc, v194, v194\n\ts_cbranch_vccz .LH1B%=\n\ts_branch .LSLS%=\n\t.LH2M%=:\ns_barrier\n\ts_waitcnt vmcnt(0)\n\ts_branch .LH2B%=\n\t" \
  ".LH3M%=:\ns_waitcnt lgkmcnt(0)\n\ts_barrier\n\ts_branch .LH3E%=\n\t.LH4M%=:\ns_barrier\n\ts_branch .LH4B%=\n\t.LH5M%=:\ns_barrier\n\t" \
  "s_cmp_eq_u32 s80, 0\n\ts_cbranch_scc1 .LH5C%=\n\ts_branch .LFE%=\n\t.LH2X%=:\ns_cmp_eq_u32 %[w], 7\n\ts_cbranch_scc1 .LTW%=\n\t" \
  "ds_read_b64 v[240:241], %[t16] offset:%[tab]+16384\n\tv_mov_b64 v[198:199], -1\n\tv_mov_b64 v[200:201], -1\n\ts_cmp_lg_u32 s80, 0\n\t" \
  "s_waitcnt lgkmcnt(0)\n\ts_barrier\n\ts_cbranch_scc1 .LH2Y%=\n\tbuffer_store_dwordx4 v[198:201], v240, %[rs], 0 offen sc1\n\t.LH2Y%=:\n" \
  "s_waitcnt vmcnt(0)\n\ts_barrier\n\ts_mov_b32 s83, %[eH]\n\ts_cmp_eq_u32 %[w], 6\n\ts_cbranch_scc1 .LSW3%=\n\ts_movk_i32 s88, 0x2000\n\t" \
  "s_cmp_lg_u32 s80, 0\n\ts_barrier\n\ts_barrier\n\ts_cbranch_scc1 .LH3Y%=\n\tbuffer_store_dwordx4 v[198:201], v241, %[rs], 0 offen sc1\n\t" \
  "buffer_store_dwordx4 v[198:201], v241, %[rs], s88 offen sc1\n\t.LH3Y%=:\ns_barrier\n\ts_waitcnt vmcnt(0)\n\ts_barrier\n\ts_branch .LH4E%=\n\t" \
  ".LSW3%=:\ns_barrier\n\ts_barrier\n\tds_read_b128 v[194:197], %[bo] offset:%[xp]\n\tds_read_b128 v[204:207], %[ss]\n\t" \
  "ds_read_b128 v[198:201], %[bo] offset:%[xp]+64\n\tds_read_b128 v[208:211], %[ss] offset:64\n\tds_read_b128 v[228:231], %[bo] offset:%[xp]+128\n\t" \
  "ds_read_b128 v[212:215], %[ss] offset:128\n\ts_waitcnt lgkmcnt(4)\n\tv_mfma_f32_16x16x32_bf16 v[232:235], v[194:197], v[204:207], 0\n\t" \
  "s_waitcnt lgkmcnt(2)\n\tv_mfma_f32_16x16x32_bf16 v[232:235], v[198:201], v[208:211], v[232:235]\n\ts_waitcnt lgkmcnt(0)\n\t" \
  "v_mfma_f32_16x16x32_bf16 v[232:235], v[228:231], v[212:215], v[232:235]\n\ts_nop 7\n\tbuffer_store_dword v232, %[dt], %[rs], s83 offen sc1\n\t" \
  "s_barrier\n\ts_barrier\n\ts_and_b32 s88, s80, %[far]\n\ts_cbranch_scc1 .LEND%=\n\ts_mov_b32 s83, %[eQ]\n\ts_barrier\n\ts_cmp_lg_u32 s80, 0\n\t" \
  "s_cbranch_scc1 .LFE%=\n\ts_barrier\n\tds_read_b128 v[194:197], %[bo] offset:%[xp]\n\tds_read_b128 v[204:207], %[ss]\n\t" \
  "ds_read_b128 v[198:201], %[bo] offset:%[xp]+64\n\tds_read_b128 v[208:211], %[ss] offset:64\n\tds_read_b128 v[228:231], %[bo] offset:%[xp]+128\n\t" \
  "ds_read_b128 v[212:215], %[ss] offset:128\n\ts_waitcnt lgkmcnt(4)\n\tv_mfma_f32_16x16x32_bf16 v[232:235], v[194:197], v[204:207], 0\n\t" \
  "s_waitcnt lgkmcnt(2)\n\tv_mfma_f32_16x16x32_bf16 v[232:235], v[198:201], v[208:211], v[232:235]\n\ts_waitcnt lgkmcnt(0)\n\t" \
  "v_mfma_f32_16x16x32_bf16 v[232:235], v[228:231], v[212:215], v[232:235]\n\ts_nop 7\n\tbuffer_store_dword v232, %[dt], %[rs], s83 offen sc1\n\t" \
  "s_branch .LH1%=\n\t.LTW%=:\ns_cmp_lg_u32 s80, 0\n\ts_cbranch_scc1 .LTW1%=\n\ts_memrealtime s[94:95]\n\tv_and_b32 v240, 0x3e0, %[t16]\n\t" \
  "v_lshlrev_b32 v241, 9, %[t16]\n\tv_and_b32 v241, 0x2000, v241\n\tv_cmp_eq_u32 vcc, 0, v240\n\tv_mov_b32 v240, %[oor]\n\t" \
  "v_cndmask_b32 v240, v240, v241, vcc\n\tv_mov_b32 v243, %[tag]\n\ts_waitcnt lgkmcnt(0)\n\ts_mov_b32 %[tc], s94\n\tv_mov_b32 v242, s94\n\t" \
  "buffer_store_dwordx2 v[242:243], v240, %[rs], %[cw] offen sc1\n\ts_waitcnt vmcnt(4)\n\ts_mov_b32 s87, %[pw]\n\t.LW1%=:\ns_sub_u32 s87, s87, 1\n\t" \
  "s_cbranch_scc0 .LW1%=\n\ts_barrier\n\ts_waitcnt vmcnt(1)\n\ts_barrier\n\ts_mov_b32 s87, %[spin]\n\t.LTWP%=:\n" \
  "buffer_load_dwordx4 v[194:197], %[t16], %[rs], %[rK] offen sc1\n\tbuffer_load_dwordx4 v[198:201], %[t16], %[rs], %[rK] offen offset:1024 sc1\n\t" \
  "s_waitcnt vmcnt(0)\n\tv_xor_b32 v247, v195, v243\n\tv_xor_b32 v246, v197, v243\n\tv_xor_b32 v199, v199, v243\n\tv_xor_b32 v201, v201, v243\n\t" \
  "v_or3_b32 v247, v247, v246, v199\n\tv_or_b32 v247, v247, v201\n\tv_cmp_ne_u32 vcc, 0, v247\n\ts_cbranch_vccz .LTWQ%=\n\ts_sleep 3\n\t" \
  "s_sub_u32 s87, s87, 1\n\ts_cbranch_scc0 .LTWP%=\n\t.LTWQ%=:\nv_subrev_u32 v194, %[tc], v194\n\tv_subrev_u32 v196, %[tc], v196\n\t" \
  "v_subrev_u32 v198, %[tc], v198\n\tv_subrev_u32 v200, %[tc], v200\n\tv_max_i32 v247, v194, v196\n\tv_max3_i32 v247, v198, v200, v247\n\ts_nop 1\n\t" \
  "v_max_i32_dpp v247, v247, v247 row_ror:1 row_mask:0xf bank_mask:0xf\n\ts_nop 1\n\t" \
  "v_max_i32_dpp v247, v247, v247 row_ror:2 row_mask:0xf bank_mask:0xf\n\ts_nop 1\n\t" \
  "v_max_i32_dpp v247, v247, v247 row_ror:4 row_mask:0xf bank_mask:0xf\n\ts_nop 1\n\t" \
  "v_max_i32_dpp v247, v247, v247 row_ror:8 row_mask:0xf bank_mask:0xf\n\ts_nop 1\n\t" \
  "v_max_i32_dpp v247, v247, v247 row_bcast:15 row_mask:0xa bank_mask:0xf\n\ts_nop 1\n\t" \
  "v_max_i32_dpp v247, v247, v247 row_bcast:31 row_mask:0xc bank_mask:0xf\n\ts_nop 1\n\tv_readlane_b32 s88, v247, 63\n\t" \
  "s_add_u32 %[tc], %[tc], %[p3e]\n\ts_add_u32 %[tc], %[tc], s88\n\ts_mov_b32 %[te], %[tc]\n\ts_add_u32 s94, s98, .LUNT%=-.LCLK%=\n\t" \
  "s_addc_u32 s95, s99, 0\n\ts_swappc_b64 s[96:97], s[94:95]\n\ts_branch .LTW3%=\n\t.LTW1%=:\ns_add_u32 %[tc], %[tc], %[p2]\n\ts_mov_b32 s87, %[pw]\n\t" \
  ".LW2%=:\ns_sub_u32 s87, s87, 1\n\ts_cbranch_scc0 .LW2%=\n\ts_barrier\n\ts_barrier\n\ts_mov_b32 s88, %[p3]\n\ts_swappc_b64 s[96:97], s[98:99]\n\t" \
  ".LTW3%=:\ns_barrier\n\ts_barrier\n\ts_mov_b32 s88, %[p4]\n\ts_swappc_b64 s[96:97], s[98:99]\n\ts_barrier\n\ts_barrier\n\t" \
  "s_and_b32 s88, s80, %[far]\n\ts_cbranch_scc1 .LEND%=\n\ts_mov_b32 s88, %[p5]\n\ts_swappc_b64 s[96:97], s[98:99]\n\ts_barrier\n\t" \
  "s_cmp_lg_u32 s80, 0\n\ts_cbranch_scc1 .LFE%=\n\ts_barrier\n\ts_branch .LH1%=\n\t.LCLK%=:\ns_memrealtime s[94:95]\n\ts_add_u32 %[tc], %[tc], s88\n\t" \
  "s_movk_i32 s87, 64\n\ts_waitcnt lgkmcnt(0)\n\t.LCL1%=:\ns_sub_i32 s88, %[tc], s94\n\ts_cmp_ge_i32 s88, %[rmin]\n\ts_cbranch_scc1 .LUNT%=\n\t" \
  "s_add_u32 %[tc], %[tc], %[rcons]\n\ts_sub_u32 s87, s87, 1\n\ts_cbranch_scc0 .LCL1%=\n\t.LUNT%=:\ns_movk_i32 s87, 0x1000\n\t.LUN1%=:\n" \
  "s_memrealtime s[94:95]\n\ts_sub_u32 s87, s87, 1\n\ts_cbranch_scc1 .LUN2%=\n\ts_waitcnt lgkmcnt(0)\n\ts_sub_i32 s88, %[tc], s94\n\t" \
  "s_cmp_gt_i32 s88, 3\n\ts_cbranch_scc1 .LUN1%=\n\ts_cmp_lt_i32 s88, 1\n\ts_cbranch_scc1 .LUN2%=\n\ts_nop 3\n\ts_cmp_lt_i32 s88, 2\n\t" \
  "s_cbranch_scc1 .LUN2%=\n\ts_nop 3\n\ts_cmp_lt_i32 s88, 3\n\ts_cbranch_scc1 .LUN2%=\n\ts_nop 9\n\t.LUN2%=:\ns_waitcnt lgkmcnt(0)\n\t" \
  "s_setpc_b64 s[96:97]\n\t.LSL2%=:\ns_add_u32 s88, s80, 2\n\ts_cmp_eq_u32 s84, s88\n\ts_cbranch_scc1 .LH2B%=\n\ts_mov_b32 s84, s88\n\t" \
  "s_mov_b32 s87, %[spin]\n\t.LSL2a%=:\nbuffer_load_dwordx2 v[202:203], %[ss], %[rs], s82 offen sc1\n\t" \
  "buffer_load_dwordx4 v[194:197], %[s0], %[rs], s81 offen sc1\n\tbuffer_load_dwordx4 v[198:201], %[s0], %[rs], s81 offen offset:128 sc1\n\t" \
  "s_waitcnt vmcnt(0)\n\tv_max3_u32 v247, v194, v195, v196\n\tv_max3_u32 v247, v247, v197, v198\n\tv_max3_u32 v247, v247, v199, v200\n\t" \
  "v_max3_u32 v247, v247, v201, v202\n\tv_max_u32 v247, v247, v203\n\tv_cmp_eq_u32 vcc, -1, v247\n\ts_cbranch_vccz .LSL2b%=\n\ts_sub_u32 s87, s87, 1\n\t" \
  "s_cbranch_scc0 .LSL2a%=\n\t.LSL2b%=:\nv_cmp_ne_u32 vcc, 0, v202\n\ts_cbranch_vccz .LRED2%=\n\tv_lshrrev_b32 v247, 5, %[t16]\n\t" \
  "v_and_b32 v247, 28, v247\n\tds_bpermute_b32 v247, v247, v202\n\tv_max_f32_dpp v246, v202, v202 quad_perm:[1,0,3,2] row_mask:0xf bank_mask:0xf\n\t" \
  "s_nop 1\n\tv_max_f32_dpp v246, v246, v246 quad_perm:[2,3,0,1] row_mask:0xf bank_mask:0xf\n\ts_nop 1\n\t" \
  "v_max_f32_dpp v246, v246, v246 row_half_mirror row_mask:0xf bank_mask:0xf\n\ts_waitcnt lgkmcnt(0)\n\tv_sub_f32 v247, v247, v246\n\t" \
  "v_sub_f32 v202, v202, v246\n\tv_exp_f32 v247, v247\n\tv_exp_f32 v202, v202\n\ts_nop 0\n\tv_mul_f32 v194, v247, v194\n\tv_mul_f32 v195, v247, v195\n\t" \
  "v_mul_f32 v196, v247, v196\n\tv_mul_f32 v197, v247, v197\n\tv_mul_f32 v198, v247, v198\n\tv_mul_f32 v199, v247, v199\n\tv_mul_f32 v200, v247, v200\n\t" \
  "v_mul_f32 v201, v247, v201\n\tv_mul_f32 v203, v202, v203\n\tv_mov_b32 v202, 0\n\ts_nop 1\n\ts_branch .LRED2%=\n\t.LSL3%=:\ns_add_u32 s88, s80, 4\n\t" \
  "s_cmp_eq_u32 s84, s88\n\ts_cbranch_scc1 .LH3B%=\n\ts_mov_b32 s84, s88\n\ts_mov_b32 s87, %[spin]\n\t.LSL3a%=:\n" \
  "buffer_load_dwordx4 v[194:197], %[t16], %[rs], s81 offen sc1\n\tbuffer_load_dwordx4 v[198:201], %[t16], %[rs], s81 offen offset:1024 sc1\n\t" \
  "s_waitcnt vmcnt(0)\n\tv_max3_u32 v247, v194, v195, v196\n\tv_max3_u32 v247, v247, v197, v198\n\tv_max3_u32 v247, v247, v199, v200\n\t" \
  "v_max_u32 v247, v247, v201\n\tv_cmp_eq_u32 vcc, -1, v247\n\ts_cbranch_vccz .LSL3b%=\n\ts_sleep 1\n\ts_sub_u32 s87, s87, 1\n\t" \
  "s_cbranch_scc0 .LSL3a%=\n\t.LSL3b%=:\ns_branch .LRED3%=\n\t.LSL4%=:\ns_add_u32 s88, s80, 6\n\ts_cmp_eq_u32 s84, s88\n\ts_cbranch_scc1 .LH4B%=\n\t" \
  "s_mov_b32 s84, s88\n\ts_mov_b32 s87, %[spin]\n\t.LSL4a%=:\nbuffer_load_dwordx2 v[202:203], %[ss], %[rs], s82 offen sc1\n\t" \
  "buffer_load_dwordx4 v[194:197], %[s0], %[rs], s81 offen sc1\n\ts_waitcnt vmcnt(0)\n\tv_max3_u32 v247, v194, v195, v196\n\t" \
  "v_max3_u32 v247, v247, v197, v202\n\tv_max_u32 v247, v247, v203\n\tv_cmp_eq_u32 vcc, -1, v247\n\ts_cbranch_vccz .LSL4b%=\n\ts_sleep 1\n\t" \
  "s_sub_u32 s87, s87, 1\n\ts_cbranch_scc0 .LSL4a%=\n\t.LSL4b%=:\ns_branch .LRED4%=\n\t.LSL5%=:\ns_add_u32 s88, s80, 8\n\ts_cmp_eq_u32 s84, s88\n\t" \
  "s_cbranch_scc1 .LH5B%=\n\ts_mov_b32 s84, s88\n\ts_mov_b32 s87, %[spin]\n\t.LSL5a%=:\nbuffer_load_dwordx4 v[194:197], %[t16], %[rs], s81 offen sc1\n\t" \
  "buffer_load_dwordx4 v[198:201], %[t16], %[rs], s81 offen offset:1024 sc1\n\ts_waitcnt vmcnt(0)\n\tv_max3_u32 v247, v194, v195, v196\n\t" \
  "v_max3_u32 v247, v247, v197, v198\n\tv_max3_u32 v247, v247, v199, v200\n\tv_max_u32 v247, v247, v201\n\tv_cmp_eq_u32 vcc, -1, v247\n\t" \
  "s_cbranch_vccz .LSL5b%=\n\ts_sleep 1\n\ts_sub_u32 s87, s87, 1\n\ts_cbranch_scc0 .LSL5a%=\n\t.LSL5b%=:\ns_branch .LRED5%=\n\t.LSL1%=:\n" \
  "s_add_u32 s88, s80, 10\n\ts_cmp_eq_u32 s84, s88\n\ts_cbranch_scc1 .LH1B%=\n\ts_mov_b32 s84, s88\n\ts_mov_b32 s87, %[spin]\n\t.LSL1a%=:\n" \
  "buffer_load_dwordx2 v[202:203], %[ss], %[rs], s82 offen sc1\n\tbuffer_load_dwordx4 v[194:197], v243, %[rs], s81 offen sc1\n\t" \
  "buffer_load_dwordx4 v[198:201], v243, %[rs], s81 offen offset:128 sc1\n\ts_waitcnt vmcnt(0)\n\tv_max3_u32 v247, v194, v195, v196\n\t" \
  "v_max3_u32 v247, v247, v197, v198\n\tv_max3_u32 v247, v247, v199, v200\n\tv_max3_u32 v247, v247, v201, v202\n\tv_max_u32 v247, v247, v203\n\t" \
  "v_cmp_eq_u32 vcc, -1, v247\n\ts_cbranch_vccz .LSL1b%=\n\ts_sleep 1\n\ts_sub_u32 s87, s87, 1\n\ts_cbranch_scc0 .LSL1a%=\n\t.LSL1b%=:\n" \
  "s_branch .LRED1%=\n\t.LSLS%=:\ns_add_u32 s88, s80, 10\n\ts_cmp_eq_u32 s84, s88\n\ts_cbranch_scc1 .LH1B%=\n\ts_mov_b32 s84, s88\n\t" \
  "s_mov_b32 s87, %[spin]\n\t.LSLSa%=:\nbuffer_load_dwordx2 v[202:203], %[ss], %[rs], s82 offen sc1\n\ts_waitcnt vmcnt(0)\n\t" \
  "v_max3_u32 v247, v202, v203, v203\n\tv_cmp_eq_u32 vcc, -1, v247\n\ts_cbranch_vccz .LSLSb%=\n\ts_sleep 1\n\ts_sub_u32 s87, s87, 1\n\t" \
  "s_cbranch_scc0 .LSLSa%=\n\t.LSLSb%=:\ns_branch .LREDS%=\n\t.LSLM%=:\nv_max_f32_dpp v247, v246, v246 row_ror:1 row_mask:0xf bank_mask:0xf\n\t" \
  "s_nop 1\n\tv_max_f32_dpp v247, v247, v247 row_ror:2 row_mask:0xf bank_mask:0xf\n\ts_nop 1\n\t" \
  "v_max_f32_dpp v247, v247, v247 row_ror:4 row_mask:0xf bank_mask:0xf\n\ts_nop 1\n\t" \
  "v_max_f32_dpp v247, v247, v247 row_ror:8 row_mask:0xf bank_mask:0xf\n\ts_nop 1\n\t" \
  "v_max_f32_dpp v247, v247, v247 row_bcast:15 row_mask:0xa bank_mask:0xf\n\ts_nop 1\n\t" \
  "v_max_f32_dpp v247, v247, v247 row_bcast:31 row_mask:0xc bank_mask:0xf\n\ts_nop 1\n\tv_readlane_b32 s88, v247, 63\n\ts_nop 3\n\t" \
  "v_mov_b32 v239, s88\n\tv_sub_f32 v246, v246, v239\n\tv_exp_f32 v203, v246\n\ts_nop 0\n\tv_cvt_pk_bf16_f32 v244, v203, 0\n\t" \
  "v_add_f32_dpp v203, v203, v203 row_ror:1 row_mask:0xf bank_mask:0xf\n\ts_nop 0\n\tv_mov_b32_dpp v245, v244 row_shl:1 row_mask:0xf bank_mask:0xf\n\t" \
  "s_branch .LPV%=\n\t.LSLC%=:\nv_max_f32_dpp v247, v204, v204 row_ror:8 row_mask:0xf bank_mask:0xf\n\ts_nop 1\n\t" \
  "v_max_f32_dpp v247, v247, v247 row_ror:4 row_mask:0xf bank_mask:0xf\n\ts_nop 1\n\t" \
  "v_max_f32_dpp v247, v247, v247 quad_perm:[1,0,3,2] row_mask:0xf bank_mask:0xf\n\tv_sub_f32 v204, v204, v247\n\tv_exp_f32 v204, v204\n\ts_nop 0\n\t" \
  "v_mul_f32 v194, v204, v194\n\tv_mul_f32 v195, v204, v195\n\tv_mul_f32 v196, v204, v196\n\tv_mul_f32 v197, v204, v197\n\ts_nop 0\n\t" \
  "v_add_f32_dpp v205, v194, v194 row_ror:8 row_mask:0xf bank_mask:0x3\n\tv_add_f32_dpp v206, v195, v195 row_ror:8 row_mask:0xf bank_mask:0x3\n\t" \
  "v_add_f32_dpp v205, v196, v196 row_ror:8 row_mask:0xf bank_mask:0xc\n\tv_add_f32_dpp v206, v197, v197 row_ror:8 row_mask:0xf bank_mask:0xc\n\t" \
  "s_nop 0\n\tv_add_f32_dpp v246, v205, v205 row_ror:12 row_mask:0xf bank_mask:0x5\n\t" \
  "v_add_f32_dpp v246, v206, v206 row_ror:4 row_mask:0xf bank_mask:0xa\n\ts_nop 1\n\t" \
  "v_add_f32_dpp v246, v246, v246 quad_perm:[1,0,3,2] row_mask:0xf bank_mask:0xf\n\ts_cmp_lg_u32 %[w], 6\n\ts_cbranch_scc1 .LCMS%=\n\t" \
  "v_cmp_eq_u32 vcc, 0, v242\n\tv_cndmask_b32 v246, v246, v247, vcc\n\ts_branch .LCMS%=\n\t.LFIN%=:\n" \
  "buffer_store_short v243, v241, %[ro], %[oo] offen\n\t.LFE%=:\n.LEND%=:\n"
static_assert(BLK == 16384 && NT == 512 && QS == 400 && L_SM == 0 && TW == 7 && SW == 6, "constants of the hand-written part");

__global__ void __launch_bounds__(NT, 2)
mega_kernel(const uint16_t* __restrict__ x_in, const uint16_t* __restrict__ kc, const uint16_t* __restrict__ vc,
            const uint16_t* __restrict__ wq, const uint16_t* __restrict__ wo, const uint16_t* __restrict__ w1,
            const uint16_t* __restrict__ b1, const uint16_t* __restrict__ w2, const uint16_t* __restrict__ b2,
            uint16_t* __restrict__ out, unsigned* epoch SCHED_ARG) {
  extern __shared__ __attribute__((aligned(16))) char lds[];
  uint32_t cp;
  const uint64_t code0 = __builtin_amdgcn_s_getpc() & ~(uint64_t)127;
  asm volatile("global_load_dword %0, %1, %2" : "=v"(cp) : "v"(min((int)((blockIdx.x >> 3) * 1024 + (threadIdx.x >> 6) * 128), CODEPF * 1024 - 128)),
               "s"(code0) : "memory");
  __builtin_amdgcn_sched_barrier(0);
  const int tid0 = threadIdx.x, w0 = __builtin_amdgcn_readfirstlane(tid0 >> 6);
  const int b0 = blockIdx.x;
  unsigned long long* gran_base = (unsigned long long*)epoch + 512;
  char* tail = (char*)(((uintptr_t)epoch + (4u << 20)) & ~(uintptr_t)((2u << 20) - 1));
  const auto rs = __builtin_amdgcn_make_buffer_rsrc(tail, 0, 128 * BLK, 0x00020000);
  auto ld16 = [&](int voff, int so) -> u32x4 { return __builtin_amdgcn_raw_buffer_load_b128(rs, voff, so, 16); };
  auto ld8 = [&](int voff, int so) -> u32x2 { return __builtin_amdgcn_raw_buffer_load_b64(rs, voff, so, 16); };
  auto ld4 = [&](int voff, int so) -> uint32_t { return __builtin_amdgcn_raw_buffer_load_b32(rs, voff, so, 16); };
  auto st16 = [&](int voff, int so, const f32x4& v) { __builtin_amdgcn_raw_buffer_store_b128(__builtin_bit_cast(u32x4, v), rs, voff, so, 16); };
  auto st4 = [&](int voff, int so, float a) { __builtin_amdgcn_raw_buffer_store_b32(__float_as_uint(a), rs, voff, so, 16); };
  auto trd = [&](int off) -> u32x2 {
    return __builtin_bit_cast(u32x2, __builtin_amdgcn_ds_read_tr16_b64_v4i16((LAS s16x4*)(uintptr_t)(uint32_t)off));
  };
  auto lrd16 = [&](int off) -> u32x4 { return *(const LAS u32x4*)(uintptr_t)(uint32_t)off; };
  auto lrd8 = [&](int off) -> u32x2 { return *(const LAS u32x2*)(uintptr_t)(uint32_t)off; };
  auto lrd4 = [&](int off) -> uint32_t { return *(const LAS uint32_t*)(uintptr_t)(uint32_t)off; };
  auto lwr2 = [&](int off, uint32_t v) { *(LAS uint16_t*)(uintptr_t)(uint32_t)off = (uint16_t)v; };
  auto lwr4 = [&](int off, uint32_t v) { *(LAS uint32_t*)(uintptr_t)(uint32_t)off = v; };
  constexpr int sm = L_SM;

  uint32_t tag;
  uint32_t tE = 0;
  uint32_t tcur = 0;
  u32x8 kt[12], vt[12];
  uint32_t ix;
  int t16, bo, sS, dT;

  auto qk_softmax = [&](float& mx, float& ev, f32x4 (&dv)[3], int qb, float qs, bool cs = false) -> uint32_t {
    u32x4 qa[3];
    f32x4 d[4];
#pragma unroll
    for (int u = 0; u < 3; ++u) qa[u] = lrd16(qb + 64 * u);
#pragma unroll
    for (int a = 0; a < 1; ++a) { ZERO4(d[0], 204, 205, 206, 207); ZERO4(d[1], 208, 209, 210, 211); ZERO4(d[2], 212, 213, 214, 215); ZERO4(d[3], 216, 217, 218, 219); }
    SB();
#pragma unroll
    for (int u = 0; u < 3; ++u)
#pragma unroll
      for (int a = 0; a < 4; ++a) {
        d[a] = smf<0>(qa[u], kt[3 * a + u], d[a], ix);
        SB();
        if (u == 1 && a == 0) ZERO4(dv[0], 220, 221, 222, 223);
        if (u == 1 && a == 2) ZERO4(dv[1], 224, 225, 226, 227);
        if (u == 2 && a == 0) ZERO4(dv[2], 228, 229, 230, 231);
        SB();
      }
    __builtin_amdgcn_s_setprio(1);
    const bool same = __builtin_amdgcn_inverse_ballot_w64(0xff00ff0000ff00ffull);
    const float f0 = same ? d[0][0] + d[0][1] : d[1][0] + d[1][1], f1 = same ? d[2][0] + d[2][1] : d[3][0] + d[3][1];
    CSTAMP(cs, 41);
    auto r = __builtin_amdgcn_permlane32_swap(__float_as_uint(f0), __float_as_uint(f1), false, false);
    const float sraw = uf(r[0]) + dppv<0x128>(uf(r[1]));
    const float sv = rbf(rbf(sraw) * 0.07216878364870322f) * LOG2E;
    uint64_t big = __builtin_amdgcn_ballot_w64(!(fabsf(sv) <= SMALL));
    PINS(big);
    SB();
    mx = 0.f;
    ev = __builtin_amdgcn_exp2f(sv);
    uint32_t pb = pk2(ev, 0.f);
    PIN1(pb);
    SB();
    if (__builtin_expect(big != 0, 0)) {
      float mxv;
      asm volatile("s_nop 1\n\t"
                   "v_max_f32_dpp %0, %1, %1 row_ror:1 row_mask:0xf bank_mask:0xf\n\ts_nop 1\n\t"
                   "v_max_f32_dpp %0, %0, %0 row_ror:2 row_mask:0xf bank_mask:0xf\n\ts_nop 1\n\t"
                   "v_max_f32_dpp %0, %0, %0 row_ror:4 row_mask:0xf bank_mask:0xf\n\ts_nop 1\n\t"
                   "v_max_f32_dpp %0, %0, %0 row_ror:8 row_mask:0xf bank_mask:0xf\n\ts_nop 1\n\t"
                   "v_max_f32_dpp %0, %0, %0 row_bcast:15 row_mask:0xa bank_mask:0xf\n\ts_nop 1\n\t"
                   "v_max_f32_dpp %0, %0, %0 row_bcast:31 row_mask:0xc bank_mask:0xf\n\ts_nop 0"
                   : "=&v"(mxv) : "v"(sv));
      mx = __int_as_float(__builtin_amdgcn_readlane(__float_as_int(mxv), 63));
      ev = __builtin_amdgcn_exp2f(sv - mx);
      pb = pk2(ev, 0.f);
    }
    __builtin_amdgcn_s_setprio(0);
    CSTAMP(cs, 42);
    return pb;
  };
  auto pv_combine = [&](const u32x4& cT, int od, bool sw, bool cs = false) {
    CSTAMP(cs, 45);
    const uint32_t m = lrd4(cT.x);
    u32x4 rv = lrd16(cT.y);
    SB();
    uint64_t shifted = __builtin_amdgcn_ballot_w64(m != 0);
    PINS(shifted);
    SB();
    float o = sum_b320(rv);
    PIN1(o);
    SB();
    if (__builtin_expect(shifted != 0, 0)) {
      float M = fmaxf(uf(m), dppv<0x128>(uf(m)));
      M = fmaxf(M, dppv<0x124>(M));
      M = fmaxf(M, dppv<0xb1>(M));
      const float f = __builtin_amdgcn_exp2f(uf(m) - M);
      rv = __builtin_bit_cast(u32x4, __builtin_bit_cast(f32x4, rv) * f);
      asm volatile("s_nop 1" : "+v"(rv));
      o = sum_b320(rv);
      if (sw && (t16 & 0xc0) == 0) o = M;
    }
    st4(cT.z, od, o);
    CSTAMP(cs, 46);
  };
  int s0;
  int oG, oD, rC, eC, eH, eQ, rK;
  int dG, dH, dR, dE;
  int oD1, cwo, oo, far, scv[9];
  u32x4 rs4, ro4;
  float eps = 1e-5f, small = SMALL;

  {
    const int w = w0, b = b0, lane = tid0 & 63;
    const int g = (b & 7) * 4 + (b >> 6), j = (b >> 3) & 7, jg = b >> 3, jj = b & 7;
    const int ck = lane & 7, jp = lane >> 3, n16 = lane & 15, kq = lane >> 4;
    const int sub = (lane >> 5) + 2 * ((lane >> 4) & 1), trl = 4 * kq + (n16 >> 2), trc = 8 * (lane & 3);
    const int xc = min(16 * w + 4 * (lane & 3) + sub, 79);
    const int gc = 64 * (w < 3 ? w : 0) + 4 * (lane & 8) + 4 * ck + sub, hc = (w & 1) ? 32 + 4 * (lane & 3) + sub : 4 * ck + sub;
    gu64* G = (gu64*)gran_base;
    STAMPV(0);
    const size_t row0 = (size_t)g * SK + (SK - WIN) + j * 64 + w * 8;
    const uint16_t *kp = kc + row0 * DH, *vp = vc + row0 * DH;
    const int rot = g, hx = (g >> 2) & 1;
    const int vo = ((kq >> 1) ^ hx) * (2048 * 384) + (kq & 1) * 384 + n16 * 16, vo2 = vo + 256 - n16 * 8;
    const int ko = ((lane & 4) + ((lane & 1) << 1) + ((lane >> 1) & 1)) * 384 + (lane & 8) * 8 + kq * 16, ko2 = ko ^ 64;
#define GRP(lo, hi) "+{v[" #lo ":" #hi "]}"
#define LOADK(t, r0, r1, r2, g0, g1, g2) if constexpr (T == t) \
      asm volatile("global_load_dwordx4 " r0 ", %3, %4 nt\n\tglobal_load_dwordx4 " r1 ", %3, %4 offset:128 nt\n\t" \
                   "global_load_dwordx4 " r2 ", %3, %4 offset:256 nt" \
                   : g0(kt[a]), g1(kt[a + 1]), g2(kt[a + 2]) : "v"(o), "s"(p) : "memory")
    auto load_k = [&](auto Tc) {
      constexpr int T = decltype(Tc)::value, a = 3 * (T >> 1);
      const int o = (T & 2) ? ko2 : ko;
      const uint16_t* p = kp + ((((T >> 1) + rot) & 3) * 512 + ((T & 1) ^ hx) * 2048) * DH;
      LOADK(0, "v[2:5]", "v[10:13]", "v[18:21]", GRP(2, 9), GRP(10, 17), GRP(18, 25));
      LOADK(1, "v[6:9]", "v[14:17]", "v[22:25]", GRP(2, 9), GRP(10, 17), GRP(18, 25));
      LOADK(2, "v[26:29]", "v[34:37]", "v[42:45]", GRP(26, 33), GRP(34, 41), GRP(42, 49));
      LOADK(3, "v[30:33]", "v[38:41]", "v[46:49]", GRP(26, 33), GRP(34, 41), GRP(42, 49));
      LOADK(4, "v[50:53]", "v[58:61]", "v[66:69]", GRP(50, 57), GRP(58, 65), GRP(66, 73));
      LOADK(5, "v[54:57]", "v[62:65]", "v[70:73]", GRP(50, 57), GRP(58, 65), GRP(66, 73));
      LOADK(6, "v[74:77]", "v[82:85]", "v[90:93]", GRP(74, 81), GRP(82, 89), GRP(90, 97));
      LOADK(7, "v[78:81]", "v[86:89]", "v[94:97]", GRP(74, 81), GRP(82, 89), GRP(90, 97));
      SB();
    };
#define LOADV(v, x0, y0, x1, y1, g0, g1, g2) if constexpr (V == v) \
      asm volatile("global_load_dwordx4 " x0 ", %3, %5 nt\n\tglobal_load_dwordx2 " y0 ", %4, %5 nt\n\t" \
                   "global_load_dwordx4 " x1 ", %3, %5 offset:768 nt\n\tglobal_load_dwordx2 " y1 ", %4, %5 offset:768 nt" \
                   : g0(vt[a]), g1(vt[a + 1]), g2(vt[a + 2]) : "v"(vo), "v"(vo2), "s"(p) : "memory")
    auto load_v = [&](auto Vc) {
      constexpr int V = decltype(Vc)::value, a = 3 * (V >> 1);
      const uint16_t* p = vp + (((2 * (V & 1) + (V >> 2) + rot) & 3) * 512 + ((V >> 1) & 1) * 4) * DH;
      LOADV(0, "v[98:101]", "v[114:115]", "v[106:109]", "v[116:117]", GRP(98, 105), GRP(106, 113), GRP(114, 121));
      LOADV(1, "v[102:105]", "v[118:119]", "v[110:113]", "v[120:121]", GRP(98, 105), GRP(106, 113), GRP(114, 121));
      LOADV(2, "v[122:125]", "v[138:139]", "v[130:133]", "v[140:141]", GRP(122, 129), GRP(130, 137), GRP(138, 145));
      LOADV(3, "v[126:129]", "v[142:143]", "v[134:137]", "v[144:145]", GRP(122, 129), GRP(130, 137), GRP(138, 145));
      LOADV(4, "v[146:149]", "v[162:163]", "v[154:157]", "v[164:165]", GRP(146, 153), GRP(154, 161), GRP(162, 169));
      LOADV(5, "v[150:153]", "v[166:167]", "v[158:161]", "v[168:169]", GRP(146, 153), GRP(154, 161), GRP(162, 169));
      LOADV(6, "v[170:173]", "v[186:187]", "v[178:181]", "v[188:189]", GRP(170, 177), GRP(178, 185), GRP(186, 193));
      LOADV(7, "v[174:177]", "v[190:191]", "v[182:185]", "v[192:193]", GRP(170, 177), GRP(178, 185), GRP(186, 193));
      SB();
    };
    int go[5];
    auto wo_prep = [&]() {
      int l = lane;
      PIN1(l);
#pragma unroll
      for (int i = 0; i < 5; ++i) { const int c = min(64 * i + l, w < 6 ? 319 : 239), row = (c * 205) >> 11; go[i] = row * (2 * DM) + (c - 10 * row) * 16; PIN1(go[i]); }
    };
    auto load_wo = [&]() {
      const uint16_t* src = (w < 6 ? wo + (size_t)(192 * g + 32 * w) * DM : w2 + (size_t)(48 * g + 24 * (w - 6)) * DM) + 80 * j;
      unsigned keep;
      asm volatile("s_mov_b32 %0, m0\n\ts_mov_b32 m0, %7\n\ts_nop 0\n\tglobal_load_lds_dwordx4 %1, %6 nt\n\t"
                   "s_add_u32 m0, m0, 0x400\n\ts_nop 0\n\tglobal_load_lds_dwordx4 %2, %6 nt\n\t"
                   "s_add_u32 m0, m0, 0x400\n\ts_nop 0\n\tglobal_load_lds_dwordx4 %3, %6 nt\n\t"
                   "s_add_u32 m0, m0, 0x400\n\ts_mov_b64 exec, %8\n\tglobal_load_lds_dwordx4 %4, %6 nt\n\t"
                   "s_add_u32 m0, m0, %10\n\ts_mov_b64 exec, %9\n\tglobal_load_lds_dwordx4 %5, %6 nt\n\t"
                   "s_mov_b64 exec, -1\n\ts_mov_b32 m0, %0"
                   : "=&s"(keep) : "v"(go[0]), "v"(go[1]), "v"(go[2]), "v"(go[3]), "v"(go[4]), "s"(src), "s"(w < 7 ? L_WO + w * 5120 : L_W2 + 3840),
                     "s"(w < 6 ? ~0ull : (1ull << 48) - 1), "s"(w < 6 ? ~0ull : 1ull), "s"(w < 6 ? 1024 : 752) : "memory", "scc");
      SB();
    };
    asm volatile("" : "=v"(vt[0]), "=v"(vt[1]), "=v"(vt[2]));
    load_v(IC<0>{});
    ESTAMP(56);
    uint32_t tp = rt32() + SC0(PO);
    int pstep = SC0(PT);
    if (TWE && w == TW) __builtin_amdgcn_s_setprio(1);
    int rix = 40;
    auto pace = [&]() {
      int rem;
      if (w == TW) asm volatile("1:\n\ts_memrealtime vcc\n\ts_waitcnt lgkmcnt(0)\n\ts_sub_i32 %0, %1, vcc_lo\n\ts_cmp_lt_u32 %0, 0x800\n\ts_cbranch_scc1 1b\n\ts_add_u32 %1, %1, %2"
                   : "=&s"(rem), "+s"(tp) : "s"(pstep) : "memory", "scc", "vcc");
    };
    const bool early = w < 4 || (TWE && w == TW);
    auto first = [&](auto Ic) {
      constexpr int i = decltype(Ic)::value, x = i <= 8 ? kord(i - 1) : vord(i - 8);
      u32x8* p = (i <= 8 ? kt : vt) + 3 * (x >> 1);
      if (!(x & 1)) asm volatile("" : "=v"(p[0]), "=v"(p[1]), "=v"(p[2]));
    };
    auto tile0 = [&](auto Ic) {
      constexpr int i = decltype(Ic)::value;
      if constexpr (i <= 8) load_k(IC<kord(i - 1)>{}); else load_v(IC<vord(i - 8)>{});
    };
    auto tile = [&](auto Ic) { first(Ic); tile0(Ic); };
    auto tile_e = [&](auto Ic) { first(Ic); if (early) tile0(Ic); SB(); };
    auto tile_l = [&](auto Ic) { SB(); if (!early) tile0(Ic); };
    auto piece = [&](int k, int n) { const int o = n * (b >> 2) + lane * 16; return lane * 16 < n ? O_S + ((b & 3) * NB + k + (o >> 13)) * BLK + (o & 8191) : OOR; };
    const int prb = O_PR + (b >> 3) * BLK + (w >> 2) * 8192 + (b & 7) * 1024 + (w & 3) * 256;
    uint32_t tv = 0;
    PINS(tv);
    if (b < NVOTE && w == SW) {
      __builtin_amdgcn_raw_buffer_store_b128((u32x4){0, 0, 0, 0}, rs, (lane & 12) != 12 && lane < 32 ? prb - 8192 - 512 + (lane >> 4) * 8192 + (lane & 12) * 64 + (lane & 3) * 16 : OOR, 0, 16);
      asm volatile("s_load_dword %0, %1, 0x80" : "+s"(tv) : "s"(tail + prb) : "memory");
    }
    SB();
    uint32_t ep = 0, xf[5] = {0, 0, 0, 0, 0}, xs = 0;
    asm volatile("" : "+v"(xf[0]), "+v"(xf[1]), "+v"(xf[2]), "+v"(xf[3]), "+v"(xf[4]), "+v"(xs), "+v"(ep));
    if (w == 1) asm volatile("global_load_dword %0, %1, off" : "+v"(xs) : "v"(lane < 40 ? b2 + 80 * jj + 2 * lane : b1 + 48 * g + 2 * lane - 80) : "memory");
    if (w == 0)
      asm volatile("global_load_dword %0, %7, %8\n\tglobal_load_dword %1, %7, %8 offset:256\n\tglobal_load_dword %2, %7, %8 offset:512\n\t"
                   "global_load_dword %3, %7, %8 offset:768\n\tglobal_load_dword %4, %7, %8 offset:1024\n\tglobal_load_dword %5, %9, %8\n\tglobal_load_dword %6, %10, %11 sc1"
                   : "+v"(xf[0]), "+v"(xf[1]), "+v"(xf[2]), "+v"(xf[3]), "+v"(xf[4]), "+v"(xs), "+v"(ep)
                   : "v"(4 * lane), "s"(x_in), "v"(160 * jj + 4 * min(lane, 39)), "v"(0), "s"(epoch) : "memory");
    SB();
    {
      int go[4];
#pragma unroll
      for (int r = 0; r < 4; ++r) { const int c = min(64 * r + lane, 249), row = (c * 41) >> 10; go[r] = row * (2 * QD) + min(c - 25 * row, 23) * 16; }
      unsigned keep;
      asm volatile("s_mov_b32 %0, m0\n\ts_mov_b32 m0, %6\n\ts_nop 0\n\tglobal_load_lds_dwordx4 %1, %5 nt\n\t"
                   "s_add_u32 m0, m0, 0x400\n\ts_nop 0\n\tglobal_load_lds_dwordx4 %2, %5 nt\n\t"
                   "s_add_u32 m0, m0, 0x400\n\ts_nop 0\n\tglobal_load_lds_dwordx4 %3, %5 nt\n\t"
                   "s_add_u32 m0, m0, 0x400\n\ts_mov_b64 exec, %7\n\tglobal_load_lds_dwordx4 %4, %5 nt\n\t"
                   "s_mov_b64 exec, -1\n\ts_mov_b32 m0, %0"
                   : "=&s"(keep) : "v"(go[0]), "v"(go[1]), "v"(go[2]), "v"(go[3]),
                     "s"(wq + (size_t)(80 * jj + 10 * w) * QD + 192 * jg), "s"(L_WQ + w * 10 * QS), "s"((1ull << 58) - 1) : "memory", "scc");
    }
    SB();
    {
      const int c = min(lane, 59), row = (c * 43) >> 8;
      unsigned keep;
      asm volatile("s_mov_b32 %0, m0\n\ts_mov_b32 m0, %3\n\ts_mov_b64 exec, %4\n\tglobal_load_lds_dwordx4 %1, %2 nt\n\ts_mov_b64 exec, -1\n\ts_mov_b32 m0, %0"
                   : "=&s"(keep) : "v"(row * (2 * FF) + (c - 6 * row) * 16), "s"(w1 + (size_t)(80 * jj + 10 * w) * FF + 48 * jg), "s"(L_W1 + w * 960), "s"((1ull << 60) - 1) : "memory");
    }
    SB();
    ESTAMP(57);
    BAR0();
    ESTAMP(58);
    tile_e(IC<1>{});
    ESTAMP(59);
    auto probe = [&](auto Oc) -> uint32_t {
      unsigned long long c0, c1; uint32_t v;
      asm volatile("s_memtime %0\n\ts_waitcnt lgkmcnt(0)\n\ts_load_dword %2, %3, %4\n\ts_waitcnt lgkmcnt(0)\n\ts_memtime %1\n\ts_waitcnt lgkmcnt(0)"
                   : "=&s"(c0), "=&s"(c1), "=&s"(v) : "s"(tail + prb), "n"(decltype(Oc)::value) : "memory");
      return (uint32_t)(c1 - c0);
    };
    {
      t16 = w * 1024 + lane * 16; bo = sm + kq * 16;
      sS = w == SW ? (n16 == 0 ? sm + S_XP + kq * 16 : L_ONE) : ck * 32;
      dT = lane < (w == SW ? 2 : 16) ? lane * 4 : OOR;
      LAS u32x4* T = (LAS u32x4*)(uintptr_t)(uint32_t)(L_TAB + t16);
      const int pk = ((lane >> 1) & 6) | (lane & 1), c4 = 8 * w + ((lane >> 3) & 6) + ((lane >> 1) & 1);
      T[0] = (u32x4){(uint32_t)(sm + S_ML + pk * 16), (uint32_t)(w < 6 ? sm + S_RED + pk * RS + c4 * 16 : sm + S_ML + pk * 16),
                     (uint32_t)(w == SW ? ((lane & ~4) == 0 ? lane : OOR) : (!(lane & 1) && w < 6) ? c4 * 16 + (lane & 12) : OOR), (uint32_t)(jp * 768 + w * 256 + ck * 16)};
      T[5 * NT] = (u32x4){(uint32_t)((kq >> 1) == ((n16 >> 2) & 1) ? sm + S_QB + (n16 & 1) * 192 + (n16 & 8) * 4 + (kq & 1) * 16 : sm + S_ZQ),
                          (uint32_t)(sm + S_RED + w * RS + lane * 4), (uint32_t)(sm + S_ML + w * 16), 0u};
      ix = ((n16 & 1) ? 0xeeeeu : 0x4444u) | (n16 >> 2) * 0x55550000u;
      const int gd = (gc >> 4) & 3, dimo = gd + (gc < 128 ? 8 * (gc & 15) + 4 * (gc >> 6) : 128 + 4 * (gc & 15));
      if (w == 1 && lane < 12) *(LAS u32x4*)(uintptr_t)(uint32_t)(sm + S_ZQ + lane * 16) = (u32x4){0, 0, 0, 0};
      T[NT] = (u32x4){(uint32_t)(sm + S_OV + opos(dimo)), (uint32_t)(L_WO + (w < 5 ? w : 0) * 32 + trl * 160 + trc),
                      (uint32_t)(sm + S_XP + 2 * kpos(xc)), (uint32_t)(L_W1 + (w < 3 ? w : 0) * 32 + trl * 96 + trc)};
      T[2 * NT] = (u32x4){(uint32_t)(w < 5 ? sm + S_HV + 2 * kpos(hc) : piece(0, 768) + (w & 1) * 8192), (uint32_t)(w < 5 ? L_W2 + w * 32 + trl * 160 + trc : piece(6, 1024)),
                          (uint32_t)(L_CS + 768 + 4 * hc), (uint32_t)((w & 1) ? 1024 + jp * 64 + (ck & 3) * 16 : lane * 16)};
      T[3 * NT] = (u32x4){(uint32_t)(sm + S_QB + qpos(gc)), (uint32_t)(L_CS + 4 * gc), (uint32_t)(L_WQ + (w < 6 ? w : 0) * 64 + trl * QS + trc), (uint32_t)(2 * xc)};
    }
    ESTAMP(60);
    tile_l(IC<1>{});
    BAR0();
    tile(IC<2>{});
    ESTAMP(61);
    asm volatile("s_waitcnt vmcnt(%8)" : "+v"(ep), "+v"(xf[0]), "+v"(xf[1]), "+v"(xf[2]), "+v"(xf[3]), "+v"(xf[4]), "+v"(xs), "+v"(cp) : "n"(3 * UP + 2) : "memory");
    if (w < 2) lwr4(sm + S_B + w * 256 + lane * 4, xs);
    if (w == 0) {
      float s1 = 0.f;
#pragma unroll
      for (int i = 0; i < 5; ++i) s1 += lo2f(xf[i]) + hi2f(xf[i]);
      const float mean = wsum_bcast(s1) * (1.0f / DM);
      float q0 = 0.f;
#pragma unroll
      for (int i = 0; i < 5; ++i) { const float d0 = lo2f(xf[i]) - mean, d1 = hi2f(xf[i]) - mean; q0 = fmaf(d0, d0, q0); q0 = fmaf(d1, d1, q0); }
      const float var = wsum_bcast(q0) * (1.0f / DM);
      const uint32_t mv = pk2(mean, var);
      const float rinv = __builtin_amdgcn_rcpf(rbf(__builtin_amdgcn_sqrtf(rbf(hi2f(mv) + 1e-5f))));
      if (lane < 48) lwr4(sm + S_XP + 2 * kpos(2 * lane), lane < 40 ? pk2(rbf(lo2f(xs) - lo2f(mv)) * rinv, rbf(hi2f(xs) - lo2f(mv)) * rinv) : 0u);
      if (lane < 48) lwr4(L_ONE + lane * 4, 0x3f803f80u);
      if (lane < 16) lwr2(sm + S_HV + 2 * kpos(48 + lane), 0);
      lwr4(L_FLAG, ep * 1664525u + 1013904223u);
    }
    if (b < NVOTE && (w & 3) != 3) lwr4(L_FLAG + 32 + 4 * w, probe(IC<0>{}));
    SB();
    asm volatile("s_waitcnt vmcnt(%0)" :: "n"(6) : "memory");
    STAMPV(1);
    lds_barrier();
    PINS(tv);
    tile_e(IC<3>{});
    tag = __builtin_amdgcn_readfirstlane(lrd4(L_FLAG));
    float pv[2];
    {
      const int xb = n16 >= 4 ? L_ONE : sm + S_XP + kq * 16;
      const u32x4 x0 = lrd16(xb), x1 = lrd16(xb + 64), x2 = lrd16(xb + 128);
#pragma unroll
      for (int r = 0; r < 2; ++r) {
        const int tl = min(2 * w + r, 14), rstr = tl < 12 ? QS : 96;
        const int a0 = (tl < 12 ? L_WQ : L_W1 - 12 * 32) + tl * 32 + trl * rstr + trc;
        f32x4 d = {0.f, 0.f, 0.f, 0.f};
        d = mfma32(x0, cat(trd(a0), trd(a0 + 16 * rstr)), d);
        d = mfma32(x1, cat(trd(a0 + 32 * rstr), trd(a0 + 48 * rstr)), d);
        d = mfma32(x2, cat(trd(a0 + 64 * rstr), (u32x2){0, 0}), d);
        pv[r] = d[0];
      }
    }
    gu64* dst = G + G_P + b * RP + (kq == 0 ? 0 : 192) + n16 + 32 * w;
    const int cu = (lane >> 2) & 7;
    int so = (w & 3) == 0 ? g * BLK + (w >> 2) * 8192 + j * 1024 + lane * 16
             : w == 1 && cu < 5 ? O_C + (2 * j + (cu >> 2)) * BLK + (lane >> 5) * 8192 + (cu & 3) * 2048 + ((g + 7 * cu) & 31) * 64 + (lane & 3) * 16 : OOR;
    if (w == TW) {
      unsigned xcc;
      asm volatile("s_getreg_b32 %0, hwreg(HW_REG_XCC_ID)" : "=s"(xcc));
      const u32x4 l0 = lrd16(L_FLAG + 32), l1 = lrd16(L_FLAG + 48);
      const uint32_t vote = ((min(min(l1.x, l1.y), l1.z) < min(min(l0.x, l0.y), l0.z) ? 1u : 0u) ^ (xcc >> 2)) & 1u;
      pv[1] = uf(vote | ((xcc >> 1) & 2) | (rt32() << 2));
    }
    PIN1(pv[0]); PIN1(pv[1]); PIN1(dst); PIN1(so);
    tile_l(IC<3>{});
    STAMPV(2);
    pace();
    BAR0();
    if (kq == 1 || (kq == 0 && w < 6)) {
      __hip_atomic_store(dst, ((unsigned long long)tag << 32) | __float_as_uint(pv[0]), RLX_AGENT);
      if (w < 7) __hip_atomic_store(dst + 16, ((unsigned long long)tag << 32) | __float_as_uint(pv[1]), RLX_AGENT);
    }
    if (w == TW && b < NVOTE && lane == 0) __hip_atomic_store(G + G_F + b, ((unsigned long long)tag << 32) | __float_as_uint(pv[1]), RLX_AGENT);
    if ((w & 2) == 0 && w != 5) __builtin_amdgcn_raw_buffer_store_b128((u32x4){SENT, SENT, SENT, SENT}, rs, so, 0, 16);
    SB();
    tile(IC<RT>{});
    pace();
    BAR0();
    tile_e(IC<RT + 1>{});
    {
      auto rd = [&](int off) { return lo2f(*(const LAS uint16_t*)(uintptr_t)(uint32_t)(sm + S_B + off)); };
      *(LAS f32x4*)(uintptr_t)(uint32_t)(L_TAB + 4 * NT * 16 + t16) = (f32x4){rd(2 * xc), rd(256 + 2 * xc), rd(416 + 2 * hc), 0.f};
    }
    tile_l(IC<RT + 1>{});
    pace();
    BAR0();
    tile(IC<RT + 2>{});
    pace();
    BAR0();
    u32x4 er = {0, 0, 0, 0};
    asm volatile("" : "+v"(er));
    auto ereq = [&](const char* src) { asm volatile("global_load_dwordx4 %0, %1, off sc1" : "+v"(er) : "v"(src) : "memory"); };
    auto efin = [&](auto Nc, const char* src) {
      asm volatile("s_waitcnt vmcnt(%1)" : "+v"(er) : "n"(decltype(Nc)::value) : "memory");
      if (XOFF & 4) return;
#pragma unroll 1
      for (unsigned sp = 0; sp < SPIN_LIMIT; ++sp) {
        if (__all(tagbad(er, tag) == 0)) break;
        __builtin_amdgcn_s_sleep(4);
        asm volatile("global_load_dwordx4 %0, %1, off sc1\n\ts_waitcnt vmcnt(0)" : "+v"(er) : "v"(src) : "memory");
      }
    };
    {
      const int un = j + 8 * w;
      const bool mine = un < 27;
      const char* src = mine ? (const char*)(G + G_P + (8 * g + jp) * RP) + un * 128 + ck * 16 : (const char*)(G + G_F) + n16 * 16;
      if (mine || w == TW) ereq(src);
      SB();
      tile(IC<RT + 3>{});
      pace();
      BAR0();
      tile_e(IC<RT + 4>{});
      asm volatile("s_waitcnt vmcnt(%0)" :: "n"(nreq(RT, RT + 3)) : "memory");
      if (mine) efin(IC<nreq(RT + 3, RT + 4)>{}, src);
      const float t = swapadd32(uf(er.x), uf(er.z));
      uint32_t sv = __float_as_uint(ror8add(swapadd16(t, t)));
      gu64* sd = G + G_R + g * RP + un * 16 + 2 * ck + (lane >> 5);
      PIN1(sv); PIN1(sd);
      tile_l(IC<RT + 4>{});
      pace();
      BAR0();
      if (mine && !(lane & 24)) __hip_atomic_store(sd, ((unsigned long long)tag << 32) | sv, RLX_AGENT);
      if (w == TW) {
        if (b < NVOTE && lane == 0) __hip_atomic_store(G + G_F + NVOTE + b, ((unsigned long long)tag << 32) | rt32(), RLX_AGENT);
        efin(IC<nreq(RT + 3, RT + 4)>{}, src);
        const uint32_t hx = (uint32_t)__ballot(er.x & 2), hz = (uint32_t)__ballot(er.z & 2);
        lwr4(L_FLAG + 16, (__popcll(__ballot(er.x & 1)) + __popcll(__ballot(er.z & 1))) << 8 | (hx & 1) | ((hz & 1) << 1) | ((hx & 2) << 1) | ((hz & 2) << 2));
        if (SC(PB)) {
          const float d = (float)((int)(er.x - (tp << 2)) >> 2) + (float)((int)(er.z - (tp << 2)) >> 2);
          tp += __builtin_amdgcn_readfirstlane((int)(wsum_bcast(d) * (1.f / 128.f))) + SC(PA);
          pstep = SC(PB);
        }
      }
      tp = __builtin_amdgcn_readfirstlane(tp); pstep = __builtin_amdgcn_readfirstlane(pstep);
      SB();
    }
    tile(IC<RT + 5>{});
    pace();
    lds_barrier();
    tile_e(IC<RT + 6>{});
    {
      const int f0 = __builtin_amdgcn_readfirstlane(lrd4(L_FLAG + 16)), votes = f0 >> 8;
      const int m4 = (f0 & 15) ^ ((SC(CPAR) < 2 ? SC(CPAR) : votes > 2 * NVOTE) ? 15 : 0);
      const int pm = m4 | ((m4 ^ 15) << 4);
      auto par = [&](int x) { return ((pm >> x) & 1) * 8192; };
      const int cw = O_C + (w >> 2) * BLK + (w & 3) * 2048;
      oG = g * BLK + par(jj); oD = oG + (w == SW ? O_SG + j * 32 : j * 768);
      rC = 2 * jj * BLK + par(jj) + cw - w * 1024; eC = 2 * j * BLK + par(j) + cw + ((g + 7 * w) & 31) * 64;
      eH = jg * BLK + par(jg >> 2) + (w == SW ? O_SH + jj * 32 : O_H + (w < 2 ? jj * 128 + w * 64 : 1024 + jj * 64));
      rK = par(jj) + O_DB - TW * 1024;
      dG = O_S + ((jj & 3) * NB + 6 + (g & 3) - g) * BLK; dH = O_S + (((jg >> 2) & 3) * NB + 6 + (jg & 3) - jg) * BLK;
      dR = O_S - O_C + ((jj & 3) * NB + 10 - 2 * jj) * BLK; dE = O_S - O_C + ((j & 3) * NB + 10 - 2 * j) * BLK;
      eQ = jg * BLK + dH - 4 * BLK + par(jg >> 2) + (w == SW ? O_SG + jj * 32 : jj * 768 + w * 128);
#define UNI(a) a = __builtin_amdgcn_readfirstlane(a)
      UNI(oG); UNI(oD); UNI(rC); UNI(eC); UNI(eH); UNI(eQ); UNI(rK);
      oD1 = oD + dG; cwo = O_DB + b * 8; oo = 160 * b; far = min(b >> 3, 1);
      rs4 = (u32x4){(uint32_t)(uintptr_t)tail, (uint32_t)((uintptr_t)tail >> 32) & 0xffffu, 128u * BLK, 0x00020000u};
      ro4 = (u32x4){(uint32_t)(uintptr_t)out, (uint32_t)((uintptr_t)out >> 32) & 0xffffu, 2u * DM, 0x00020000u};
#pragma unroll
      for (int i = 0; i < 9; ++i) { scv[i] = i < 8 ? SC(i) : SC(PW); PINS(scv[i]); }
      PINS2(oG, oD); PINS2(rC, eC); PINS2(eH, eQ); PINS2(rK, oD1); PINS2(dG, dH); PINS2(dR, dE); PINS2(cwo, oo); PINS(far); PINS2(rs4, ro4); PINS2(eps, small);
      PIN1(sS); PIN1(dT);
    }
    tile_l(IC<RT + 6>{});
    pace();
    BAR0();
    {
      const int v0 = min(128 * w + 2 * lane, RP - 2);
      const char* src = w < 4 ? (const char*)(G + G_R + g * RP + v0) : (const char*)(G + G_F + NVOTE) + n16 * 16;
      if (w < 4 || w == TW) ereq(src);
      SB();
      tile(IC<RT + 7>{});
      pace();
      BAR0();
      tile(IC<RT + 8>{});
      pace();
      BAR0();
      if (w == TW && SC(PD)) {
        asm volatile("s_waitcnt vmcnt(%1)" : "+v"(er) : "n"(nreq(RT + 7, RT + 8)) : "memory");
        const bool v0 = er.y == tag, v1 = er.w == tag;
        const float d = (v0 ? (float)(int)(er.x - tp) : 0.f) + (v1 ? (float)(int)(er.z - tp) : 0.f);
        const int n = __popcll(__ballot(v0)) + __popcll(__ballot(v1));
        if (n > 32) {
          tp += __builtin_amdgcn_readfirstlane((int)(wsum_bcast(d) * __builtin_amdgcn_rcpf((float)n))) + SC(PC);
          pstep = SC(PD);
        }
      }
      tp = __builtin_amdgcn_readfirstlane(tp); pstep = __builtin_amdgcn_readfirstlane(pstep);
      SB();
      tile(IC<RT + 9>{});
      if (w < 4) {
        efin(IC<nreq(RT + 7, RT + 9)>{}, src);
        if (v0 < 192) lwr4(sm + S_QB + qpos(v0), pk2(uf(er.x), uf(er.z)));
        else *(LAS u32x2*)(uintptr_t)(uint32_t)(L_CS + 4 * (v0 - 192)) = (u32x2){er.x, er.z};
      }
    }
    SB();
    STAMPV(3);
    pace();
    lds_barrier();
    tile_e(IC<RT + 10>{});
    STAMPV(4);
    asm volatile("s_waitcnt vmcnt(%12)"
                 : GRP(2, 9)(kt[0]), GRP(10, 17)(kt[1]), GRP(18, 25)(kt[2]), GRP(26, 33)(kt[3]), GRP(34, 41)(kt[4]), GRP(42, 49)(kt[5]),
                   GRP(50, 57)(kt[6]), GRP(58, 65)(kt[7]), GRP(66, 73)(kt[8]), GRP(74, 81)(kt[9]), GRP(82, 89)(kt[10]), GRP(90, 97)(kt[11])
                 : "n"(nreq(9, RT + 9)) : "memory");
    const u32x4 aT = lrd16(L_TAB + 5 * NT * 16 + t16);
    uint32_t pb;
    f32x4 dv[3];
    {
      float mx, ev;
      pb = qk_softmax(mx, ev, dv, aT.x, 1.f);
      const float ls = wsum63(ev);
      if (lane == 63) *(LAS f32x2*)(uintptr_t)(uint32_t)aT.z = (f32x2){mx, ls};
    }
#define VFIX(v, e0, e1, e2, e3, g0, g1, g2) if constexpr (V == v) \
      asm volatile("s_waitcnt vmcnt(%3)\n\tv_swap_b32 v" #e0 ", v" #e2 "\n\tv_swap_b32 v" #e1 ", v" #e3 "\n\ts_nop 0" \
                   : g0(vt[a]), g1(vt[a + 1]), g2(vt[a + 2]) : "n"(decltype(YN)::value) : "memory")
    auto v_fix = [&](auto Vc, auto YN) {
      constexpr int V = decltype(Vc)::value, a = 3 * (V >> 1);
      SB();
      VFIX(0, 100, 101, 106, 107, GRP(98, 105), GRP(106, 113), GRP(114, 121));
      VFIX(1, 104, 105, 110, 111, GRP(98, 105), GRP(106, 113), GRP(114, 121));
      VFIX(2, 124, 125, 130, 131, GRP(122, 129), GRP(130, 137), GRP(138, 145));
      VFIX(3, 128, 129, 134, 135, GRP(122, 129), GRP(130, 137), GRP(138, 145));
      VFIX(4, 148, 149, 154, 155, GRP(146, 153), GRP(154, 161), GRP(162, 169));
      VFIX(5, 152, 153, 158, 159, GRP(146, 153), GRP(154, 161), GRP(162, 169));
      VFIX(6, 172, 173, 178, 179, GRP(170, 177), GRP(178, 185), GRP(186, 193));
      VFIX(7, 176, 177, 182, 183, GRP(170, 177), GRP(178, 185), GRP(186, 193));
      SB();
    };
    auto mm_step = [&](auto Ic, auto YN) {
      constexpr int i = decltype(Ic)::value;
      u32x4 pa = pset<i>(pb);
      PIN1(pa);
      v_fix(IC<2 * i + 1>{}, YN);
      dv[2] = smf<1>(pa, vt[3 * i + 2], dv[2], ix); SB();
      dv[0] = smf<1>(pa, vt[3 * i], dv[0], ix); dv[1] = smf<1>(pa, vt[3 * i + 1], dv[1], ix);
    };
    SB();
    wo_prep();
    tile_l(IC<RT + 10>{});
    pace();
    lds_barrier();
    tile_e(IC<15>{});
    v_fix(IC<0>{}, IC<16>{}); v_fix(IC<2>{}, IC<16>{});
    mm_step(IC<0>{}, IC<16>{}); mm_step(IC<1>{}, IC<12>{}); v_fix(IC<4>{}, IC<8>{});
    tile_l(IC<15>{});
    BAR0();
    load_wo();
    STAMPV(26);
    v_fix(IC<6>{}, IC<nreq(14, 15)>{});
    mm_step(IC<2>{}, IC<nreq(15, 15)>{});
    SB();
    u32x4 cT = lrd16(L_TAB + t16);
    PIN1(cT);
    mm_step(IC<3>{}, IC<5>{});
    STAMPV(27);
    lwr4(aT.y + 512, __float_as_uint(dv[2][0])); lwr4(aT.y, __float_as_uint(dv[0][0])); lwr4(aT.y + 256, __float_as_uint(dv[1][0]));
    lds_barrier();
    pv_combine(cT, oD, w == SW);
    s0 = cT.w;
  }

  if (TWE) __builtin_amdgcn_s_setprio(0);
  {
    asm volatile(TAIL_ASM
                 : [s0] "+v"(s0), [tc] "+s"(tcur), [te] "+s"(tE), [oG] "+s"(oG), [rC] "+s"(rC), [eC] "+s"(eC), [eH] "+s"(eH)
                 : [t16] "v"(t16), [bo] "v"(bo), [ss] "v"(sS), [dt] "v"(dT), [ix] "v"(ix), [rs] "s"(rs4), [ro] "s"(ro4), [w] "s"(w0), [far] "s"(far),
                   [oD] "s"(oD1), [dG] "s"(dG), [dH] "s"(dH), [dR] "s"(dR), [dE] "s"(dE), [eQ] "s"(eQ), [rK] "s"(rK), [tag] "s"(tag), [cw] "s"(cwo), [oo] "s"(oo),
                   [p3e] "s"(scv[0]), [p2] "s"(scv[1]), [p3] "s"(scv[2]), [p4] "s"(scv[3]), [p5] "s"(scv[4]), [p1] "s"(scv[5]), [rmin] "s"(scv[6]), [rcons] "s"(scv[7]), [pw] "s"(scv[8]),
                   [eps] "s"(eps), [small] "s"(small), [spin] "n"(SPIN_LIMIT), [tab] "n"(L_TAB), [xp] "n"(S_XP), [ov] "n"(S_OV), [hv] "n"(S_HV),
                   [osg] "n"(O_SG), [osh] "n"(O_SH), [oh] "n"(O_H), [oor] "n"(OOR), [qsw] "n"(L_FLAG + 4),
                   "{v[2:9]}"(kt[0]), "{v[10:17]}"(kt[1]), "{v[18:25]}"(kt[2]), "{v[26:33]}"(kt[3]), "{v[34:41]}"(kt[4]), "{v[42:49]}"(kt[5]),
                   "{v[50:57]}"(kt[6]), "{v[58:65]}"(kt[7]), "{v[66:73]}"(kt[8]), "{v[74:81]}"(kt[9]), "{v[82:89]}"(kt[10]), "{v[90:97]}"(kt[11]),
                   "{v[98:105]}"(vt[0]), "{v[106:113]}"(vt[1]), "{v[114:121]}"(vt[2]), "{v[122:129]}"(vt[3]), "{v[130:137]}"(vt[4]),
                   "{v[138:145]}"(vt[5]), "{v[146:153]}"(vt[6]), "{v[154:161]}"(vt[7]), "{v[162:169]}"(vt[8]), "{v[170:177]}"(vt[9]),
                   "{v[178:185]}"(vt[10]), "{v[186:193]}"(vt[11])
                 : "memory", "scc", "vcc",
                   "v194", "v195", "v196", "v197", "v198", "v199", "v200", "v201", "v202", "v203", "v204", "v205", "v206", "v207", "v208", "v209",
                   "v210", "v211", "v212", "v213", "v214", "v215", "v216", "v217", "v218", "v219", "v220", "v221", "v222", "v223", "v224", "v225",
                   "v226", "v227", "v228", "v229", "v230", "v231", "v232", "v233", "v234", "v235", "v236", "v237", "v238", "v239", "v240", "v241",
                   "v242", "v243", "v244", "v245", "v246", "v247", "s80", "s81", "s82", "s83", "s84", "s85", "s86", "s87", "s88", "s89", "s90", "s91",
                   "s92", "s93", "s94", "s95", "s96", "s97", "s98", "s99"
                 );
  }
  if (b0 == 8 && t16 == SW * 1024) __hip_atomic_store((gu32*)epoch, tag, RLX_AGENT);
}

extern "C" void kernel_launch(void* const* d_in, const int* in_sizes, int n_in, void* d_out, int out_size,
                              void* d_ws, size_t ws_size, hipStream_t stream) {
  (void)in_sizes; (void)n_in; (void)out_size; (void)ws_size;
  (void)hipFuncSetAttribute((const void*)mega_kernel, hipFuncAttributeMaxDynamicSharedMemorySize, L_TOTAL);
  mega_kernel<<<NWG, NT, L_TOTAL, stream>>>(
      (const uint16_t*)d_in[0], (const uint16_t*)d_in[1], (const uint16_t*)d_in[2], (const uint16_t*)d_in[3],
      (const uint16_t*)d_in[4], (const uint16_t*)d_in[6], (const uint16_t*)d_in[7], (const uint16_t*)d_in[8],
      (const uint16_t*)d_in[9], (uint16_t*)d_out, (unsigned*)d_ws
  );
}
```

```cpp
#include <hip/hip_runtime.h>
#include <hip/hip_bf16.h>
#include <stdint.h>

#define GAS __attribute__((address_space(1)))
#define LAS __attribute__((address_space(3)))
typedef GAS unsigned long long gu64;
typedef GAS unsigned gu32;
typedef uint32_t u32x2 __attribute__((ext_vector_type(2)));
typedef uint32_t u32x4 __attribute__((ext_vector_type(4)));
typedef uint32_t u32x8 __attribute__((ext_vector_type(8)));
typedef float f32x2 __attribute__((ext_vector_type(2)));
typedef float f32x4 __attribute__((ext_vector_type(4)));
typedef short s16x4 __attribute__((ext_vector_type(4)));
typedef __attribute__((ext_vector_type(2))) __bf16 bf16x2;
typedef __attribute__((ext_vector_type(8))) __bf16 bf16x8;
typedef __attribute__((ext_vector_type(16))) __bf16 bf16x16;
#define RLX_AGENT __ATOMIC_RELAXED, __HIP_MEMORY_SCOPE_AGENT

constexpr int DM = 640, NH = 32, DH = 192, SK = 32768, WIN = 4096, FF = 1536, QD = NH * DH;
constexpr int NWG = 256, NT = 512, NW = NT / 64;
#define SPINB 20
constexpr unsigned SPIN_LIMIT = 1u << SPINB;
constexpr int TW = 7, SW = 6;
constexpr int CODEPF = 15;
constexpr int UP = 3;
constexpr int nreq(int i0, int i1) { int n = 0; for (int i = i0; i <= i1 && i < 16; ++i) n += i == 15 ? 9 : (i == 0 || i > 8) ? 4 : 3; return n; }
constexpr int kord(int n) { return 2 * (n & 3) + (n >> 2); }
constexpr int vord(int n) { return (n & 4) + 2 * (n & 1) + ((n >> 1) & 1); }
constexpr int RT = UP + 1;
constexpr float LOG2E = 1.4426950408889634f;
constexpr float SMALL = 40.f;
constexpr uint32_t SENT = 0xffffffffu;
constexpr int OOR = 1 << 22;

constexpr int RP = 27 * 16;
constexpr int G_P = 0, G_R = G_P + NWG * RP, G_F = G_R + NH * RP;
constexpr int BLK = 16384, NB = 14, O_SG = 6144, O_SH = 6400, O_H = 6656, O_C = 32 * BLK, O_S = 48 * BLK, O_DB = O_S + 4 * NB * BLK, O_PR = O_DB + BLK;
constexpr int NVOTE = 32;

constexpr int QS = 400;
constexpr int RS = 800;
constexpr int S_XP = 0, S_OV = 256, S_HV = 768, S_QB = 896, S_ZQ = 1280, S_RED = 1472, S_ML = S_RED + NW * RS, S_B = S_ML + 128, S_END = S_B + 512;
constexpr int L_SM = 0, L_CS = L_SM + S_END, L_ONE = L_CS + 960, L_FLAG = L_ONE + 192, L_SCH = L_FLAG + 64, L_TAB = L_SCH + 64,
              L_WQ = L_TAB + 6 * NT * 16, L_WO = L_WQ + 32768, L_W2 = L_WO + 30720, L_W1 = L_W2 + 8192, L_TOTAL = L_W1 + 8192;
static_assert(L_TOTAL <= 160 * 1024 && L_TOTAL > 80 * 1024 && S_END % 16 == 0, "LDS");

enum { P3E, P2, P3, P4, P5, P1, RMIN, RCONS, CPAR, PO, PT, PA, PB, PC, PD, PW, NSCH };
#define SCHED_INIT {120, 125, 58, 61, 57, 67, 5, 50, 2, 410, 88, 620, 88, 470, 90, 6}
constexpr int sched[NSCH] = SCHED_INIT;
#define SC0(i) sched[i]
#define SC(i) sched[i]

__device__ __forceinline__ float lo2f(uint32_t p) { return __uint_as_float(p << 16); }
__device__ __forceinline__ float hi2f(uint32_t p) { return __uint_as_float(p & 0xffff0000u); }
__device__ __forceinline__ uint32_t pk2(float a, float b) {
  f32x2 v = {a, b};
  return __builtin_bit_cast(uint32_t, __builtin_convertvector(v, bf16x2));
}
__device__ __forceinline__ float rbf(float f) { return __uint_as_float(pk2(0.f, f)); }
template <int CTRL> __device__ __forceinline__ float dppv(float v) {
  return __int_as_float(__builtin_amdgcn_update_dpp(0, __float_as_int(v), CTRL, 0xf, 0xf, false));
}
template <int CTRL, int RM> __device__ __forceinline__ float dpp_add(float v) {
  return v + __int_as_float(__builtin_amdgcn_update_dpp(0, __float_as_int(v), CTRL, RM, 0xf, CTRL < 0x140));
}
__device__ __forceinline__ float wsum63(float v) {
  v = dpp_add<0x111, 0xf>(v); v = dpp_add<0x112, 0xf>(v); v = dpp_add<0x114, 0xf>(v); v = dpp_add<0x118, 0xf>(v);
  v = dpp_add<0x142, 0xa>(v); v = dpp_add<0x143, 0xc>(v);
  return v;
}
__device__ __forceinline__ float wsum_bcast(float v) {
  return __int_as_float(__builtin_amdgcn_readlane(__float_as_int(wsum63(v)), 63));
}
__device__ __forceinline__ float swapadd32(float a, float b) {
  auto r = __builtin_amdgcn_permlane32_swap(__float_as_uint(a), __float_as_uint(b), false, false);
  return __uint_as_float(r[0]) + __uint_as_float(r[1]);
}
__device__ __forceinline__ float swapadd16(float a, float b) {
  auto r = __builtin_amdgcn_permlane16_swap(__float_as_uint(a), __float_as_uint(b), false, false);
  return __uint_as_float(r[0]) + __uint_as_float(r[1]);
}
__device__ __forceinline__ float ror8add(float v) { return v + dppv<0x128>(v); }
__device__ __forceinline__ float sum_b320(const u32x4& v) {
  float a, b, c;
  asm("v_add_f32_dpp %0, %3, %3 row_ror:8 row_mask:0xf bank_mask:0x3\n\tv_add_f32_dpp %1, %4, %4 row_ror:8 row_mask:0xf bank_mask:0x3\n\t"
      "v_add_f32_dpp %0, %5, %5 row_ror:8 row_mask:0xf bank_mask:0xc\n\tv_add_f32_dpp %1, %6, %6 row_ror:8 row_mask:0xf bank_mask:0xc\n\ts_nop 0\n\t"
      "v_add_f32_dpp %2, %0, %0 row_ror:12 row_mask:0xf bank_mask:0x5\n\tv_add_f32_dpp %2, %1, %1 row_ror:4 row_mask:0xf bank_mask:0xa\n\ts_nop 1\n\t"
      "v_add_f32_dpp %2, %2, %2 quad_perm:[1,0,3,2] row_mask:0xf bank_mask:0xf"
      : "=&v"(a), "=&v"(b), "=&v"(c) : "v"(v.x), "v"(v.y), "v"(v.z), "v"(v.w));
  return c;
}
template <int N> __device__ __forceinline__ uint32_t rbc(uint32_t v) { return __builtin_amdgcn_mov_dpp(v, 0x150 + N, 0xf, 0xf, false); }
template <int I> __device__ __forceinline__ u32x4 pset(uint32_t p) { return (u32x4){rbc<4 * I>(p), rbc<4 * I + 1>(p), rbc<4 * I + 2>(p), rbc<4 * I + 3>(p)}; }
#define ZERO4(d, a, b, c, e) asm volatile("v_mov_b64 v[" #a ":" #b "], 0\n\tv_mov_b64 v[" #c ":" #e "], 0" : "={v[" #a ":" #e "]}"(d))
__device__ __forceinline__ int opos(int k) { return ((k >> 1) & 1) * 192 + (k >> 6) * 64 + (k & 32) + (k & 8) * 2 + (k & 4) * 2 + ((k >> 4) & 1) * 4 + (k & 1) * 2; }
__device__ __forceinline__ void lds_barrier() { asm volatile("s_waitcnt lgkmcnt(0)\n\ts_barrier" ::: "memory"); }
__device__ __forceinline__ uint32_t tagbad(const u32x4& r, uint32_t tag) { return (r.y ^ tag) | (r.w ^ tag); }
__device__ __forceinline__ float uf(uint32_t u) { return __uint_as_float(u); }
__device__ __forceinline__ uint32_t rt32() {
  unsigned long long t;
  asm volatile("s_memrealtime %0\n\ts_waitcnt lgkmcnt(0)" : "=s"(t)::"memory");
  return (uint32_t)t;
}
__device__ __forceinline__ f32x4 mfma32(const u32x4& a, const u32x4& b, const f32x4& c) {
  return __builtin_amdgcn_mfma_f32_16x16x32_bf16(__builtin_bit_cast(bf16x8, a), __builtin_bit_cast(bf16x8, b), c, 0, 0, 0);
}
template <int H> __device__ __forceinline__ f32x4 smf(const u32x4& a, const u32x8& b, const f32x4& c, uint32_t ix) {
  return __builtin_amdgcn_smfmac_f32_16x16x64_bf16(__builtin_bit_cast(bf16x8, a), __builtin_bit_cast(bf16x16, b), c, ix, 0, H);
}
__device__ __forceinline__ int qpos(int v) { return ((v >> 1) & 1) * 192 + (v >> 2) * 4 + (v & 1) * 2; }
__device__ __forceinline__ u32x4 cat(const u32x2& a, const u32x2& b) { return (u32x4){a.x, a.y, b.x, b.y}; }
__device__ __forceinline__ int kpos(int k) { return (k & ~31) | ((k & 12) << 1) | ((k & 16) >> 2) | (k & 3); }

#define PIN1(a) asm volatile("" : "+v"(a))
#define PINS(a) asm volatile("" : "+s"(a))
#define PINS2(a, c) asm volatile("" : "+s"(a), "+s"(c))
#define SB() do { asm volatile("" ::: "memory"); __builtin_amdgcn_sched_barrier(0); } while (0)
#define BAR0() asm volatile("s_barrier" ::: "memory")
template <int V> using IC = std::integral_constant<int, V>;

#define TAIL_ASM \
  "s_mov_b32 s80, 0\n\ts_mov_b32 s84, -1\n\ts_getpc_b64 s[98:99]\n\t.LPC%=:\ns_add_u32 s98, s98, .LCLK%=-.LPC%=\n\ts_addc_u32 s99, s99, 0\n\t.LLOOP%=:\n" \
  "s_cmp_gt_u32 %[w], 4\n\ts_cbranch_scc1 .LH2X%=\n\tds_read_b64 v[240:241], %[t16] offset:%[tab]+8192\n\ts_mov_b32 s81, %[oG]\n\t" \
  "s_add_u32 s82, s81, %[osg]\n\ts_mov_b32 s83, %[eC]\n\tv_and_b32 v242, 16, %[t16]\n\tv_mov_b64 v[232:233], 0\n\tv_mov_b64 v[234:235], 0\n\t" \
  "v_mad_u32_u24 v242, v242, 12, %[bo]\n\ts_cmp_eq_u32 s80, 0\n\ts_cbranch_scc1 .LH2A%=\n\ts_waitcnt lgkmcnt(0)\n\t" \
  "ds_read_b64_tr_b16 v[204:205], v241\n\tds_read_b64_tr_b16 v[206:207], v241 offset:2560\n\tds_read_b64_tr_b16 v[208:209], v241 offset:5120\n\t" \
  "ds_read_b64_tr_b16 v[210:211], v241 offset:7680\n\tds_read_b64_tr_b16 v[212:213], v241 offset:10240\n\t" \
  "ds_read_b64_tr_b16 v[214:215], v241 offset:12800\n\tds_read_b64_tr_b16 v[216:217], v241 offset:15360\n\t" \
  "ds_read_b64_tr_b16 v[218:219], v241 offset:17920\n\tds_read_b64_tr_b16 v[220:221], v241 offset:20480\n\t" \
  "ds_read_b64_tr_b16 v[222:223], v241 offset:23040\n\tds_read_b64_tr_b16 v[224:225], v241 offset:25600\n\t" \
  "ds_read_b64_tr_b16 v[226:227], v241 offset:28160\n\t.LH2A%=:\ns_cmp_gt_u32 %[w], 2\n\ts_cbranch_scc1 .LH2M%=\n\ts_barrier\n\t" \
  "buffer_load_dwordx2 v[202:203], %[ss], %[rs], s82 offen sc1\n\tbuffer_load_dwordx4 v[194:197], %[s0], %[rs], s81 offen sc1\n\t" \
  "buffer_load_dwordx4 v[198:201], %[s0], %[rs], s81 offen offset:128 sc1\n\ts_waitcnt vmcnt(2) lgkmcnt(0)\n\t.LRED2%=:\n" \
  "v_cmp_ne_u32 s[90:91], 0, v202\n\ts_waitcnt vmcnt(0)\n\tv_permlane32_swap_b32 v194, v195\n\tv_permlane32_swap_b32 v196, v197\n\t" \
  "v_add_f32_dpp v247, v203, v203 quad_perm:[1,0,3,2] row_mask:0xf bank_mask:0xf\n\tv_permlane32_swap_b32 v198, v199\n\t" \
  "v_permlane32_swap_b32 v200, v201\n\tv_add_f32 v194, v194, v195\n\tv_add_f32 v196, v196, v197\n\t" \
  "v_add_f32_dpp v247, v247, v247 quad_perm:[2,3,0,1] row_mask:0xf bank_mask:0xf\n\tv_add_f32 v198, v198, v199\n\tv_add_f32 v200, v200, v201\n\t" \
  "v_permlane16_swap_b32 v194, v196\n\tv_add_f32_dpp v247, v247, v247 row_half_mirror row_mask:0xf bank_mask:0xf\n\tv_permlane16_swap_b32 v198, v200\n\t" \
  "v_add_f32 v194, v194, v196\n\tv_add_f32 v198, v198, v200\n\tv_rcp_f32 v247, v247\n\t" \
  "v_add_f32_dpp v246, v194, v194 row_ror:8 row_mask:0xf bank_mask:0x3\n\tv_add_f32_dpp v246, v198, v198 row_ror:8 row_mask:0xf bank_mask:0xc\n\t" \
  "v_mul_f32 v246, v246, v247\n\tv_cvt_pk_bf16_f32 v247, v246, v246\n\tds_write_b16 v240, v247\n\tv_cmp_u_f32 s[92:93], v246, v246\n\t" \
  "s_or_b64 s[90:91], s[90:91], s[92:93]\n\ts_cbranch_scc1 .LSL2%=\n\t.LH2B%=:\ns_cmp_eq_u32 s80, 0\n\ts_cbranch_scc1 .LWO0%=\n\ts_waitcnt lgkmcnt(0)\n\t" \
  "s_barrier\n\tds_read_b128 v[194:197], v242 offset:%[ov]\n\tds_read_b128 v[198:201], v242 offset:%[ov]+64\n\t" \
  "ds_read_b128 v[228:231], v242 offset:%[ov]+128\n\ts_waitcnt lgkmcnt(2)\n\tv_smfmac_f32_16x16x64_bf16 v[232:235], v[194:197], v[204:211], %[ix]\n\t" \
  "s_waitcnt lgkmcnt(1)\n\tv_smfmac_f32_16x16x64_bf16 v[232:235], v[198:201], v[212:219], %[ix]\n\ts_waitcnt lgkmcnt(0)\n\t" \
  "v_smfmac_f32_16x16x64_bf16 v[232:235], v[228:231], v[220:227], %[ix]\n\t.LWOE%=:\ns_nop 7\n\tv_add_f32 v232, v232, v233\n\t" \
  "buffer_store_dword v232, %[dt], %[rs], s83 offen sc1\n\tds_read_b64 v[240:241], %[t16] offset:%[tab]+8192+8\n\t" \
  "ds_read_b32 %[s0], %[t16] offset:%[tab]+16384+12\n\tds_read_b32 v242, %[t16] offset:%[tab]+32768\n\ts_mov_b32 s81, %[rC]\n\ts_mov_b32 s83, %[eH]\n\t" \
  "v_mov_b64 v[214:215], 0\n\ts_waitcnt lgkmcnt(2)\n\tds_read_b64_tr_b16 v[204:205], v241\n\tds_read_b64_tr_b16 v[206:207], v241 offset:1536\n\t" \
  "ds_read_b64_tr_b16 v[208:209], v241 offset:3072\n\tds_read_b64_tr_b16 v[210:211], v241 offset:4608\n\t" \
  "ds_read_b64_tr_b16 v[212:213], v241 offset:6144\n\ts_waitcnt lgkmcnt(0)\n\ts_barrier\n\t" \
  "buffer_load_dwordx4 v[194:197], %[t16], %[rs], s81 offen sc1\n\tbuffer_load_dwordx4 v[198:201], %[t16], %[rs], s81 offen offset:1024 sc1\n\t" \
  "s_waitcnt vmcnt(0)\n\t.LRED3%=:\nv_pk_add_f32 v[194:195], v[194:195], v[198:199]\n\tv_pk_add_f32 v[196:197], v[196:197], v[200:201]\n\ts_nop 0\n\t" \
  "v_permlane32_swap_b32 v194, v195\n\tv_permlane32_swap_b32 v196, v197\n\tv_add_f32 v194, v194, v195\n\tv_add_f32 v196, v196, v197\n\ts_nop 1\n\t" \
  "v_permlane16_swap_b32 v194, v196\n\tv_add_f32 v194, v194, v196\n\ts_nop 1\n\tv_add_f32_dpp v194, v194, v194 row_ror:8 row_mask:0xf bank_mask:0xf\n\t" \
  "s_nop 1\n\tv_add_f32_dpp v246, v194, v194 row_ror:4 row_mask:0xf bank_mask:0xf\n\tv_cvt_pk_bf16_f32 v247, 0, v246\n\tv_add_f32 v247, v242, v247\n\t" \
  "v_cvt_pk_bf16_f32 v243, v247, 0\n\tds_write_b16 v240, v243\n\tv_cmp_u_f32 vcc, v246, v246\n\ts_cbranch_vccnz .LSL3%=\n\t.LH3B%=:\n" \
  "s_bitcmp0_b32 11, %[w]\n\ts_cbranch_scc1 .LH3M%=\n\ts_waitcnt lgkmcnt(0)\n\ts_barrier\n\tds_read_b128 v[194:197], %[bo] offset:%[xp]\n\t" \
  "ds_read_b128 v[198:201], %[bo] offset:%[xp]+64\n\tds_read_b128 v[228:231], %[bo] offset:%[xp]+128\n\ts_waitcnt lgkmcnt(2)\n\t" \
  "v_mfma_f32_16x16x32_bf16 v[232:235], v[194:197], v[204:207], 0\n\ts_waitcnt lgkmcnt(1)\n\t" \
  "v_mfma_f32_16x16x32_bf16 v[232:235], v[198:201], v[208:211], v[232:235]\n\ts_waitcnt lgkmcnt(0)\n\t" \
  "v_mfma_f32_16x16x32_bf16 v[232:235], v[228:231], v[212:215], v[232:235]\n\ts_nop 7\n\tbuffer_store_dword v232, %[dt], %[rs], s83 offen sc1\n\t" \
  ".LH3E%=:\nv_lshlrev_b32 v243, 16, v243\n\tds_write_b32 %[t16], v243 offset:%[tab]+32768\n\tds_read_b128 v[240:243], %[t16] offset:%[tab]+16384\n\t" \
  "ds_read_b32 v244, %[t16] offset:%[tab]+32768+8\n\ts_add_u32 s82, %[oG], %[osh]\n\ts_add_u32 s81, %[oG], %[oh]\n\ts_sub_u32 s85, %[dE], 0x28000\n\t" \
  "s_cmp_eq_u32 s80, 0\n\ts_cselect_b32 s85, s85, 0x8000\n\ts_add_u32 s83, %[eC], s85\n\tv_mov_b64 v[210:211], 0\n\tv_mov_b32 v245, 0xc0135761\n\t" \
  "s_waitcnt lgkmcnt(1)\n\tds_read_b64_tr_b16 v[204:205], v241\n\tds_read_b64_tr_b16 v[206:207], v241 offset:2560\n\t" \
  "ds_read_b64_tr_b16 v[208:209], v241 offset:5120\n\tds_read_b32 v242, v242\n\ts_cmp_gt_u32 %[w], 1\n\ts_cbranch_scc1 .LH4M%=\n\t" \
  "s_waitcnt lgkmcnt(0)\n\ts_barrier\n\tbuffer_load_dwordx2 v[202:203], %[ss], %[rs], s82 offen sc1\n\t" \
  "buffer_load_dwordx4 v[194:197], %[s0], %[rs], s81 offen sc1\n\ts_waitcnt vmcnt(1)\n\t.LRED4%=:\n" \
  "v_add_f32_dpp v198, v202, v202 quad_perm:[1,0,3,2] row_mask:0xf bank_mask:0xf\n\t" \
  "v_add_f32_dpp v199, v203, v203 quad_perm:[1,0,3,2] row_mask:0xf bank_mask:0xf\n\ts_waitcnt vmcnt(0)\n\tv_permlane32_swap_b32 v194, v195\n\t" \
  "v_add_f32_dpp v198, v198, v198 quad_perm:[2,3,0,1] row_mask:0xf bank_mask:0xf\n\t" \
  "v_add_f32_dpp v199, v199, v199 quad_perm:[2,3,0,1] row_mask:0xf bank_mask:0xf\n\tv_permlane32_swap_b32 v196, v197\n\tv_add_f32 v194, v194, v195\n\t" \
  "v_add_f32_dpp v198, v198, v198 row_half_mirror row_mask:0xf bank_mask:0xf\n\t" \
  "v_add_f32_dpp v199, v199, v199 row_half_mirror row_mask:0xf bank_mask:0xf\n\tv_add_f32 v196, v196, v197\n\tv_mul_f32 v199, 0x3acccccd, v199\n\t" \
  "v_mul_f32 v198, 0x3acccccd, v198\n\tv_permlane16_swap_b32 v194, v196\n\tv_fma_f32 v198, -v199, v199, v198\n\tv_add_f32 v194, v194, v196\n\t" \
  "v_cvt_pk_bf16_f32 v198, 0, v198\n\tv_cvt_pk_bf16_f32 v199, 0, v199\n\tv_add_f32 v198, |v198|, %[eps]\n\t" \
  "v_add_f32_dpp v194, v194, v194 row_ror:8 row_mask:0xf bank_mask:0xf\n\tv_cvt_pk_bf16_f32 v198, 0, v198\n\tv_sqrt_f32 v198, v198\n\t" \
  "v_fma_f32 v194, -v199, v242, v194\n\tv_cvt_pk_bf16_f32 v198, 0, v198\n\tv_rcp_f32 v198, v198\n\ts_nop 0\n\tv_mul_f32 v246, v194, v198\n\t" \
  "v_cvt_pk_bf16_f32 v247, 0, v246\n\tv_add_f32 v247, v244, v247\n\tv_cvt_pk_bf16_f32 v247, 0, v247\n\tv_mul_f32 v198, v247, v247\n\t" \
  "v_fmamk_f32 v198, v198, 0xbdd2d3e7, v245\n\tv_mul_f32 v198, v198, v247\n\tv_exp_f32 v198, v198\n\tv_cmp_u_f32 vcc, v246, v246\n\t" \
  "v_add_f32 v198, 1.0, v198\n\tv_rcp_f32 v198, v198\n\ts_nop 0\n\tv_mul_f32 v198, v198, v247\n\tv_cvt_pk_bf16_f32 v198, v198, v198\n\t" \
  "ds_write_b16 v240, v198\n\ts_cbranch_vccnz .LSL4%=\n\t.LH4B%=:\ns_waitcnt lgkmcnt(0)\n\ts_barrier\n\tds_read_b128 v[194:197], %[bo] offset:%[hv]\n\t" \
  "ds_read_b128 v[198:201], %[bo] offset:%[hv]+64\n\ts_waitcnt lgkmcnt(1)\n\tv_mfma_f32_16x16x32_bf16 v[232:235], v[194:197], v[204:207], 0\n\t" \
  "s_waitcnt lgkmcnt(0)\n\tv_mfma_f32_16x16x32_bf16 v[232:235], v[198:201], v[208:211], v[232:235]\n\ts_nop 7\n\t" \
  "buffer_store_dword v232, %[dt], %[rs], s83 offen sc1\n\t.LH4E%=:\ns_and_b32 s88, s80, %[far]\n\ts_cbranch_scc1 .LEND%=\n\t" \
  "ds_read_b64 v[240:241], %[t16] offset:%[tab]+24576+8\n\tds_read_b32 v242, %[t16] offset:%[tab]+8192+8\n\t" \
  "ds_read_b64 v[244:245], %[t16] offset:%[tab]+32768\n\ts_sub_u32 s85, %[dR], 0x28000\n\ts_cmp_eq_u32 s80, 0\n\ts_cselect_b32 s85, s85, 0x8000\n\t" \
  "s_add_u32 s81, %[rC], s85\n\ts_mov_b32 s83, %[eQ]\n\tv_mov_b64 v[214:215], 0\n\tv_mov_b64 v[226:227], 0\n\ts_waitcnt lgkmcnt(2)\n\t" \
  "ds_read_b64_tr_b16 v[204:205], v240\n\tds_read_b64_tr_b16 v[216:217], v240 offset:32\n\tds_read_b64_tr_b16 v[206:207], v240 offset:6400\n\t" \
  "ds_read_b64_tr_b16 v[218:219], v240 offset:6432\n\tds_read_b64_tr_b16 v[208:209], v240 offset:12800\n\t" \
  "ds_read_b64_tr_b16 v[220:221], v240 offset:12832\n\tds_read_b64_tr_b16 v[210:211], v240 offset:19200\n\t" \
  "ds_read_b64_tr_b16 v[222:223], v240 offset:19232\n\tds_read_b64_tr_b16 v[212:213], v240 offset:25600\n\t" \
  "ds_read_b64_tr_b16 v[224:225], v240 offset:25632\n\ts_cmp_gt_u32 %[w], 4\n\ts_cbranch_scc1 .LH5M%=\n\ts_waitcnt lgkmcnt(0)\n\ts_barrier\n\t" \
  "buffer_load_dwordx4 v[194:197], %[t16], %[rs], s81 offen sc1\n\tbuffer_load_dwordx4 v[198:201], %[t16], %[rs], s81 offen offset:1024 sc1\n\t" \
  "s_waitcnt vmcnt(0)\n\t.LRED5%=:\nv_pk_add_f32 v[194:195], v[194:195], v[198:199]\n\tv_pk_add_f32 v[196:197], v[196:197], v[200:201]\n\ts_nop 0\n\t" \
  "v_permlane32_swap_b32 v194, v195\n\tv_permlane32_swap_b32 v196, v197\n\tv_add_f32 v194, v194, v195\n\tv_add_f32 v196, v196, v197\n\ts_nop 1\n\t" \
  "v_permlane16_swap_b32 v194, v196\n\tv_add_f32 v194, v194, v196\n\ts_nop 1\n\tv_add_f32_dpp v194, v194, v194 row_ror:8 row_mask:0xf bank_mask:0xf\n\t" \
  "s_nop 1\n\tv_add_f32_dpp v246, v194, v194 row_ror:4 row_mask:0xf bank_mask:0xf\n\tv_cvt_pk_bf16_f32 v247, 0, v246\n\tv_cmp_u_f32 vcc, v246, v246\n\t" \
  "v_add_f32 v247, v245, v247\n\tv_cvt_pk_bf16_f32 v247, 0, v247\n\tv_add_f32 v247, v244, v247\n\tv_cvt_pk_bf16_f32 v243, v247, 0\n\t" \
  "ds_write_b16 v242, v243\n\ts_cbranch_vccnz .LSL5%=\n\t.LH5B%=:\ns_cmp_lg_u32 s80, 0\n\ts_cbranch_scc1 .LFIN%=\n\t.LH5C%=:\ns_waitcnt lgkmcnt(0)\n\t" \
  "s_barrier\n\tds_read_b128 v[194:197], %[bo] offset:%[xp]\n\tds_read_b128 v[198:201], %[bo] offset:%[xp]+64\n\t" \
  "ds_read_b128 v[228:231], %[bo] offset:%[xp]+128\n\ts_waitcnt lgkmcnt(2)\n\tv_mfma_f32_16x16x32_bf16 v[232:235], v[194:197], v[204:207], 0\n\t" \
  "v_mfma_f32_16x16x32_bf16 v[236:239], v[194:197], v[216:219], 0\n\ts_waitcnt lgkmcnt(1)\n\t" \
  "v_mfma_f32_16x16x32_bf16 v[232:235], v[198:201], v[208:211], v[232:235]\n\tv_mfma_f32_16x16x32_bf16 v[236:239], v[198:201], v[220:223], v[236:239]\n\t" \
  "s_waitcnt lgkmcnt(0)\n\tv_mfma_f32_16x16x32_bf16 v[232:235], v[228:231], v[212:215], v[232:235]\n\t" \
  "v_mfma_f32_16x16x32_bf16 v[236:239], v[228:231], v[224:227], v[236:239]\n\ts_nop 6\n\tbuffer_store_dword v232, %[dt], %[rs], s83 offen sc1\n\t" \
  "buffer_store_dword v236, %[dt], %[rs], s83 offen offset:64 sc1\n\tv_lshlrev_b32 v243, 16, v243\n\tds_write_b32 %[t16], v243 offset:%[tab]+32768\n\t" \
  ".LH1%=:\nds_read_b64 v[204:205], %[t16] offset:%[tab]+24576\n\tds_read_b128 v[240:243], %[t16] offset:%[tab]\n\t" \
  "ds_read_b128 v[236:239], %[t16] offset:%[tab]+40960\n\ts_add_u32 %[oG], %[oG], %[dG]\n\ts_add_u32 %[eH], %[eH], %[dH]\n\t" \
  "s_add_u32 %[rC], %[rC], %[dR]\n\ts_add_u32 %[eC], %[eC], %[dE]\n\ts_sub_u32 s81, %[oG], 0x10000\n\ts_add_u32 s82, s81, %[osg]\n\t" \
  "s_mov_b32 s92, 0x00ff00ff\n\ts_mov_b32 s93, 0xff00ff00\n\tv_mov_b64 v[208:209], 0\n\tv_mov_b64 v[210:211], 0\n\tv_mov_b64 v[212:213], 0\n\t" \
  "v_mov_b64 v[214:215], 0\n\tv_mov_b64 v[216:217], 0\n\tv_mov_b64 v[218:219], 0\n\tv_mov_b64 v[220:221], 0\n\tv_mov_b64 v[222:223], 0\n\t" \
  "v_mov_b64 v[224:225], 0\n\tv_mov_b64 v[226:227], 0\n\tv_mov_b64 v[228:229], 0\n\tv_mov_b64 v[230:231], 0\n\tv_mov_b64 v[232:233], 0\n\t" \
  "v_mov_b64 v[234:235], 0\n\ts_waitcnt lgkmcnt(2)\n\tds_read_b32 v205, v205\n\ts_cmp_gt_u32 %[w], 2\n\ts_cbranch_scc1 .LH1X%=\n\t" \
  "s_waitcnt lgkmcnt(0)\n\ts_barrier\n\tbuffer_load_dwordx2 v[202:203], %[ss], %[rs], s82 offen sc1\n\t" \
  "buffer_load_dwordx4 v[194:197], v243, %[rs], s81 offen sc1\n\tbuffer_load_dwordx4 v[198:201], v243, %[rs], s81 offen offset:128 sc1\n\t" \
  "s_waitcnt vmcnt(0)\n\t.LRED1%=:\nv_permlane32_swap_b32 v194, v195\n\tv_permlane32_swap_b32 v196, v197\n\t" \
  "v_add_f32_dpp v206, v203, v203 quad_perm:[1,0,3,2] row_mask:0xf bank_mask:0xf\n\tv_permlane32_swap_b32 v198, v199\n\t" \
  "v_permlane32_swap_b32 v200, v201\n\tv_add_f32 v194, v194, v195\n\tv_add_f32 v196, v196, v197\n\t" \
  "v_add_f32_dpp v206, v206, v206 quad_perm:[2,3,0,1] row_mask:0xf bank_mask:0xf\n\tv_add_f32 v198, v198, v199\n\tv_add_f32 v200, v200, v201\n\t" \
  "v_permlane16_swap_b32 v194, v196\n\tv_add_f32_dpp v206, v206, v206 row_half_mirror row_mask:0xf bank_mask:0xf\n\tv_permlane16_swap_b32 v198, v200\n\t" \
  "v_add_f32 v194, v194, v196\n\tv_add_f32 v198, v198, v200\n\tv_mul_f32 v206, 0x3acccccd, v206\n\t" \
  "v_add_f32_dpp v246, v194, v194 row_ror:8 row_mask:0xf bank_mask:0x3\n\tv_add_f32_dpp v246, v198, v198 row_ror:8 row_mask:0xf bank_mask:0xc\n\t" \
  "v_cvt_pk_bf16_f32 v247, 0, v206\n\tv_fma_f32 v246, -v247, v205, v246\n\tv_cvt_pk_bf16_f32 v247, v246, v246\n\tds_write_b16 v204, v247\n\t" \
  "v_cmp_u_f32 vcc, v246, v246\n\ts_cbranch_vccnz .LSL1%=\n\t.LH1B%=:\ns_waitcnt lgkmcnt(0)\n\ts_barrier\n\tds_read_b128 v[194:197], v236\n\t" \
  "ds_read_b128 v[198:201], v236 offset:64\n\tds_read_b128 v[204:207], v236 offset:128\n\tds_read_b32 v202, v239 offset:%[qsw]\n\t" \
  "s_waitcnt lgkmcnt(3)\n\tv_smfmac_f32_16x16x64_bf16 v[208:211], v[194:197], v[2:9], %[ix]\n\t" \
  "v_smfmac_f32_16x16x64_bf16 v[212:215], v[194:197], v[26:33], %[ix]\n\tv_smfmac_f32_16x16x64_bf16 v[216:219], v[194:197], v[50:57], %[ix]\n\t" \
  "v_smfmac_f32_16x16x64_bf16 v[220:223], v[194:197], v[74:81], %[ix]\n\ts_waitcnt lgkmcnt(2)\n\t" \
  "v_smfmac_f32_16x16x64_bf16 v[208:211], v[198:201], v[10:17], %[ix]\n\tv_smfmac_f32_16x16x64_bf16 v[212:215], v[198:201], v[34:41], %[ix]\n\t" \
  "v_smfmac_f32_16x16x64_bf16 v[216:219], v[198:201], v[58:65], %[ix]\n\tv_smfmac_f32_16x16x64_bf16 v[220:223], v[198:201], v[82:89], %[ix]\n\t" \
  "s_waitcnt lgkmcnt(1)\n\tv_smfmac_f32_16x16x64_bf16 v[208:211], v[204:207], v[18:25], %[ix]\n\t" \
  "v_smfmac_f32_16x16x64_bf16 v[212:215], v[204:207], v[42:49], %[ix]\n\tv_smfmac_f32_16x16x64_bf16 v[216:219], v[204:207], v[66:73], %[ix]\n\t" \
  "v_smfmac_f32_16x16x64_bf16 v[220:223], v[204:207], v[90:97], %[ix]\n\ts_setprio 1\n\ts_nop 3\n\tv_add_f32 v246, v208, v209\n\t" \
  "v_add_f32 v203, v212, v213\n\tv_cndmask_b32 v246, v203, v246, s[92:93]\n\tv_add_f32 v247, v216, v217\n\tv_add_f32 v203, v220, v221\n\t" \
  "v_cndmask_b32 v247, v203, v247, s[92:93]\n\ts_waitcnt lgkmcnt(0)\n\ts_nop 0\n\tv_permlane32_swap_b32 v246, v247\n\ts_nop 1\n\t" \
  "v_add_f32_dpp v246, v247, v246 row_ror:8 row_mask:0xf bank_mask:0xf\n\tv_mul_f32 v246, v246, v202\n\tv_cvt_pk_bf16_f32 v246, 0, v246\n\t" \
  "v_mul_f32 v246, 0x3d93cd3a, v246\n\tv_cvt_pk_bf16_f32 v246, 0, v246\n\tv_mul_f32 v246, 0x3fb8aa3b, v246\n\tv_exp_f32 v203, v246\n\t" \
  "v_cmp_nle_f32 vcc, |v246|, %[small]\n\tv_cvt_pk_bf16_f32 v244, v203, 0\n\tv_add_f32_dpp v203, v203, v203 row_ror:1 row_mask:0xf bank_mask:0xf\n\t" \
  "s_nop 0\n\tv_mov_b32_dpp v245, v244 row_shl:1 row_mask:0xf bank_mask:0xf\n\ts_cbranch_vccnz .LSLM%=\n\t.LPV%=:\ns_setprio 0\n\t" \
  "v_mov_b64_dpp v[208:209], v[244:245] row_newbcast:0 row_mask:0xf bank_mask:0xf\n\t" \
  "v_mov_b64_dpp v[210:211], v[244:245] row_newbcast:2 row_mask:0xf bank_mask:0xf\n\t" \
  "v_add_f32_dpp v203, v203, v203 row_ror:2 row_mask:0xf bank_mask:0xf\n\t" \
  "v_mov_b64_dpp v[212:213], v[244:245] row_newbcast:4 row_mask:0xf bank_mask:0xf\n\t" \
  "v_smfmac_f32_16x16x64_bf16 v[224:227], v[208:211], v[98:105], %[ix] abid:1\n\t" \
  "v_mov_b64_dpp v[214:215], v[244:245] row_newbcast:6 row_mask:0xf bank_mask:0xf\n\t" \
  "v_smfmac_f32_16x16x64_bf16 v[228:231], v[208:211], v[106:113], %[ix] abid:1\n\tv_add_f32_dpp v203, v203, v203 row_ror:4 row_mask:0xf bank_mask:0xf\n\t" \
  "v_smfmac_f32_16x16x64_bf16 v[232:235], v[208:211], v[114:121], %[ix] abid:1\n\t" \
  "v_smfmac_f32_16x16x64_bf16 v[224:227], v[212:215], v[122:129], %[ix] abid:1\n\t" \
  "v_mov_b64_dpp v[208:209], v[244:245] row_newbcast:8 row_mask:0xf bank_mask:0xf\n\t" \
  "v_mov_b64_dpp v[210:211], v[244:245] row_newbcast:10 row_mask:0xf bank_mask:0xf\n\t" \
  "v_smfmac_f32_16x16x64_bf16 v[228:231], v[212:215], v[130:137], %[ix] abid:1\n\tv_add_f32_dpp v203, v203, v203 row_ror:8 row_mask:0xf bank_mask:0xf\n\t" \
  "v_smfmac_f32_16x16x64_bf16 v[232:235], v[212:215], v[138:145], %[ix] abid:1\n\t" \
  "v_smfmac_f32_16x16x64_bf16 v[224:227], v[208:211], v[146:153], %[ix] abid:1\n\t" \
  "v_mov_b64_dpp v[212:213], v[244:245] row_newbcast:12 row_mask:0xf bank_mask:0xf\n\t" \
  "v_mov_b64_dpp v[214:215], v[244:245] row_newbcast:14 row_mask:0xf bank_mask:0xf\n\t" \
  "v_smfmac_f32_16x16x64_bf16 v[228:231], v[208:211], v[154:161], %[ix] abid:1\n\t" \
  "v_add_f32_dpp v203, v203, v203 row_bcast:15 row_mask:0xa bank_mask:0xf\n\t" \
  "v_smfmac_f32_16x16x64_bf16 v[232:235], v[208:211], v[162:169], %[ix] abid:1\n\t" \
  "v_smfmac_f32_16x16x64_bf16 v[224:227], v[212:215], v[170:177], %[ix] abid:1\n\t" \
  "v_smfmac_f32_16x16x64_bf16 v[228:231], v[212:215], v[178:185], %[ix] abid:1\n\t" \
  "v_smfmac_f32_16x16x64_bf16 v[232:235], v[212:215], v[186:193], %[ix] abid:1\n\t" \
  "v_add_f32_dpp v203, v203, v203 row_bcast:31 row_mask:0xc bank_mask:0xf\n\ts_lshl_b64 exec, 1, 63\n\tds_write2_b32 v238, v239, v203 offset1:1\n\t" \
  "s_mov_b64 exec, -1\n\ts_mov_b32 s80, 1\n\ts_mov_b32 s83, %[oD]\n\ts_nop 0\n\tds_write2st64_b32 v237, v224, v228 offset1:1\n\t" \
  "ds_write_b32 v237, v232 offset:512\n\ts_waitcnt lgkmcnt(0)\n\ts_barrier\n\tds_read_b128 v[194:197], v241\n\tds_read_b32 v204, v240\n\t" \
  "v_mov_b32 %[s0], v243\n\ts_waitcnt lgkmcnt(1)\n\t.LCMB%=:\nv_add_f32_dpp v205, v194, v194 row_ror:8 row_mask:0xf bank_mask:0x3\n\t" \
  "v_add_f32_dpp v206, v195, v195 row_ror:8 row_mask:0xf bank_mask:0x3\n\tv_add_f32_dpp v205, v196, v196 row_ror:8 row_mask:0xf bank_mask:0xc\n\t" \
  "v_add_f32_dpp v206, v197, v197 row_ror:8 row_mask:0xf bank_mask:0xc\n\ts_waitcnt lgkmcnt(0)\n\tv_cmp_ne_u32 vcc, 0, v204\n\t" \
  "v_add_f32_dpp v246, v205, v205 row_ror:12 row_mask:0xf bank_mask:0x5\n\tv_add_f32_dpp v246, v206, v206 row_ror:4 row_mask:0xf bank_mask:0xa\n\t" \
  "s_nop 1\n\tv_add_f32_dpp v246, v246, v246 quad_perm:[1,0,3,2] row_mask:0xf bank_mask:0xf\n\ts_cbranch_vccnz .LSLC%=\n\t.LCMS%=:\n" \
  "buffer_store_dword v246, v242, %[rs], s83 offen sc1\n\ts_branch .LLOOP%=\n\t.LWO0%=:\ns_waitcnt lgkmcnt(0)\n\ts_barrier\n\t" \
  "ds_read_b128 v[194:197], v242 offset:%[ov]+0\n\tds_read_b64_tr_b16 v[204:205], v241\n\tds_read_b64_tr_b16 v[206:207], v241 offset:2560\n\t" \
  "ds_read_b64_tr_b16 v[208:209], v241 offset:5120\n\tds_read_b64_tr_b16 v[210:211], v241 offset:7680\n\t" \
  "ds_read_b128 v[198:201], v242 offset:%[ov]+64\n\tds_read_b64_tr_b16 v[212:213], v241 offset:10240\n\t" \
  "ds_read_b64_tr_b16 v[214:215], v241 offset:12800\n\tds_read_b64_tr_b16 v[216:217], v241 offset:15360\n\t" \
  "ds_read_b64_tr_b16 v[218:219], v241 offset:17920\n\tds_read_b128 v[228:231], v242 offset:%[ov]+128\n\t" \
  "ds_read_b64_tr_b16 v[220:221], v241 offset:20480\n\tds_read_b64_tr_b16 v[222:223], v241 offset:23040\n\t" \
  "ds_read_b64_tr_b16 v[224:225], v241 offset:25600\n\tds_read_b64_tr_b16 v[226:227], v241 offset:28160\n\ts_waitcnt lgkmcnt(10)\n\t" \
  "v_smfmac_f32_16x16x64_bf16 v[232:235], v[194:197], v[204:211], %[ix]\n\ts_waitcnt lgkmcnt(5)\n\t" \
  "v_smfmac_f32_16x16x64_bf16 v[232:235], v[198:201], v[212:219], %[ix]\n\ts_waitcnt lgkmcnt(0)\n\t" \
  "v_smfmac_f32_16x16x64_bf16 v[232:235], v[228:231], v[220:227], %[ix]\n\ts_branch .LWOE%=\n\t.LH1X%=:\ns_cmp_eq_u32 %[w], 3\n\t" \
  "s_cbranch_scc1 .LH1S%=\n\ts_cmp_lg_u32 %[w], 7\n\ts_cbranch_scc1 .LH1Y%=\n\ts_mov_b32 s88, %[p1]\n\ts_swappc_b64 s[96:97], s[98:99]\n\t.LH1Y%=:\n" \
  "s_barrier\n\ts_branch .LH1B%=\n\t.LH1S%=:\ns_waitcnt lgkmcnt(0)\n\ts_barrier\n\tbuffer_load_dwordx2 v[202:203], %[ss], %[rs], s82 offen sc1\n\t" \
  "s_waitcnt vmcnt(0)\n\t.LREDS%=:\nv_add_f32_dpp v194, v202, v202 quad_perm:[1,0,3,2] row_mask:0xf bank_mask:0xf\n\t" \
  "v_add_f32_dpp v195, v203, v203 quad_perm:[1,0,3,2] row_mask:0xf bank_mask:0xf\n\ts_nop 0\n\t" \
  "v_add_f32_dpp v194, v194, v194 quad_perm:[2,3,0,1] row_mask:0xf bank_mask:0xf\n\t" \
  "v_add_f32_dpp v195, v195, v195 quad_perm:[2,3,0,1] row_mask:0xf bank_mask:0xf\n\ts_nop 0\n\t" \
  "v_add_f32_dpp v194, v194, v194 row_half_mirror row_mask:0xf bank_mask:0xf\n\t" \
  "v_add_f32_dpp v195, v195, v195 row_half_mirror row_mask:0xf bank_mask:0xf\n\tv_mul_f32 v194, 0x3acccccd, v194\n\tv_mul_f32 v195, 0x3acccccd, v195\n\t" \
  "v_fma_f32 v194, -v195, v195, v194\n\tv_cvt_pk_bf16_f32 v194, 0, v194\n\tv_add_f32 v194, |v194|, %[eps]\n\tv_cvt_pk_bf16_f32 v194, 0, v194\n\t" \
  "v_sqrt_f32 v194, v194\n\ts_nop 0\n\tv_cvt_pk_bf16_f32 v194, 0, v194\n\tv_rcp_f32 v194, v194\n\ts_nop 0\n\tds_write_b32 v239, v194 offset:%[qsw]\n\t" \
  "v_cmp_u_f32 vcc, v194, v194\n\ts_cbranch_vccz .LH1B%=\n\ts_branch .LSLS%=\n\t.LH2M%=:\ns_barrier\n\ts_waitcnt vmcnt(0)\n\ts_branch .LH2B%=\n\t" \
  ".LH3M%=:\ns_waitcnt lgkmcnt(0)\n\ts_barrier\n\ts_branch .LH3E%=\n\t.LH4M%=:\ns_barrier\n\ts_branch .LH4B%=\n\t.LH5M%=:\ns_barrier\n\t" \
  "s_cmp_eq_u32 s80, 0\n\ts_cbranch_scc1 .LH5C%=\n\ts_branch .LFE%=\n\t.LH2X%=:\ns_cmp_eq_u32 %[w], 7\n\ts_cbranch_scc1 .LTW%=\n\t" \
  "ds_read_b64 v[240:241], %[t16] offset:%[tab]+16384\n\tv_mov_b64 v[198:199], -1\n\tv_mov_b64 v[200:201], -1\n\ts_cmp_lg_u32 s80, 0\n\t" \
  "s_waitcnt lgkmcnt(0)\n\ts_barrier\n\ts_cbranch_scc1 .LH2Y%=\n\tbuffer_store_dwordx4 v[198:201], v240, %[rs], 0 offen sc1\n\t.LH2Y%=:\n" \
  "s_waitcnt vmcnt(0)\n\ts_barrier\n\ts_mov_b32 s83, %[eH]\n\ts_cmp_eq_u32 %[w], 6\n\ts_cbranch_scc1 .LSW3%=\n\ts_movk_i32 s88, 0x2000\n\t" \
  "s_cmp_lg_u32 s80, 0\n\ts_barrier\n\ts_barrier\n\ts_cbranch_scc1 .LH3Y%=\n\tbuffer_store_dwordx4 v[198:201], v241, %[rs], 0 offen sc1\n\t" \
  "buffer_store_dwordx4 v[198:201], v241, %[rs], s88 offen sc1\n\t.LH3Y%=:\ns_barrier\n\ts_waitcnt vmcnt(0)\n\ts_barrier\n\ts_branch .LH4E%=\n\t" \
  ".LSW3%=:\ns_barrier\n\ts_barrier\n\tds_read_b128 v[194:197], %[bo] offset:%[xp]\n\tds_read_b128 v[204:207], %[ss]\n\t" \
  "ds_read_b128 v[198:201], %[bo] offset:%[xp]+64\n\tds_read_b128 v[208:211], %[ss] offset:64\n\tds_read_b128 v[228:231], %[bo] offset:%[xp]+128\n\t" \
  "ds_read_b128 v[212:215], %[ss] offset:128\n\ts_waitcnt lgkmcnt(4)\n\tv_mfma_f32_16x16x32_bf16 v[232:235], v[194:197], v[204:207], 0\n\t" \
  "s_waitcnt lgkmcnt(2)\n\tv_mfma_f32_16x16x32_bf16 v[232:235], v[198:201], v[208:211], v[232:235]\n\ts_waitcnt lgkmcnt(0)\n\t" \
  "v_mfma_f32_16x16x32_bf16 v[232:235], v[228:231], v[212:215], v[232:235]\n\ts_nop 7\n\tbuffer_store_dword v232, %[dt], %[rs], s83 offen sc1\n\t" \
  "s_barrier\n\ts_barrier\n\ts_and_b32 s88, s80, %[far]\n\ts_cbranch_scc1 .LEND%=\n\ts_mov_b32 s83, %[eQ]\n\ts_barrier\n\ts_cmp_lg_u32 s80, 0\n\t" \
  "s_cbranch_scc1 .LFE%=\n\ts_barrier\n\tds_read_b128 v[194:197], %[bo] offset:%[xp]\n\tds_read_b128 v[204:207], %[ss]\n\t" \
  "ds_read_b128 v[198:201], %[bo] offset:%[xp]+64\n\tds_read_b128 v[208:211], %[ss] offset:64\n\tds_read_b128 v[228:231], %[bo] offset:%[xp]+128\n\t" \
  "ds_read_b128 v[212:215], %[ss] offset:128\n\ts_waitcnt lgkmcnt(4)\n\tv_mfma_f32_16x16x32_bf16 v[232:235], v[194:197], v[204:207], 0\n\t" \
  "s_waitcnt lgkmcnt(2)\n\tv_mfma_f32_16x16x32_bf16 v[232:235], v[198:201], v[208:211], v[232:235]\n\ts_waitcnt lgkmcnt(0)\n\t" \
  "v_mfma_f32_16x16x32_bf16 v[232:235], v[228:231], v[212:215], v[232:235]\n\ts_nop 7\n\tbuffer_store_dword v232, %[dt], %[rs], s83 offen sc1\n\t" \
  "s_branch .LH1%=\n\t.LTW%=:\ns_cmp_lg_u32 s80, 0\n\ts_cbranch_scc1 .LTW1%=\n\ts_memrealtime s[94:95]\n\tv_and_b32 v240, 0x3e0, %[t16]\n\t" \
  "v_lshlrev_b32 v241, 9, %[t16]\n\tv_and_b32 v241, 0x2000, v241\n\tv_cmp_eq_u32 vcc, 0, v240\n\tv_mov_b32 v240, %[oor]\n\t" \
  "v_cndmask_b32 v240, v240, v241, vcc\n\tv_mov_b32 v243, %[tag]\n\ts_waitcnt lgkmcnt(0)\n\ts_mov_b32 %[tc], s94\n\tv_mov_b32 v242, s94\n\t" \
  "buffer_store_dwordx2 v[242:243], v240, %[rs], %[cw] offen sc1\n\ts_waitcnt vmcnt(4)\n\ts_mov_b32 s87, %[pw]\n\t.LW1%=:\ns_sub_u32 s87, s87, 1\n\t" \
  "s_cbranch_scc0 .LW1%=\n\ts_barrier\n\ts_waitcnt vmcnt(1)\n\ts_barrier\n\ts_mov_b32 s87, %[spin]\n\t.LTWP%=:\n" \
  "buffer_load_dwordx4 v[194:197], %[t16], %[rs], %[rK] offen sc1\n\tbuffer_load_dwordx4 v[198:201], %[t16], %[rs], %[rK] offen offset:1024 sc1\n\t" \
  "s_waitcnt vmcnt(0)\n\tv_xor_b32 v247, v195, v243\n\tv_xor_b32 v246, v197, v243\n\tv_xor_b32 v199, v199, v243\n\tv_xor_b32 v201, v201, v243\n\t" \
  "v_or3_b32 v247, v247, v246, v199\n\tv_or_b32 v247, v247, v201\n\tv_cmp_ne_u32 vcc, 0, v247\n\ts_cbranch_vccz .LTWQ%=\n\ts_sleep 3\n\t" \
  "s_sub_u32 s87, s87, 1\n\ts_cbranch_scc0 .LTWP%=\n\t.LTWQ%=:\nv_subrev_u32 v194, %[tc], v194\n\tv_subrev_u32 v196, %[tc], v196\n\t" \
  "v_subrev_u32 v198, %[tc], v198\n\tv_subrev_u32 v200, %[tc], v200\n\tv_max_i32 v247, v194, v196\n\tv_max3_i32 v247, v198, v200, v247\n\ts_nop 1\n\t" \
  "v_max_i32_dpp v247, v247, v247 row_ror:1 row_mask:0xf bank_mask:0xf\n\ts_nop 1\n\t" \
  "v_max_i32_dpp v247, v247, v247 row_ror:2 row_mask:0xf bank_mask:0xf\n\ts_nop 1\n\t" \
  "v_max_i32_dpp v247, v247, v247 row_ror:4 row_mask:0xf bank_mask:0xf\n\ts_nop 1\n\t" \
  "v_max_i32_dpp v247, v247, v247 row_ror:8 row_mask:0xf bank_mask:0xf\n\ts_nop 1\n\t" \
  "v_max_i32_dpp v247, v247, v247 row_bcast:15 row_mask:0xa bank_mask:0xf\n\ts_nop 1\n\t" \
  "v_max_i32_dpp v247, v247, v247 row_bcast:31 row_mask:0xc bank_mask:0xf\n\ts_nop 1\n\tv_readlane_b32 s88, v247, 63\n\t" \
  "s_add_u32 %[tc], %[tc], %[p3e]\n\ts_add_u32 %[tc], %[tc], s88\n\ts_mov_b32 %[te], %[tc]\n\ts_add_u32 s94, s98, .LUNT%=-.LCLK%=\n\t" \
  "s_addc_u32 s95, s99, 0\n\ts_swappc_b64 s[96:97], s[94:95]\n\ts_branch .LTW3%=\n\t.LTW1%=:\ns_add_u32 %[tc], %[tc], %[p2]\n\ts_mov_b32 s87, %[pw]\n\t" \
  ".LW2%=:\ns_sub_u32 s87, s87, 1\n\ts_cbranch_scc0 .LW2%=\n\ts_barrier\n\ts_barrier\n\ts_mov_b32 s88, %[p3]\n\ts_swappc_b64 s[96:97], s[98:99]\n\t" \
  ".LTW3%=:\ns_barrier\n\ts_barrier\n\ts_mov_b32 s88, %[p4]\n\ts_swappc_b64 s[96:97], s[98:99]\n\ts_barrier\n\ts_barrier\n\t" \
  "s_and_b32 s88, s80, %[far]\n\ts_cbranch_scc1 .LEND%=\n\ts_mov_b32 s88, %[p5]\n\ts_swappc_b64 s[96:97], s[98:99]\n\ts_barrier\n\t" \
  "s_cmp_lg_u32 s80, 0\n\ts_cbranch_scc1 .LFE%=\n\ts_barrier\n\ts_branch .LH1%=\n\t.LCLK%=:\ns_memrealtime s[94:95]\n\ts_add_u32 %[tc], %[tc], s88\n\t" \
  "s_movk_i32 s87, 64\n\ts_waitcnt lgkmcnt(0)\n\t.LCL1%=:\ns_sub_i32 s88, %[tc], s94\n\ts_cmp_ge_i32 s88, %[rmin]\n\ts_cbranch_scc1 .LUNT%=\n\t" \
  "s_add_u32 %[tc], %[tc], %[rcons]\n\ts_sub_u32 s87, s87, 1\n\ts_cbranch_scc0 .LCL1%=\n\t.LUNT%=:\ns_movk_i32 s87, 0x1000\n\t.LUN1%=:\n" \
  "s_memrealtime s[94:95]\n\ts_sub_u32 s87, s87, 1\n\ts_cbranch_scc1 .LUN2%=\n\ts_waitcnt lgkmcnt(0)\n\ts_sub_i32 s88, %[tc], s94\n\t" \
  "s_cmp_gt_i32 s88, 3\n\ts_cbranch_scc1 .LUN1%=\n\ts_cmp_lt_i32 s88, 1\n\ts_cbranch_scc1 .LUN2%=\n\ts_nop 3\n\ts_cmp_lt_i32 s88, 2\n\t" \
  "s_cbranch_scc1 .LUN2%=\n\ts_nop 3\n\ts_cmp_lt_i32 s88, 3\n\ts_cbranch_scc1 .LUN2%=\n\ts_nop 9\n\t.LUN2%=:\ns_waitcnt lgkmcnt(0)\n\t" \
  "s_setpc_b64 s[96:97]\n\t.LSL2%=:\ns_add_u32 s88, s80, 2\n\ts_cmp_eq_u32 s84, s88\n\ts_cbranch_scc1 .LH2B%=\n\ts_mov_b32 s84, s88\n\t" \
  "s_mov_b32 s87, %[spin]\n\t.LSL2a%=:\nbuffer_load_dwordx2 v[202:203], %[ss], %[rs], s82 offen sc1\n\t" \
  "buffer_load_dwordx4 v[194:197], %[s0], %[rs], s81 offen sc1\n\tbuffer_load_dwordx4 v[198:201], %[s0], %[rs], s81 offen offset:128 sc1\n\t" \
  "s_waitcnt vmcnt(0)\n\tv_max3_u32 v247, v194, v195, v196\n\tv_max3_u32 v247, v247, v197, v198\n\tv_max3_u32 v247, v247, v199, v200\n\t" \
  "v_max3_u32 v247, v247, v201, v202\n\tv_max_u32 v247, v247, v203\n\tv_cmp_eq_u32 vcc, -1, v247\n\ts_cbranch_vccz .LSL2b%=\n\ts_sub_u32 s87, s87, 1\n\t" \
  "s_cbranch_scc0 .LSL2a%=\n\t.LSL2b%=:\nv_cmp_ne_u32 vcc, 0, v202\n\ts_cbranch_vccz .LRED2%=\n\tv_lshrrev_b32 v247, 5, %[t16]\n\t" \
  "v_and_b32 v247, 28, v247\n\tds_bpermute_b32 v247, v247, v202\n\tv_max_f32_dpp v246, v202, v202 quad_perm:[1,0,3,2] row_mask:0xf bank_mask:0xf\n\t" \
  "s_nop 1\n\tv_max_f32_dpp v246, v246, v246 quad_perm:[2,3,0,1] row_mask:0xf bank_mask:0xf\n\ts_nop 1\n\t" \
  "v_max_f32_dpp v246, v246, v246 row_half_mirror row_mask:0xf bank_mask:0xf\n\ts_waitcnt lgkmcnt(0)\n\tv_sub_f32 v247, v247, v246\n\t" \
  "v_sub_f32 v202, v202, v246\n\tv_exp_f32 v247, v247\n\tv_exp_f32 v202, v202\n\ts_nop 0\n\tv_mul_f32 v194, v247, v194\n\tv_mul_f32 v195, v247, v195\n\t" \
  "v_mul_f32 v196, v247, v196\n\tv_mul_f32 v197, v247, v197\n\tv_mul_f32 v198, v247, v198\n\tv_mul_f32 v199, v247, v199\n\tv_mul_f32 v200, v247, v200\n\t" \
  "v_mul_f32 v201, v247, v201\n\tv_mul_f32 v203, v202, v203\n\tv_mov_b32 v202, 0\n\ts_nop 1\n\ts_branch .LRED2%=\n\t.LSL3%=:\ns_add_u32 s88, s80, 4\n\t" \
  "s_cmp_eq_u32 s84, s88\n\ts_cbranch_scc1 .LH3B%=\n\ts_mov_b32 s84, s88\n\ts_mov_b32 s87, %[spin]\n\t.LSL3a%=:\n" \
  "buffer_load_dwordx4 v[194:197], %[t16], %[rs], s81 offen sc1\n\tbuffer_load_dwordx4 v[198:201], %[t16], %[rs], s81 offen offset:1024 sc1\n\t" \
  "s_waitcnt vmcnt(0)\n\tv_max3_u32 v247, v194, v195, v196\n\tv_max3_u32 v247, v247, v197, v198\n\tv_max3_u32 v247, v247, v199, v200\n\t" \
  "v_max_u32 v247, v247, v201\n\tv_cmp_eq_u32 vcc, -1, v247\n\ts_cbranch_vccz .LSL3b%=\n\ts_sleep 1\n\ts_sub_u32 s87, s87, 1\n\t" \
  "s_cbranch_scc0 .LSL3a%=\n\t.LSL3b%=:\ns_branch .LRED3%=\n\t.LSL4%=:\ns_add_u32 s88, s80, 6\n\ts_cmp_eq_u32 s84, s88\n\ts_cbranch_scc1 .LH4B%=\n\t" \
  "s_mov_b32 s84, s88\n\ts_mov_b32 s87, %[spin]\n\t.LSL4a%=:\nbuffer_load_dwordx2 v[202:203], %[ss], %[rs], s82 offen sc1\n\t" \
  "buffer_load_dwordx4 v[194:197], %[s0], %[rs], s81 offen sc1\n\ts_waitcnt vmcnt(0)\n\tv_max3_u32 v247, v194, v195, v196\n\t" \
  "v_max3_u32 v247, v247, v197, v202\n\tv_max_u32 v247, v247, v203\n\tv_cmp_eq_u32 vcc, -1, v247\n\ts_cbranch_vccz .LSL4b%=\n\ts_sleep 1\n\t" \
  "s_sub_u32 s87, s87, 1\n\ts_cbranch_scc0 .LSL4a%=\n\t.LSL4b%=:\ns_branch .LRED4%=\n\t.LSL5%=:\ns_add_u32 s88, s80, 8\n\ts_cmp_eq_u32 s84, s88\n\t" \
  "s_cbranch_scc1 .LH5B%=\n\ts_mov_b32 s84, s88\n\ts_mov_b32 s87, %[spin]\n\t.LSL5a%=:\nbuffer_load_dwordx4 v[194:197], %[t16], %[rs], s81 offen sc1\n\t" \
  "buffer_load_dwordx4 v[198:201], %[t16], %[rs], s81 offen offset:1024 sc1\n\ts_waitcnt vmcnt(0)\n\tv_max3_u32 v247, v194, v195, v196\n\t" \
  "v_max3_u32 v247, v247, v197, v198\n\tv_max3_u32 v247, v247, v199, v200\n\tv_max_u32 v247, v247, v201\n\tv_cmp_eq_u32 vcc, -1, v247\n\t" \
  "s_cbranch_vccz .LSL5b%=\n\ts_sleep 1\n\ts_sub_u32 s87, s87, 1\n\ts_cbranch_scc0 .LSL5a%=\n\t.LSL5b%=:\ns_branch .LRED5%=\n\t.LSL1%=:\n" \
  "s_add_u32 s88, s80, 10\n\ts_cmp_eq_u32 s84, s88\n\ts_cbranch_scc1 .LH1B%=\n\ts_mov_b32 s84, s88\n\ts_mov_b32 s87, %[spin]\n\t.LSL1a%=:\n" \
  "buffer_load_dwordx2 v[202:203], %[ss], %[rs], s82 offen sc1\n\tbuffer_load_dwordx4 v[194:197], v243, %[rs], s81 offen sc1\n\t" \
  "buffer_load_dwordx4 v[198:201], v243, %[rs], s81 offen offset:128 sc1\n\ts_waitcnt vmcnt(0)\n\tv_max3_u32 v247, v194, v195, v196\n\t" \
  "v_max3_u32 v247, v247, v197, v198\n\tv_max3_u32 v247, v247, v199, v200\n\tv_max3_u32 v247, v247, v201, v202\n\tv_max_u32 v247, v247, v203\n\t" \
  "v_cmp_eq_u32 vcc, -1, v247\n\ts_cbranch_vccz .LSL1b%=\n\ts_sleep 1\n\ts_sub_u32 s87, s87, 1\n\ts_cbranch_scc0 .LSL1a%=\n\t.LSL1b%=:\n" \
  "s_branch .LRED1%=\n\t.LSLS%=:\ns_add_u32 s88, s80, 10\n\ts_cmp_eq_u32 s84, s88\n\ts_cbranch_scc1 .LH1B%=\n\ts_mov_b32 s84, s88\n\t" \
  "s_mov_b32 s87, %[spin]\n\t.LSLSa%=:\nbuffer_load_dwordx2 v[202:203], %[ss], %[rs], s82 offen sc1\n\ts_waitcnt vmcnt(0)\n\t" \
  "v_max3_u32 v247, v202, v203, v203\n\tv_cmp_eq_u32 vcc, -1, v247\n\ts_cbranch_vccz .LSLSb%=\n\ts_sleep 1\n\ts_sub_u32 s87, s87, 1\n\t" \
  "s_cbranch_scc0 .LSLSa%=\n\t.LSLSb%=:\ns_branch .LREDS%=\n\t.LSLM%=:\nv_max_f32_dpp v247, v246, v246 row_ror:1 row_mask:0xf bank_mask:0xf\n\t" \
  "s_nop 1\n\tv_max_f32_dpp v247, v247, v247 row_ror:2 row_mask:0xf bank_mask:0xf\n\ts_nop 1\n\t" \
  "v_max_f32_dpp v247, v247, v247 row_ror:4 row_mask:0xf bank_mask:0xf\n\ts_nop 1\n\t" \
  "v_max_f32_dpp v247, v247, v247 row_ror:8 row_mask:0xf bank_mask:0xf\n\ts_nop 1\n\t" \
  "v_max_f32_dpp v247, v247, v247 row_bcast:15 row_mask:0xa bank_mask:0xf\n\ts_nop 1\n\t" \
  "v_max_f32_dpp v247, v247, v247 row_bcast:31 row_mask:0xc bank_mask:0xf\n\ts_nop 1\n\tv_readlane_b32 s88, v247, 63\n\ts_nop 3\n\t" \
  "v_mov_b32 v239, s88\n\tv_sub_f32 v246, v246, v239\n\tv_exp_f32 v203, v246\n\ts_nop 0\n\tv_cvt_pk_bf16_f32 v244, v203, 0\n\t" \
  "v_add_f32_dpp v203, v203, v203 row_ror:1 row_mask:0xf bank_mask:0xf\n\ts_nop 0\n\tv_mov_b32_dpp v245, v244 row_shl:1 row_mask:0xf bank_mask:0xf\n\t" \
  "s_branch .LPV%=\n\t.LSLC%=:\nv_max_f32_dpp v247, v204, v204 row_ror:8 row_mask:0xf bank_mask:0xf\n\ts_nop 1\n\t" \
  "v_max_f32_dpp v247, v247, v247 row_ror:4 row_mask:0xf bank_mask:0xf\n\ts_nop 1\n\t" \
  "v_max_f32_dpp v247, v247, v247 quad_perm:[1,0,3,2] row_mask:0xf bank_mask:0xf\n\tv_sub_f32 v204, v204, v247\n\tv_exp_f32 v204, v204\n\ts_nop 0\n\t" \
  "v_mul_f32 v194, v204, v194\n\tv_mul_f32 v195, v204, v195\n\tv_mul_f32 v196, v204, v196\n\tv_mul_f32 v197, v204, v197\n\ts_nop 0\n\t" \
  "v_add_f32_dpp v205, v194, v194 row_ror:8 row_mask:0xf bank_mask:0x3\n\tv_add_f32_dpp v206, v195, v195 row_ror:8 row_mask:0xf bank_mask:0x3\n\t" \
  "v_add_f32_dpp v205, v196, v196 row_ror:8 row_mask:0xf bank_mask:0xc\n\tv_add_f32_dpp v206, v197, v197 row_ror:8 row_mask:0xf bank_mask:0xc\n\t" \
  "s_nop 0\n\tv_add_f32_dpp v246, v205, v205 row_ror:12 row_mask:0xf bank_mask:0x5\n\t" \
  "v_add_f32_dpp v246, v206, v206 row_ror:4 row_mask:0xf bank_mask:0xa\n\ts_nop 1\n\t" \
  "v_add_f32_dpp v246, v246, v246 quad_perm:[1,0,3,2] row_mask:0xf bank_mask:0xf\n\ts_cmp_lg_u32 %[w], 6\n\ts_cbranch_scc1 .LCMS%=\n\t" \
  "v_cmp_eq_u32 vcc, 0, v242\n\tv_cndmask_b32 v246, v246, v247, vcc\n\ts_branch .LCMS%=\n\t.LFIN%=:\n" \
  "buffer_store_short v243, v241, %[ro], %[oo] offen\n\t.LFE%=:\n.LEND%=:\n"
static_assert(BLK == 16384 && NT == 512 && QS == 400 && L_SM == 0 && TW == 7 && SW == 6, "constants of the hand-written part");

__global__ void __launch_bounds__(NT, 2)
mega_kernel(const uint16_t* __restrict__ x_in, const uint16_t* __restrict__ kc, const uint16_t* __restrict__ vc,
            const uint16_t* __restrict__ wq, const uint16_t* __restrict__ wo, const uint16_t* __restrict__ w1,
            const uint16_t* __restrict__ b1, const uint16_t* __restrict__ w2, const uint16_t* __restrict__ b2,
            uint16_t* __restrict__ out, unsigned* epoch) {
  extern __shared__ __attribute__((aligned(16))) char lds[];
  uint32_t cp;
  const uint64_t code0 = __builtin_amdgcn_s_getpc() & ~(uint64_t)127;
  asm volatile("global_load_dword %0, %1, %2" : "=v"(cp) : "v"(min((int)((blockIdx.x >> 3) * 1024 + (threadIdx.x >> 6) * 128), CODEPF * 1024 - 128)),
               "s"(code0) : "memory");
  __builtin_amdgcn_sched_barrier(0);
  const int tid0 = threadIdx.x, w0 = __builtin_amdgcn_readfirstlane(tid0 >> 6);
  const int b0 = blockIdx.x;
  unsigned long long* gran_base = (unsigned long long*)epoch + 512;
  char* tail = (char*)(((uintptr_t)epoch + (4u << 20)) & ~(uintptr_t)((2u << 20) - 1));
  const auto rs = __builtin_amdgcn_make_buffer_rsrc(tail, 0, 128 * BLK, 0x00020000);
  auto st4 = [&](int voff, int so, float a) { __builtin_amdgcn_raw_buffer_store_b32(__float_as_uint(a), rs, voff, so, 16); };
  auto trd = [&](int off) -> u32x2 {
    return __builtin_bit_cast(u32x2, __builtin_amdgcn_ds_read_tr16_b64_v4i16((LAS s16x4*)(uintptr_t)(uint32_t)off));
  };
  auto lrd16 = [&](int off) -> u32x4 { return *(const LAS u32x4*)(uintptr_t)(uint32_t)off; };
  auto lrd4 = [&](int off) -> uint32_t { return *(const LAS uint32_t*)(uintptr_t)(uint32_t)off; };
  auto lwr2 = [&](int off, uint32_t v) { *(LAS uint16_t*)(uintptr_t)(uint32_t)off = (uint16_t)v; };
  auto lwr4 = [&](int off, uint32_t v) { *(LAS uint32_t*)(uintptr_t)(uint32_t)off = v; };
  constexpr int sm = L_SM;

  uint32_t tag;
  uint32_t tE = 0;
  uint32_t tcur = 0;
  u32x8 kt[12], vt[12];
  uint32_t ix;
  int t16, bo, sS, dT;

  auto qk_softmax = [&](float& mx, float& ev, f32x4 (&dv)[3], int qb, float qs) -> uint32_t {
    u32x4 qa[3];
    f32x4 d[4];
#pragma unroll
    for (int u = 0; u < 3; ++u) qa[u] = lrd16(qb + 64 * u);
#pragma unroll
    for (int a = 0; a < 1; ++a) { ZERO4(d[0], 204, 205, 206, 207); ZERO4(d[1], 208, 209, 210, 211); ZERO4(d[2], 212, 213, 214, 215); ZERO4(d[3], 216, 217, 218, 219); }
    SB();
#pragma unroll
    for (int u = 0; u < 3; ++u)
#pragma unroll
      for (int a = 0; a < 4; ++a) {
        d[a] = smf<0>(qa[u], kt[3 * a + u], d[a], ix);
        SB();
        if (u == 1 && a == 0) ZERO4(dv[0], 220, 221, 222, 223);
        if (u == 1 && a == 2) ZERO4(dv[1], 224, 225, 226, 227);
        if (u == 2 && a == 0) ZERO4(dv[2], 228, 229, 230, 231);
        SB();
      }
    __builtin_amdgcn_s_setprio(1);
    const bool same = __builtin_amdgcn_inverse_ballot_w64(0xff00ff0000ff00ffull);
    const float f0 = same ? d[0][0] + d[0][1] : d[1][0] + d[1][1], f1 = same ? d[2][0] + d[2][1] : d[3][0] + d[3][1];
    auto r = __builtin_amdgcn_permlane32_swap(__float_as_uint(f0), __float_as_uint(f1), false, false);
    const float sraw = uf(r[0]) + dppv<0x128>(uf(r[1]));
    const float sv = rbf(rbf(sraw) * 0.07216878364870322f) * LOG2E;
    uint64_t big = __builtin_amdgcn_ballot_w64(!(fabsf(sv) <= SMALL));
    PINS(big);
    SB();
    mx = 0.f;
    ev = __builtin_amdgcn_exp2f(sv);
    uint32_t pb = pk2(ev, 0.f);
    PIN1(pb);
    SB();
    if (__builtin_expect(big != 0, 0)) {
      float mxv;
      asm volatile("s_nop 1\n\t"
                   "v_max_f32_dpp %0, %1, %1 row_ror:1 row_mask:0xf bank_mask:0xf\n\ts_nop 1\n\t"
                   "v_max_f32_dpp %0, %0, %0 row_ror:2 row_mask:0xf bank_mask:0xf\n\ts_nop 1\n\t"
                   "v_max_f32_dpp %0, %0, %0 row_ror:4 row_mask:0xf bank_mask:0xf\n\ts_nop 1\n\t"
                   "v_max_f32_dpp %0, %0, %0 row_ror:8 row_mask:0xf bank_mask:0xf\n\ts_nop 1\n\t"
                   "v_max_f32_dpp %0, %0, %0 row_bcast:15 row_mask:0xa bank_mask:0xf\n\ts_nop 1\n\t"
                   "v_max_f32_dpp %0, %0, %0 row_bcast:31 row_mask:0xc bank_mask:0xf\n\ts_nop 0"
                   : "=&v"(mxv) : "v"(sv));
      mx = __int_as_float(__builtin_amdgcn_readlane(__float_as_int(mxv), 63));
      ev = __builtin_amdgcn_exp2f(sv - mx);
      pb = pk2(ev, 0.f);
    }
    __builtin_amdgcn_s_setprio(0);
    return pb;
  };
  auto pv_combine = [&](const u32x4& cT, int od, bool sw) {
    const uint32_t m = lrd4(cT.x);
    u32x4 rv = lrd16(cT.y);
    SB();
    uint64_t shifted = __builtin_amdgcn_ballot_w64(m != 0);
    PINS(shifted);
    SB();
    float o = sum_b320(rv);
    PIN1(o);
    SB();
    if (__builtin_expect(shifted != 0, 0)) {
      float M = fmaxf(uf(m), dppv<0x128>(uf(m)));
      M = fmaxf(M, dppv<0x124>(M));
      M = fmaxf(M, dppv<0xb1>(M));
      const float f = __builtin_amdgcn_exp2f(uf(m) - M);
      rv = __builtin_bit_cast(u32x4, __builtin_bit_cast(f32x4, rv) * f);
      asm volatile("s_nop 1" : "+v"(rv));
      o = sum_b320(rv);
      if (sw && (t16 & 0xc0) == 0) o = M;
    }
    st4(cT.z, od, o);
  };
  int s0;
  int oG, oD, rC, eC, eH, eQ, rK;
  int dG, dH, dR, dE;
  int oD1, cwo, oo, far, scv[9];
  u32x4 rs4, ro4;
  float eps = 1e-5f, small = SMALL;

  {
    const int w = w0, b = b0, lane = tid0 & 63;
    const int g = (b & 7) * 4 + (b >> 6), j = (b >> 3) & 7, jg = b >> 3, jj = b & 7;
    const int ck = lane & 7, jp = lane >> 3, n16 = lane & 15, kq = lane >> 4;
    const int sub = (lane >> 5) + 2 * ((lane >> 4) & 1), trl = 4 * kq + (n16 >> 2), trc = 8 * (lane & 3);
    const int xc = min(16 * w + 4 * (lane & 3) + sub, 79);
    const int gc = 64 * (w < 3 ? w : 0) + 4 * (lane & 8) + 4 * ck + sub, hc = (w & 1) ? 32 + 4 * (lane & 3) + sub : 4 * ck + sub;
    gu64* G = (gu64*)gran_base;
    const size_t row0 = (size_t)g * SK + (SK - WIN) + j * 64 + w * 8;
    const uint16_t *kp = kc + row0 * DH, *vp = vc + row0 * DH;
    const int rot = g, hx = (g >> 2) & 1;
    const int vo = ((kq >> 1) ^ hx) * (2048 * 384) + (kq & 1) * 384 + n16 * 16, vo2 = vo + 256 - n16 * 8;
    const int ko = ((lane & 4) + ((lane & 1) << 1) + ((lane >> 1) & 1)) * 384 + (lane & 8) * 8 + kq * 16, ko2 = ko ^ 64;
#define GRP(lo, hi) "+{v[" #lo ":" #hi "]}"
#define LOADK(t, r0, r1, r2, g0, g1, g2) if constexpr (T == t) \
      asm volatile("global_load_dwordx4 " r0 ", %3, %4 nt\n\tglobal_load_dwordx4 " r1 ", %3, %4 offset:128 nt\n\t" \
                   "global_load_dwordx4 " r2 ", %3, %4 offset:256 nt" \
                   : g0(kt[a]), g1(kt[a + 1]), g2(kt[a + 2]) : "v"(o), "s"(p) : "memory")
    auto load_k = [&](auto Tc) {
      constexpr int T = decltype(Tc)::value, a = 3 * (T >> 1);
      const int o = (T & 2) ? ko2 : ko;
      const uint16_t* p = kp + ((((T >> 1) + rot) & 3) * 512 + ((T & 1) ^ hx) * 2048) * DH;
      LOADK(0, "v[2:5]", "v[10:13]", "v[18:21]", GRP(2, 9), GRP(10, 17), GRP(18, 25));
      LOADK(1, "v[6:9]", "v[14:17]", "v[22:25]", GRP(2, 9), GRP(10, 17), GRP(18, 25));
      LOADK(2, "v[26:29]", "v[34:37]", "v[42:45]", GRP(26, 33), GRP(34, 41), GRP(42, 49));
      LOADK(3, "v[30:33]", "v[38:41]", "v[46:49]", GRP(26, 33), GRP(34, 41), GRP(42, 49));
      LOADK(4, "v[50:53]", "v[58:61]", "v[66:69]", GRP(50, 57), GRP(58, 65), GRP(66, 73));
      LOADK(5, "v[54:57]", "v[62:65]", "v[70:73]", GRP(50, 57), GRP(58, 65), GRP(66, 73));
      LOADK(6, "v[74:77]", "v[82:85]", "v[90:93]", GRP(74, 81), GRP(82, 89), GRP(90, 97));
      LOADK(7, "v[78:81]", "v[86:89]", "v[94:97]", GRP(74, 81), GRP(82, 89), GRP(90, 97));
      SB();
    };
#define LOADV(v, x0, y0, x1, y1, g0, g1, g2) if constexpr (V == v) \
      asm volatile("global_load_dwordx4 " x0 ", %3, %5 nt\n\tglobal_load_dwordx2 " y0 ", %4, %5 nt\n\t" \
                   "global_load_dwordx4 " x1 ", %3, %5 offset:768 nt\n\tglobal_load_dwordx2 " y1 ", %4, %5 offset:768 nt" \
                   : g0(vt[a]), g1(vt[a + 1]), g2(vt[a + 2]) : "v"(vo), "v"(vo2), "s"(p) : "memory")
    auto load_v = [&](auto Vc) {
      constexpr int V = decltype(Vc)::value, a = 3 * (V >> 1);
      const uint16_t* p = vp + (((2 * (V & 1) + (V >> 2) + rot) & 3) * 512 + ((V >> 1) & 1) * 4) * DH;
      LOADV(0, "v[98:101]", "v[114:115]", "v[106:109]", "v[116:117]", GRP(98, 105), GRP(106, 113), GRP(114, 121));
      LOADV(1, "v[102:105]", "v[118:119]", "v[110:113]", "v[120:121]", GRP(98, 105), GRP(106, 113), GRP(114, 121));
      LOADV(2, "v[122:125]", "v[138:139]", "v[130:133]", "v[140:141]", GRP(122, 129), GRP(130, 137), GRP(138, 145));
      LOADV(3, "v[126:129]", "v[142:143]", "v[134:137]", "v[144:145]", GRP(122, 129), GRP(130, 137), GRP(138, 145));
      LOADV(4, "v[146:149]", "v[162:163]", "v[154:157]", "v[164:165]", GRP(146, 153), GRP(154, 161), GRP(162, 169));
      LOADV(5, "v[150:153]", "v[166:167]", "v[158:161]", "v[168:169]", GRP(146, 153), GRP(154, 161), GRP(162, 169));
      LOADV(6, "v[170:173]", "v[186:187]", "v[178:181]", "v[188:189]", GRP(170, 177), GRP(178, 185), GRP(186, 193));
      LOADV(7, "v[174:177]", "v[190:191]", "v[182:185]", "v[192:193]", GRP(170, 177), GRP(178, 185), GRP(186, 193));
      SB();
    };
    int go[5];
    auto wo_prep = [&]() {
      int l = lane;
      PIN1(l);
#pragma unroll
      for (int i = 0; i < 5; ++i) { const int c = min(64 * i + l, w < 6 ? 319 : 239), row = (c * 205) >> 11; go[i] = row * (2 * DM) + (c - 10 * row) * 16; PIN1(go[i]); }
    };
    auto load_wo = [&]() {
      const uint16_t* src = (w < 6 ? wo + (size_t)(192 * g + 32 * w) * DM : w2 + (size_t)(48 * g + 24 * (w - 6)) * DM) + 80 * j;
      unsigned keep;
      asm volatile("s_mov_b32 %0, m0\n\ts_mov_b32 m0, %7\n\ts_nop 0\n\tglobal_load_lds_dwordx4 %1, %6 nt\n\t"
                   "s_add_u32 m0, m0, 0x400\n\ts_nop 0\n\tglobal_load_lds_dwordx4 %2, %6 nt\n\t"
                   "s_add_u32 m0, m0, 0x400\n\ts_nop 0\n\tglobal_load_lds_dwordx4 %3, %6 nt\n\t"
                   "s_add_u32 m0, m0, 0x400\n\ts_mov_b64 exec, %8\n\tglobal_load_lds_dwordx4 %4, %6 nt\n\t"
                   "s_add_u32 m0, m0, %10\n\ts_mov_b64 exec, %9\n\tglobal_load_lds_dwordx4 %5, %6 nt\n\t"
                   "s_mov_b64 exec, -1\n\ts_mov_b32 m0, %0"
                   : "=&s"(keep) : "v"(go[0]), "v"(go[1]), "v"(go[2]), "v"(go[3]), "v"(go[4]), "s"(src), "s"(w < 7 ? L_WO + w * 5120 : L_W2 + 3840),
                     "s"(w < 6 ? ~0ull : (1ull << 48) - 1), "s"(w < 6 ? ~0ull : 1ull), "s"(w < 6 ? 1024 : 752) : "memory", "scc");
      SB();
    };
    asm volatile("" : "=v"(vt[0]), "=v"(vt[1]), "=v"(vt[2]));
    load_v(IC<0>{});
    uint32_t tp = rt32() + SC0(PO);
    int pstep = SC0(PT);
    auto pace = [&]() {
      int rem;
      if (w == TW) asm volatile("1:\n\ts_memrealtime vcc\n\ts_waitcnt lgkmcnt(0)\n\ts_sub_i32 %0, %1, vcc_lo\n\ts_cmp_lt_u32 %0, 0x800\n\ts_cbranch_scc1 1b\n\ts_add_u32 %1, %1, %2"
                   : "=&s"(rem), "+s"(tp) : "s"(pstep) : "memory", "scc", "vcc");
    };
    const bool early = w < 4;
    auto first = [&](auto Ic) {
      constexpr int i = decltype(Ic)::value, x = i <= 8 ? kord(i - 1) : vord(i - 8);
      u32x8* p = (i <= 8 ? kt : vt) + 3 * (x >> 1);
      if (!(x & 1)) asm volatile("" : "=v"(p[0]), "=v"(p[1]), "=v"(p[2]));
    };
    auto tile0 = [&](auto Ic) {
      constexpr int i = decltype(Ic)::value;
      if constexpr (i <= 8) load_k(IC<kord(i - 1)>{}); else load_v(IC<vord(i - 8)>{});
    };
    auto tile = [&](auto Ic) { first(Ic); tile0(Ic); };
    auto tile_e = [&](auto Ic) { first(Ic); if (early) tile0(Ic); SB(); };
    auto tile_l = [&](auto Ic) { SB(); if (!early) tile0(Ic); };
    auto piece = [&](int k, int n) { const int o = n * (b >> 2) + lane * 16; return lane * 16 < n ? O_S + ((b & 3) * NB + k + (o >> 13)) * BLK + (o & 8191) : OOR; };
    const int prb = O_PR + (b >> 3) * BLK + (w >> 2) * 8192 + (b & 7) * 1024 + (w & 3) * 256;
    uint32_t tv = 0;
    PINS(tv);
    if (b < NVOTE && w == SW) {
      __builtin_amdgcn_raw_buffer_store_b128((u32x4){0, 0, 0, 0}, rs, (lane & 12) != 12 && lane < 32 ? prb - 8192 - 512 + (lane >> 4) * 8192 + (lane & 12) * 64 + (lane & 3) * 16 : OOR, 0, 16);
      asm volatile("s_load_dword %0, %1, 0x80" : "+s"(tv) : "s"(tail + prb) : "memory");
    }
    SB();
    uint32_t ep = 0, xf[5] = {0, 0, 0, 0, 0}, xs = 0;
    asm volatile("" : "+v"(xf[0]), "+v"(xf[1]), "+v"(xf[2]), "+v"(xf[3]), "+v"(xf[4]), "+v"(xs), "+v"(ep));
    if (w == 1) asm volatile("global_load_dword %0, %1, off" : "+v"(xs) : "v"(lane < 40 ? b2 + 80 * jj + 2 * lane : b1 + 48 * g + 2 * lane - 80) : "memory");
    if (w == 0)
      asm volatile("global_load_dword %0, %7, %8\n\tglobal_load_dword %1, %7, %8 offset:256\n\tglobal_load_dword %2, %7, %8 offset:512\n\t"
                   "global_load_dword %3, %7, %8 offset:768\n\tglobal_load_dword %4, %7, %8 offset:1024\n\tglobal_load_dword %5, %9, %8\n\tglobal_load_dword %6, %10, %11 sc1"
                   : "+v"(xf[0]), "+v"(xf[1]), "+v"(xf[2]), "+v"(xf[3]), "+v"(xf[4]), "+v"(xs), "+v"(ep)
                   : "v"(4 * lane), "s"(x_in), "v"(160 * jj + 4 * min(lane, 39)), "v"(0), "s"(epoch) : "memory");
    SB();
    {
      int go[4];
#pragma unroll
      for (int r = 0; r < 4; ++r) { const int c = min(64 * r + lane, 249), row = (c * 41) >> 10; go[r] = row * (2 * QD) + min(c - 25 * row, 23) * 16; }
      unsigned keep;
      asm volatile("s_mov_b32 %0, m0\n\ts_mov_b32 m0, %6\n\ts_nop 0\n\tglobal_load_lds_dwordx4 %1, %5 nt\n\t"
                   "s_add_u32 m0, m0, 0x400\n\ts_nop 0\n\tglobal_load_lds_dwordx4 %2, %5 nt\n\t"
                   "s_add_u32 m0, m0, 0x400\n\ts_nop 0\n\tglobal_load_lds_dwordx4 %3, %5 nt\n\t"
                   "s_add_u32 m0, m0, 0x400\n\ts_mov_b64 exec, %7\n\tglobal_load_lds_dwordx4 %4, %5 nt\n\t"
                   "s_mov_b64 exec, -1\n\ts_mov_b32 m0, %0"
                   : "=&s"(keep) : "v"(go[0]), "v"(go[1]), "v"(go[2]), "v"(go[3]),
                     "s"(wq + (size_t)(80 * jj + 10 * w) * QD + 192 * jg), "s"(L_WQ + w * 10 * QS), "s"((1ull << 58) - 1) : "memory", "scc");
    }
    SB();
    {
      const int c = min(lane, 59), row = (c * 43) >> 8;
      unsigned keep;
      asm volatile("s_mov_b32 %0, m0\n\ts_mov_b32 m0, %3\n\ts_mov_b64 exec, %4\n\tglobal_load_lds_dwordx4 %1, %2 nt\n\ts_mov_b64 exec, -1\n\ts_mov_b32 m0, %0"
                   : "=&s"(keep) : "v"(row * (2 * FF) + (c - 6 * row) * 16), "s"(w1 + (size_t)(80 * jj + 10 * w) * FF + 48 * jg), "s"(L_W1 + w * 960), "s"((1ull << 60) - 1) : "memory");
    }
    SB();
    BAR0();
    tile_e(IC<1>{});
    auto probe = [&](auto Oc) -> uint32_t {
      unsigned long long c0, c1; uint32_t v;
      asm volatile("s_memtime %0\n\ts_waitcnt lgkmcnt(0)\n\ts_load_dword %2, %3, %4\n\ts_waitcnt lgkmcnt(0)\n\ts_memtime %1\n\ts_waitcnt lgkmcnt(0)"
                   : "=&s"(c0), "=&s"(c1), "=&s"(v) : "s"(tail + prb), "n"(decltype(Oc)::value) : "memory");
      return (uint32_t)(c1 - c0);
    };
    {
      t16 = w * 1024 + lane * 16; bo = sm + kq * 16;
      sS = w == SW ? (n16 == 0 ? sm + S_XP + kq * 16 : L_ONE) : ck * 32;
      dT = lane < (w == SW ? 2 : 16) ? lane * 4 : OOR;
      LAS u32x4* T = (LAS u32x4*)(uintptr_t)(uint32_t)(L_TAB + t16);
      const int pk = ((lane >> 1) & 6) | (lane & 1), c4 = 8 * w + ((lane >> 3) & 6) + ((lane >> 1) & 1);
      T[0] = (u32x4){(uint32_t)(sm + S_ML + pk * 16), (uint32_t)(w < 6 ? sm + S_RED + pk * RS + c4 * 16 : sm + S_ML + pk * 16),
                     (uint32_t)(w == SW ? ((lane & ~4) == 0 ? lane : OOR) : (!(lane & 1) && w < 6) ? c4 * 16 + (lane & 12) : OOR), (uint32_t)(jp * 768 + w * 256 + ck * 16)};
      T[5 * NT] = (u32x4){(uint32_t)((kq >> 1) == ((n16 >> 2) & 1) ? sm + S_QB + (n16 & 1) * 192 + (n16 & 8) * 4 + (kq & 1) * 16 : sm + S_ZQ),
                          (uint32_t)(sm + S_RED + w * RS + lane * 4), (uint32_t)(sm + S_ML + w * 16), 0u};
      ix = ((n16 & 1) ? 0xeeeeu : 0x4444u) | (n16 >> 2) * 0x55550000u;
      const int gd = (gc >> 4) & 3, dimo = gd + (gc < 128 ? 8 * (gc & 15) + 4 * (gc >> 6) : 128 + 4 * (gc & 15));
      if (w == 1 && lane < 12) *(LAS u32x4*)(uintptr_t)(uint32_t)(sm + S_ZQ + lane * 16) = (u32x4){0, 0, 0, 0};
      T[NT] = (u32x4){(uint32_t)(sm + S_OV + opos(dimo)), (uint32_t)(L_WO + (w < 5 ? w : 0) * 32 + trl * 160 + trc),
                      (uint32_t)(sm + S_XP + 2 * kpos(xc)), (uint32_t)(L_W1 + min(w, 2) * 32 + trl * 96 + trc)};
      T[2 * NT] = (u32x4){(uint32_t)(w < 5 ? sm + S_HV + 2 * kpos(hc) : piece(0, 768) + (w & 1) * 8192), (uint32_t)(w < 5 ? L_W2 + w * 32 + trl * 160 + trc : piece(6, 1024)),
                          (uint32_t)(L_CS + 768 + 4 * hc), (uint32_t)((w & 1) ? 1024 + jp * 64 + (ck & 3) * 16 : lane * 16)};
      T[3 * NT] = (u32x4){(uint32_t)(sm + S_QB + qpos(gc)), (uint32_t)(L_CS + 4 * gc), (uint32_t)(L_WQ + (w < 6 ? w : 0) * 64 + trl * QS + trc), (uint32_t)(2 * xc)};
    }
    tile_l(IC<1>{});
    BAR0();
    tile(IC<2>{});
    asm volatile("s_waitcnt vmcnt(%8)" : "+v"(ep), "+v"(xf[0]), "+v"(xf[1]), "+v"(xf[2]), "+v"(xf[3]), "+v"(xf[4]), "+v"(xs), "+v"(cp) : "n"(3 * UP + 2) : "memory");
    if (w < 2) lwr4(sm + S_B + w * 256 + lane * 4, xs);
    if (w == 0) {
      float s1 = 0.f;
#pragma unroll
      for (int i = 0; i < 5; ++i) s1 += lo2f(xf[i]) + hi2f(xf[i]);
      const float mean = wsum_bcast(s1) * (1.0f / DM);
      float q0 = 0.f;
#pragma unroll
      for (int i = 0; i < 5; ++i) { const float d0 = lo2f(xf[i]) - mean, d1 = hi2f(xf[i]) - mean; q0 = fmaf(d0, d0, q0); q0 = fmaf(d1, d1, q0); }
      const float var = wsum_bcast(q0) * (1.0f / DM);
      const uint32_t mv = pk2(mean, var);
      const float rinv = __builtin_amdgcn_rcpf(rbf(__builtin_amdgcn_sqrtf(rbf(hi2f(mv) + 1e-5f))));
      if (lane < 48) lwr4(sm + S_XP + 2 * kpos(2 * lane), lane < 40 ? pk2(rbf(lo2f(xs) - lo2f(mv)) * rinv, rbf(hi2f(xs) - lo2f(mv)) * rinv) : 0u);
      if (lane < 48) lwr4(L_ONE + lane * 4, 0x3f803f80u);
      if (lane < 16) lwr2(sm + S_HV + 2 * kpos(48 + lane), 0);
      lwr4(L_FLAG, ep * 1664525u + 1013904223u);
    }
    if (b < NVOTE && (w & 3) != 3) lwr4(L_FLAG + 32 + 4 * w, probe(IC<0>{}));
    SB();
    asm volatile("s_waitcnt vmcnt(%0)" :: "n"(6) : "memory");
    lds_barrier();
    PINS(tv);
    tile_e(IC<3>{});
    tag = __builtin_amdgcn_readfirstlane(lrd4(L_FLAG));
    float pv[2];
    {
      const int xb = n16 >= 4 ? L_ONE : sm + S_XP + kq * 16;
      const u32x4 x0 = lrd16(xb), x1 = lrd16(xb + 64), x2 = lrd16(xb + 128);
#pragma unroll
      for (int r = 0; r < 2; ++r) {
        const int tl = min(2 * w + r, 14), rstr = tl < 12 ? QS : 96;
        const int a0 = (tl < 12 ? L_WQ : L_W1 - 12 * 32) + tl * 32 + trl * rstr + trc;
        f32x4 d = {0.f, 0.f, 0.f, 0.f};
        d = mfma32(x0, cat(trd(a0), trd(a0 + 16 * rstr)), d);
        d = mfma32(x1, cat(trd(a0 + 32 * rstr), trd(a0 + 48 * rstr)), d);
        d = mfma32(x2, cat(trd(a0 + 64 * rstr), (u32x2){0, 0}), d);
        pv[r] = d[0];
      }
    }
    gu64* dst = G + G_P + b * RP + (kq == 0 ? 0 : 192) + n16 + 32 * w;
    const int cu = (lane >> 2) & 7;
    int so = (w & 3) == 0 ? g * BLK + (w >> 2) * 8192 + j * 1024 + lane * 16
             : w == 1 && cu < 5 ? O_C + (2 * j + (cu >> 2)) * BLK + (lane >> 5) * 8192 + (cu & 3) * 2048 + ((g + 7 * cu) & 31) * 64 + (lane & 3) * 16 : OOR;
    if (w == TW) {
      unsigned xcc;
      asm volatile("s_getreg_b32 %0, hwreg(HW_REG_XCC_ID)" : "=s"(xcc));
      const u32x4 l0 = lrd16(L_FLAG + 32), l1 = lrd16(L_FLAG + 48);
      const uint32_t vote = ((min(min(l1.x, l1.y), l1.z) < min(min(l0.x, l0.y), l0.z) ? 1u : 0u) ^ (xcc >> 2)) & 1u;
      pv[1] = uf(vote | ((xcc >> 1) & 2) | (rt32() << 2));
    }
    PIN1(pv[0]); PIN1(pv[1]); PIN1(dst); PIN1(so);
    tile_l(IC<3>{});
    pace();
    BAR0();
    if (kq == 1 || (kq == 0 && w < 6)) {
      __hip_atomic_store(dst, ((unsigned long long)tag << 32) | __float_as_uint(pv[0]), RLX_AGENT);
      if (w < 7) __hip_atomic_store(dst + 16, ((unsigned long long)tag << 32) | __float_as_uint(pv[1]), RLX_AGENT);
    }
    if (w == TW && b < NVOTE && lane == 0) __hip_atomic_store(G + G_F + b, ((unsigned long long)tag << 32) | __float_as_uint(pv[1]), RLX_AGENT);
    if ((w & 2) == 0 && w != 5) __builtin_amdgcn_raw_buffer_store_b128((u32x4){SENT, SENT, SENT, SENT}, rs, so, 0, 16);
    SB();
    tile(IC<RT>{});
    pace();
    BAR0();
    tile_e(IC<RT + 1>{});
    {
      auto rd = [&](int off) { return lo2f(*(const LAS uint16_t*)(uintptr_t)(uint32_t)(sm + S_B + off)); };
      *(LAS f32x4*)(uintptr_t)(uint32_t)(L_TAB + 4 * NT * 16 + t16) = (f32x4){rd(2 * xc), rd(256 + 2 * xc), rd(416 + 2 * hc), 0.f};
    }
    tile_l(IC<RT + 1>{});
    pace();
    BAR0();
    tile(IC<RT + 2>{});
    pace();
    BAR0();
    u32x4 er = {0, 0, 0, 0};
    asm volatile("" : "+v"(er));
    auto ereq = [&](const char* src) { asm volatile("global_load_dwordx4 %0, %1, off sc1" : "+v"(er) : "v"(src) : "memory"); };
    auto efin = [&](auto Nc, const char* src) {
      asm volatile("s_waitcnt vmcnt(%1)" : "+v"(er) : "n"(decltype(Nc)::value) : "memory");
#pragma unroll 1
      for (unsigned sp = 0; sp < SPIN_LIMIT; ++sp) {
        if (__all(tagbad(er, tag) == 0)) break;
        __builtin_amdgcn_s_sleep(4);
        asm volatile("global_load_dwordx4 %0, %1, off sc1\n\ts_waitcnt vmcnt(0)" : "+v"(er) : "v"(src) : "memory");
      }
    };
    {
      const int un = j + 8 * w;
      const bool mine = un < 27;
      const char* src = mine ? (const char*)(G + G_P + (8 * g + jp) * RP) + un * 128 + ck * 16 : (const char*)(G + G_F) + n16 * 16;
      if (mine || w == TW) ereq(src);
      SB();
      tile(IC<RT + 3>{});
      pace();
      BAR0();
      tile_e(IC<RT + 4>{});
      asm volatile("s_waitcnt vmcnt(%0)" :: "n"(nreq(RT, RT + 3)) : "memory");
      if (mine) efin(IC<nreq(RT + 3, RT + 4)>{}, src);
      const float t = swapadd32(uf(er.x), uf(er.z));
      uint32_t sv = __float_as_uint(ror8add(swapadd16(t, t)));
      gu64* sd = G + G_R + g * RP + un * 16 + 2 * ck + (lane >> 5);
      PIN1(sv); PIN1(sd);
      tile_l(IC<RT + 4>{});
      pace();
      BAR0();
      if (mine && !(lane & 24)) __hip_atomic_store(sd, ((unsigned long long)tag << 32) | sv, RLX_AGENT);
      if (w == TW) {
        if (b < NVOTE && lane == 0) __hip_atomic_store(G + G_F + NVOTE + b, ((unsigned long long)tag << 32) | rt32(), RLX_AGENT);
        efin(IC<nreq(RT + 3, RT + 4)>{}, src);
        const uint32_t hx = (uint32_t)__ballot(er.x & 2), hz = (uint32_t)__ballot(er.z & 2);
        lwr4(L_FLAG + 16, (__popcll(__ballot(er.x & 1)) + __popcll(__ballot(er.z & 1))) << 8 | (hx & 1) | ((hz & 1) << 1) | ((hx & 2) << 1) | ((hz & 2) << 2));
        if (SC(PB)) {
          const float d = (float)((int)(er.x - (tp << 2)) >> 2) + (float)((int)(er.z - (tp << 2)) >> 2);
          tp += __builtin_amdgcn_readfirstlane((int)(wsum_bcast(d) * (1.f / 128.f))) + SC(PA);
          pstep = SC(PB);
        }
      }
      tp = __builtin_amdgcn_readfirstlane(tp); pstep = __builtin_amdgcn_readfirstlane(pstep);
      SB();
    }
    tile(IC<RT + 5>{});
    pace();
    lds_barrier();
    tile_e(IC<RT + 6>{});
    {
      const int f0 = __builtin_amdgcn_readfirstlane(lrd4(L_FLAG + 16)), votes = f0 >> 8;
      const int m4 = (f0 & 15) ^ ((SC(CPAR) < 2 ? SC(CPAR) : votes > 2 * NVOTE) ? 15 : 0);
      const int pm = m4 | ((m4 ^ 15) << 4);
      auto par = [&](int x) { return ((pm >> x) & 1) * 8192; };
      const int cw = O_C + (w >> 2) * BLK + (w & 3) * 2048;
      oG = g * BLK + par(jj); oD = oG + (w == SW ? O_SG + j * 32 : j * 768);
      rC = 2 * jj * BLK + par(jj) + cw - w * 1024; eC = 2 * j * BLK + par(j) + cw + ((g + 7 * w) & 31) * 64;
      eH = jg * BLK + par(jg >> 2) + (w == SW ? O_SH + jj * 32 : O_H + (w < 2 ? jj * 128 + w * 64 : 1024 + jj * 64));
      rK = par(jj) + O_DB - TW * 1024;
      dG = O_S + ((jj & 3) * NB + 6 + (g & 3) - g) * BLK; dH = O_S + (((jg >> 2) & 3) * NB + 6 + (jg & 3) - jg) * BLK;
      dR = O_S - O_C + ((jj & 3) * NB + 10 - 2 * jj) * BLK; dE = O_S - O_C + ((j & 3) * NB + 10 - 2 * j) * BLK;
      eQ = jg * BLK + dH - 4 * BLK + par(jg >> 2) + (w == SW ? O_SG + jj * 32 : jj * 768 + w * 128);
#define UNI(a) a = __builtin_amdgcn_readfirstlane(a)
      UNI(oG); UNI(oD); UNI(rC); UNI(eC); UNI(eH); UNI(eQ); UNI(rK);
      oD1 = oD + dG; cwo = O_DB + b * 8; oo = 160 * b; far = min(b >> 3, 1);
      rs4 = (u32x4){(uint32_t)(uintptr_t)tail, (uint32_t)((uintptr_t)tail >> 32) & 0xffffu, 128u * BLK, 0x00020000u};
      ro4 = (u32x4){(uint32_t)(uintptr_t)out, (uint32_t)((uintptr_t)out >> 32) & 0xffffu, 2u * DM, 0x00020000u};
#pragma unroll
      for (int i = 0; i < 9; ++i) { scv[i] = i < 8 ? SC(i) : SC(PW); PINS(scv[i]); }
      PINS2(oG, oD); PINS2(rC, eC); PINS2(eH, eQ); PINS2(rK, oD1); PINS2(dG, dH); PINS2(dR, dE); PINS2(cwo, oo); PINS(far); PINS2(rs4, ro4); PINS2(eps, small);
      PIN1(sS); PIN1(dT);
    }
    tile_l(IC<RT + 6>{});
    pace();
    BAR0();
    {
      const int v0 = min(128 * w + 2 * lane, RP - 2);
      const char* src = w < 4 ? (const char*)(G + G_R + g * RP + v0) : (const char*)(G + G_F + NVOTE) + n16 * 16;
      if (w < 4 || w == TW) ereq(src);
      SB();
      tile(IC<RT + 7>{});
      pace();
      BAR0();
      tile(IC<RT + 8>{});
      pace();
      BAR0();
      if (w == TW && SC(PD)) {
        asm volatile("s_waitcnt vmcnt(%1)" : "+v"(er) : "n"(nreq(RT + 7, RT + 8)) : "memory");
        const bool v0 = er.y == tag, v1 = er.w == tag;
        const float d = (v0 ? (float)(int)(er.x - tp) : 0.f) + (v1 ? (float)(int)(er.z - tp) : 0.f);
        const int n = __popcll(__ballot(v0)) + __popcll(__ballot(v1));
        if (n > 32) {
          tp += __builtin_amdgcn_readfirstlane((int)(wsum_bcast(d) * __builtin_amdgcn_rcpf((float)n))) + SC(PC);
          pstep = SC(PD);
        }
      }
      tp = __builtin_amdgcn_readfirstlane(tp); pstep = __builtin_amdgcn_readfirstlane(pstep);
      SB();
      tile(IC<RT + 9>{});
      if (w < 4) {
        efin(IC<nreq(RT + 7, RT + 9)>{}, src);
        if (v0 < 192) lwr4(sm + S_QB + qpos(v0), pk2(uf(er.x), uf(er.z)));
        else *(LAS u32x2*)(uintptr_t)(uint32_t)(L_CS + 4 * (v0 - 192)) = (u32x2){er.x, er.z};
      }
    }
    SB();
    pace();
    lds_barrier();
    tile_e(IC<RT + 10>{});
    asm volatile("s_waitcnt vmcnt(%12)"
                 : GRP(2, 9)(kt[0]), GRP(10, 17)(kt[1]), GRP(18, 25)(kt[2]), GRP(26, 33)(kt[3]), GRP(34, 41)(kt[4]), GRP(42, 49)(kt[5]),
                   GRP(50, 57)(kt[6]), GRP(58, 65)(kt[7]), GRP(66, 73)(kt[8]), GRP(74, 81)(kt[9]), GRP(82, 89)(kt[10]), GRP(90, 97)(kt[11])
                 : "n"(nreq(9, RT + 9)) : "memory");
    const u32x4 aT = lrd16(L_TAB + 5 * NT * 16 + t16);
    uint32_t pb;
    f32x4 dv[3];
    {
      float mx, ev;
      pb = qk_softmax(mx, ev, dv, aT.x, 1.f);
      const float ls = wsum63(ev);
      if (lane == 63) *(LAS f32x2*)(uintptr_t)(uint32_t)aT.z = (f32x2){mx, ls};
    }
#define VFIX(v, e0, e1, e2, e3, g0, g1, g2) if constexpr (V == v) \
      asm volatile("s_waitcnt vmcnt(%3)\n\tv_swap_b32 v" #e0 ", v" #e2 "\n\tv_swap_b32 v" #e1 ", v" #e3 "\n\ts_nop 0" \
                   : g0(vt[a]), g1(vt[a + 1]), g2(vt[a + 2]) : "n"(decltype(YN)::value) : "memory")
    auto v_fix = [&](auto Vc, auto YN) {
      constexpr int V = decltype(Vc)::value, a = 3 * (V >> 1);
      SB();
      VFIX(0, 100, 101, 106, 107, GRP(98, 105), GRP(106, 113), GRP(114, 121));
      VFIX(1, 104, 105, 110, 111, GRP(98, 105), GRP(106, 113), GRP(114, 121));
      VFIX(2, 124, 125, 130, 131, GRP(122, 129), GRP(130, 137), GRP(138, 145));
      VFIX(3, 128, 129, 134, 135, GRP(122, 129), GRP(130, 137), GRP(138, 145));
      VFIX(4, 148, 149, 154, 155, GRP(146, 153), GRP(154, 161), GRP(162, 169));
      VFIX(5, 152, 153, 158, 159, GRP(146, 153), GRP(154, 161), GRP(162, 169));
      VFIX(6, 172, 173, 178, 179, GRP(170, 177), GRP(178, 185), GRP(186, 193));
      VFIX(7, 176, 177, 182, 183, GRP(170, 177), GRP(178, 185), GRP(186, 193));
      SB();
    };
    auto mm_step = [&](auto Ic, auto YN) {
      constexpr int i = decltype(Ic)::value;
      u32x4 pa = pset<i>(pb);
      PIN1(pa);
      v_fix(IC<2 * i + 1>{}, YN);
      dv[2] = smf<1>(pa, vt[3 * i + 2], dv[2], ix); SB();
      dv[0] = smf<1>(pa, vt[3 * i], dv[0], ix); dv[1] = smf<1>(pa, vt[3 * i + 1], dv[1], ix);
    };
    SB();
    wo_prep();
    tile_l(IC<RT + 10>{});
    pace();
    lds_barrier();
    tile_e(IC<15>{});
    v_fix(IC<0>{}, IC<16>{}); v_fix(IC<2>{}, IC<16>{});
    mm_step(IC<0>{}, IC<16>{}); mm_step(IC<1>{}, IC<12>{}); v_fix(IC<4>{}, IC<8>{});
    tile_l(IC<15>{});
    BAR0();
    load_wo();
    v_fix(IC<6>{}, IC<nreq(14, 15)>{});
    mm_step(IC<2>{}, IC<nreq(15, 15)>{});
    SB();
    u32x4 cT = lrd16(L_TAB + t16);
    PIN1(cT);
    mm_step(IC<3>{}, IC<5>{});
    lwr4(aT.y + 512, __float_as_uint(dv[2][0])); lwr4(aT.y, __float_as_uint(dv[0][0])); lwr4(aT.y + 256, __float_as_uint(dv[1][0]));
    lds_barrier();
    pv_combine(cT, oD, w == SW);
    s0 = cT.w;
  }
  {
    asm volatile(TAIL_ASM
                 : [s0] "+v"(s0), [tc] "+s"(tcur), [te] "+s"(tE), [oG] "+s"(oG), [rC] "+s"(rC), [eC] "+s"(eC), [eH] "+s"(eH)
                 : [t16] "v"(t16), [bo] "v"(bo), [ss] "v"(sS), [dt] "v"(dT), [ix] "v"(ix), [rs] "s"(rs4), [ro] "s"(ro4), [w] "s"(w0), [far] "s"(far),
                   [oD] "s"(oD1), [dG] "s"(dG), [dH] "s"(dH), [dR] "s"(dR), [dE] "s"(dE), [eQ] "s"(eQ), [rK] "s"(rK), [tag] "s"(tag), [cw] "s"(cwo), [oo] "s"(oo),
                   [p3e] "s"(scv[0]), [p2] "s"(scv[1]), [p3] "s"(scv[2]), [p4] "s"(scv[3]), [p5] "s"(scv[4]), [p1] "s"(scv[5]), [rmin] "s"(scv[6]), [rcons] "s"(scv[7]), [pw] "s"(scv[8]),
                   [eps] "s"(eps), [small] "s"(small), [spin] "n"(SPIN_LIMIT), [tab] "n"(L_TAB), [xp] "n"(S_XP), [ov] "n"(S_OV), [hv] "n"(S_HV),
                   [osg] "n"(O_SG), [osh] "n"(O_SH), [oh] "n"(O_H), [oor] "n"(OOR), [qsw] "n"(L_FLAG + 4),
                   "{v[2:9]}"(kt[0]), "{v[10:17]}"(kt[1]), "{v[18:25]}"(kt[2]), "{v[26:33]}"(kt[3]), "{v[34:41]}"(kt[4]), "{v[42:49]}"(kt[5]),
                   "{v[50:57]}"(kt[6]), "{v[58:65]}"(kt[7]), "{v[66:73]}"(kt[8]), "{v[74:81]}"(kt[9]), "{v[82:89]}"(kt[10]), "{v[90:97]}"(kt[11]),
                   "{v[98:105]}"(vt[0]), "{v[106:113]}"(vt[1]), "{v[114:121]}"(vt[2]), "{v[122:129]}"(vt[3]), "{v[130:137]}"(vt[4]),
                   "{v[138:145]}"(vt[5]), "{v[146:153]}"(vt[6]), "{v[154:161]}"(vt[7]), "{v[162:169]}"(vt[8]), "{v[170:177]}"(vt[9]),
                   "{v[178:185]}"(vt[10]), "{v[186:193]}"(vt[11])
                 : "memory", "scc", "vcc",
                   "v194", "v195", "v196", "v197", "v198", "v199", "v200", "v201", "v202", "v203", "v204", "v205", "v206", "v207", "v208", "v209",
                   "v210", "v211", "v212", "v213", "v214", "v215", "v216", "v217", "v218", "v219", "v220", "v221", "v222", "v223", "v224", "v225",
                   "v226", "v227", "v228", "v229", "v230", "v231", "v232", "v233", "v234", "v235", "v236", "v237", "v238", "v239", "v240", "v241",
                   "v242", "v243", "v244", "v245", "v246", "v247", "s80", "s81", "s82", "s83", "s84", "s85", "s86", "s87", "s88", "s89", "s90", "s91",
                   "s92", "s93", "s94", "s95", "s96", "s97", "s98", "s99"
                 );
  }
  if (b0 == 8 && t16 == SW * 1024) __hip_atomic_store((gu32*)epoch, tag, RLX_AGENT);
}


extern "C" void kernel_launch(void* const* d_in, const int* in_sizes, int n_in, void* d_out, int out_size,
                              void* d_ws, size_t ws_size, hipStream_t stream) {
  (void)in_sizes; (void)n_in; (void)out_size; (void)ws_size;
  (void)hipFuncSetAttribute((const void*)mega_kernel, hipFuncAttributeMaxDynamicSharedMemorySize, L_TOTAL);
  mega_kernel<<<NWG, NT, L_TOTAL, stream>>>(
      (const uint16_t*)d_in[0], (const uint16_t*)d_in[1], (const uint16_t*)d_in[2], (const uint16_t*)d_in[3],
      (const uint16_t*)d_in[4], (const uint16_t*)d_in[6], (const uint16_t*)d_in[7], (const uint16_t*)d_in[8],
      (const uint16_t*)d_in[9], (uint16_t*)d_out, (unsigned*)d_ws
  );
}
```

```cpp
#include <hip/hip_runtime.h>
#include <hip/hip_bf16.h>
#include <stdint.h>

#define GAS __attribute__((address_space(1)))
#define LAS __attribute__((address_space(3)))
typedef GAS unsigned long long gu64;
typedef GAS unsigned gu32;
typedef uint32_t u32x2 __attribute__((ext_vector_type(2)));
typedef uint32_t u32x4 __attribute__((ext_vector_type(4)));
typedef uint32_t u32x8 __attribute__((ext_vector_type(8)));
typedef float f32x2 __attribute__((ext_vector_type(2)));
typedef float f32x4 __attribute__((ext_vector_type(4)));
typedef short s16x4 __attribute__((ext_vector_type(4)));
typedef __attribute__((ext_vector_type(2))) __bf16 bf16x2;
typedef __attribute__((ext_vector_type(8))) __bf16 bf16x8;
typedef __attribute__((ext_vector_type(16))) __bf16 bf16x16;
#define RLX_AGENT __ATOMIC_RELAXED, __HIP_MEMORY_SCOPE_AGENT

constexpr int DM = 640, NH = 32, DH = 192, SK = 32768, WIN = 4096, FF = 1536, QD = NH * DH;
constexpr int NWG = 256, NT = 512, NW = NT / 64;
#define SPINB 20
constexpr unsigned SPIN_LIMIT = 1u << SPINB;
constexpr int TW = 7, SW = 6;
#define CODEB 15872
constexpr int CODEPF = CODEB;
constexpr int UP = 3;
constexpr int nreq(int i0, int i1) { int n = 0; for (int i = i0; i <= i1 && i < 16; ++i) n += i == 15 ? 9 : (i == 0 || i > 8) ? 4 : 3; return n; }
constexpr int kord(int n) { return 2 * (n & 3) + (n >> 2); }
constexpr int vord(int n) { return (n & 4) + 2 * (n & 1) + ((n >> 1) & 1); }
constexpr int RT = UP + 1;
constexpr float LOG2E = 1.4426950408889634f;
constexpr float SMALL = 40.f;
constexpr uint32_t SENT = 0xffffffffu;
constexpr int OOR = 1 << 22;

constexpr int RP = 27 * 16;
constexpr int G_P = 0, G_R = G_P + NWG * RP, G_F = G_R + NH * RP;
constexpr int BLK = 16384, NB = 14, O_SG = 6144, O_SH = 6400, O_H = 6656, O_C = 32 * BLK, O_S = 48 * BLK, O_DB = O_S + 4 * NB * BLK, O_PR = O_DB + BLK;
constexpr int NVOTE = 32;

constexpr int QS = 400;
constexpr int RS = 800;
constexpr int S_XP = 0, S_OV = 256, S_HV = 768, S_QB = 896, S_ZQ = 1280, S_RED = 1472, S_ML = S_RED + NW * RS, S_B = S_ML + 128, S_END = S_B + 512;
constexpr int L_SM = 0, L_CS = L_SM + S_END, L_ONE = L_CS + 960, L_FLAG = L_ONE + 192, L_SCH = L_FLAG + 64, L_TAB = L_SCH + 64,
              L_WQ = L_TAB + 6 * NT * 16, L_WO = L_WQ + 32768, L_W2 = L_WO + 30720, L_W1 = L_W2 + 8192, L_TOTAL = L_W1 + 8192;
static_assert(L_TOTAL <= 160 * 1024 && L_TOTAL > 80 * 1024 && S_END % 16 == 0, "LDS");

enum { P3E, P2, P3, P4, P5, P1, RMIN, RCONS, CPAR, PO, PT, PA, PB, PC, PD, PW, NSCH };
#define SCHED_INIT {120, 127, 58, 61, 57, 67, 5, 50, 2, 410, 88, 620, 88, 470, 90, 6}
constexpr int sched[NSCH] = SCHED_INIT;
#define SC0(i) sched[i]
#define SC(i) sched[i]

__device__ __forceinline__ float lo2f(uint32_t p) { return __uint_as_float(p << 16); }
__device__ __forceinline__ float hi2f(uint32_t p) { return __uint_as_float(p & 0xffff0000u); }
__device__ __forceinline__ uint32_t pk2(float a, float b) {
  f32x2 v = {a, b};
  return __builtin_bit_cast(uint32_t, __builtin_convertvector(v, bf16x2));
}
__device__ __forceinline__ float rbf(float f) { return __uint_as_float(pk2(0.f, f)); }
template <int CTRL> __device__ __forceinline__ float dppv(float v) {
  return __int_as_float(__builtin_amdgcn_update_dpp(0, __float_as_int(v), CTRL, 0xf, 0xf, false));
}
template <int CTRL, int RM> __device__ __forceinline__ float dpp_add(float v) {
  return v + __int_as_float(__builtin_amdgcn_update_dpp(0, __float_as_int(v), CTRL, RM, 0xf, CTRL < 0x140));
}
__device__ __forceinline__ float wsum63(float v) {
  v = dpp_add<0x111, 0xf>(v); v = dpp_add<0x112, 0xf>(v); v = dpp_add<0x114, 0xf>(v); v = dpp_add<0x118, 0xf>(v);
  v = dpp_add<0x142, 0xa>(v); v = dpp_add<0x143, 0xc>(v);
  return v;
}
__device__ __forceinline__ float wsum_bcast(float v) {
  return __int_as_float(__builtin_amdgcn_readlane(__float_as_int(wsum63(v)), 63));
}
__device__ __forceinline__ float swapadd32(float a, float b) {
  auto r = __builtin_amdgcn_permlane32_swap(__float_as_uint(a), __float_as_uint(b), false, false);
  return __uint_as_float(r[0]) + __uint_as_float(r[1]);
}
__device__ __forceinline__ float swapadd16(float a, float b) {
  auto r = __builtin_amdgcn_permlane16_swap(__float_as_uint(a), __float_as_uint(b), false, false);
  return __uint_as_float(r[0]) + __uint_as_float(r[1]);
}
__device__ __forceinline__ float ror8add(float v) { return v + dppv<0x128>(v); }
__device__ __forceinline__ float sum_b320(const u32x4& v) {
  float a, b, c;
  asm("v_add_f32_dpp %0, %3, %3 row_ror:8 row_mask:0xf bank_mask:0x3\n\tv_add_f32_dpp %1, %4, %4 row_ror:8 row_mask:0xf bank_mask:0x3\n\t"
      "v_add_f32_dpp %0, %5, %5 row_ror:8 row_mask:0xf bank_mask:0xc\n\tv_add_f32_dpp %1, %6, %6 row_ror:8 row_mask:0xf bank_mask:0xc\n\ts_nop 0\n\t"
      "v_add_f32_dpp %2, %0, %0 row_ror:12 row_mask:0xf bank_mask:0x5\n\tv_add_f32_dpp %2, %1, %1 row_ror:4 row_mask:0xf bank_mask:0xa\n\ts_nop 1\n\t"
      "v_add_f32_dpp %2, %2, %2 quad_perm:[1,0,3,2] row_mask:0xf bank_mask:0xf"
      : "=&v"(a), "=&v"(b), "=&v"(c) : "v"(v.x), "v"(v.y), "v"(v.z), "v"(v.w));
  return c;
}
template <int N> __device__ __forceinline__ uint32_t rbc(uint32_t v) { return __builtin_amdgcn_mov_dpp(v, 0x150 + N, 0xf, 0xf, false); }
template <int I> __device__ __forceinline__ u32x4 pset(uint32_t p) { return (u32x4){rbc<4 * I>(p), rbc<4 * I + 1>(p), rbc<4 * I + 2>(p), rbc<4 * I + 3>(p)}; }
#define ZERO4(d, a, b, c, e) asm volatile("v_mov_b64 v[" #a ":" #b "], 0\n\tv_mov_b64 v[" #c ":" #e "], 0" : "={v[" #a ":" #e "]}"(d))
__device__ __forceinline__ int opos(int k) { return ((k >> 1) & 1) * 192 + (k >> 6) * 64 + (k & 32) + (k & 8) * 2 + (k & 4) * 2 + ((k >> 4) & 1) * 4 + (k & 1) * 2; }
__device__ __forceinline__ void lds_barrier() { asm volatile("s_waitcnt lgkmcnt(0)\n\ts_barrier" ::: "memory"); }
__device__ __forceinline__ uint32_t tagbad(const u32x4& r, uint32_t tag) { return (r.y ^ tag) | (r.w ^ tag); }
__device__ __forceinline__ float uf(uint32_t u) { return __uint_as_float(u); }
__device__ __forceinline__ uint32_t rt32() {
  unsigned long long t;
  asm volatile("s_memrealtime %0\n\ts_waitcnt lgkmcnt(0)" : "=s"(t)::"memory");
  return (uint32_t)t;
}
__device__ __forceinline__ f32x4 mfma32(const u32x4& a, const u32x4& b, const f32x4& c) {
  return __builtin_amdgcn_mfma_f32_16x16x32_bf16(__builtin_bit_cast(bf16x8, a), __builtin_bit_cast(bf16x8, b), c, 0, 0, 0);
}
template <int H> __device__ __forceinline__ f32x4 smf(const u32x4& a, const u32x8& b, const f32x4& c, uint32_t ix) {
  return __builtin_amdgcn_smfmac_f32_16x16x64_bf16(__builtin_bit_cast(bf16x8, a), __builtin_bit_cast(bf16x16, b), c, ix, 0, H);
}
__device__ __forceinline__ int qpos(int v) { return ((v >> 1) & 1) * 192 + (v >> 2) * 4 + (v & 1) * 2; }
__device__ __forceinline__ u32x4 cat(const u32x2& a, const u32x2& b) { return (u32x4){a.x, a.y, b.x, b.y}; }
__device__ __forceinline__ int kpos(int k) { return (k & ~31) | ((k & 12) << 1) | ((k & 16) >> 2) | (k & 3); }

#define PIN1(a) asm volatile("" : "+v"(a))
#define PINS(a) asm volatile("" : "+s"(a))
#define PINS2(a, c) asm volatile("" : "+s"(a), "+s"(c))
#define SB() do { asm volatile("" ::: "memory"); __builtin_amdgcn_sched_barrier(0); } while (0)
#define BAR0() asm volatile("s_barrier" ::: "memory")
template <int V> using IC = std::integral_constant<int, V>;

#define TAIL_ASM \
  "s_branch .LGO%=\n\t.p2align 7\n\ts_nop 0\n\ts_nop 0\n\ts_nop 0\n\ts_nop 0\n\ts_nop 0\n\ts_nop 0\n\ts_nop 0\n\ts_nop 0\n\t.LGO%=:\ns_mov_b32 s80, 0\n\t" \
  "s_mov_b32 s84, -1\n\ts_getpc_b64 s[98:99]\n\t.LPC%=:\ns_add_u32 s98, s98, .LCLK%=-.LPC%=\n\ts_addc_u32 s99, s99, 0\n\t.LLOOP%=:\n" \
  "s_cmp_gt_u32 %[w], 4\n\ts_cbranch_scc1 .LH2X%=\n\tds_read_b64 v[240:241], %[t16] offset:%[tab]+8192\n\ts_mov_b32 s81, %[oG]\n\t" \
  "s_add_u32 s82, s81, %[osg]\n\ts_mov_b32 s83, %[eC]\n\tv_and_b32 v242, 16, %[t16]\n\tv_mov_b64 v[232:233], 0\n\tv_mov_b64 v[234:235], 0\n\t" \
  "v_mad_u32_u24 v242, v242, 12, %[bo]\n\ts_cmp_eq_u32 s80, 0\n\ts_cbranch_scc1 .LH2A%=\n\ts_waitcnt lgkmcnt(0)\n\t" \
  "ds_read_b64_tr_b16 v[204:205], v241\n\tds_read_b64_tr_b16 v[206:207], v241 offset:2560\n\tds_read_b64_tr_b16 v[208:209], v241 offset:5120\n\t" \
  "ds_read_b64_tr_b16 v[210:211], v241 offset:7680\n\tds_read_b64_tr_b16 v[212:213], v241 offset:10240\n\t" \
  "ds_read_b64_tr_b16 v[214:215], v241 offset:12800\n\tds_read_b64_tr_b16 v[216:217], v241 offset:15360\n\t" \
  "ds_read_b64_tr_b16 v[218:219], v241 offset:17920\n\tds_read_b64_tr_b16 v[220:221], v241 offset:20480\n\t" \
  "ds_read_b64_tr_b16 v[222:223], v241 offset:23040\n\tds_read_b64_tr_b16 v[224:225], v241 offset:25600\n\t" \
  "ds_read_b64_tr_b16 v[226:227], v241 offset:28160\n\t.LH2A%=:\ns_cmp_gt_u32 %[w], 2\n\ts_cbranch_scc1 .LH2M%=\n\ts_barrier\n\t" \
  "buffer_load_dwordx2 v[202:203], %[ss], %[rs], s82 offen sc1\n\tbuffer_load_dwordx4 v[194:197], %[s0], %[rs], s81 offen sc1\n\t" \
  "buffer_load_dwordx4 v[198:201], %[s0], %[rs], s81 offen offset:128 sc1\n\ts_waitcnt vmcnt(2) lgkmcnt(0)\n\t.LRED2%=:\n" \
  "v_cmp_ne_u32 s[90:91], 0, v202\n\ts_waitcnt vmcnt(0)\n\tv_permlane32_swap_b32 v194, v195\n\tv_permlane32_swap_b32 v196, v197\n\t" \
  "v_add_f32_dpp v247, v203, v203 quad_perm:[1,0,3,2] row_mask:0xf bank_mask:0xf\n\tv_permlane32_swap_b32 v198, v199\n\t" \
  "v_permlane32_swap_b32 v200, v201\n\tv_add_f32 v194, v194, v195\n\tv_add_f32 v196, v196, v197\n\t" \
  "v_add_f32_dpp v247, v247, v247 quad_perm:[2,3,0,1] row_mask:0xf bank_mask:0xf\n\tv_add_f32 v198, v198, v199\n\tv_add_f32 v200, v200, v201\n\t" \
  "v_permlane16_swap_b32 v194, v196\n\tv_add_f32_dpp v247, v247, v247 row_half_mirror row_mask:0xf bank_mask:0xf\n\tv_permlane16_swap_b32 v198, v200\n\t" \
  "v_add_f32 v194, v194, v196\n\tv_add_f32 v198, v198, v200\n\tv_rcp_f32 v247, v247\n\t" \
  "v_add_f32_dpp v246, v194, v194 row_ror:8 row_mask:0xf bank_mask:0x3\n\tv_add_f32_dpp v246, v198, v198 row_ror:8 row_mask:0xf bank_mask:0xc\n\t" \
  "v_mul_f32 v246, v246, v247\n\tv_cvt_pk_bf16_f32 v247, v246, v246\n\tds_write_b16 v240, v247\n\tv_cmp_u_f32 s[92:93], v246, v246\n\t" \
  "s_or_b64 s[90:91], s[90:91], s[92:93]\n\ts_cbranch_scc1 .LSL2%=\n\t.LH2B%=:\ns_cmp_eq_u32 s80, 0\n\ts_cbranch_scc1 .LWO0%=\n\ts_waitcnt lgkmcnt(0)\n\t" \
  "s_barrier\n\tds_read_b128 v[194:197], v242 offset:%[ov]\n\tds_read_b128 v[198:201], v242 offset:%[ov]+64\n\t" \
  "ds_read_b128 v[228:231], v242 offset:%[ov]+128\n\ts_waitcnt lgkmcnt(2)\n\tv_smfmac_f32_16x16x64_bf16 v[232:235], v[194:197], v[204:211], %[ix]\n\t" \
  "s_waitcnt lgkmcnt(1)\n\tv_smfmac_f32_16x16x64_bf16 v[232:235], v[198:201], v[212:219], %[ix]\n\ts_waitcnt lgkmcnt(0)\n\t" \
  "v_smfmac_f32_16x16x64_bf16 v[232:235], v[228:231], v[220:227], %[ix]\n\t.LWOE%=:\ns_nop 7\n\tv_add_f32 v232, v232, v233\n\t" \
  "buffer_store_dword v232, %[dt], %[rs], s83 offen sc1\n\tds_read_b64 v[240:241], %[t16] offset:%[tab]+8192+8\n\t" \
  "ds_read_b32 %[s0], %[t16] offset:%[tab]+16384+12\n\tds_read_b32 v242, %[t16] offset:%[tab]+32768\n\ts_mov_b32 s81, %[rC]\n\ts_mov_b32 s83, %[eH]\n\t" \
  "v_mov_b64 v[214:215], 0\n\ts_waitcnt lgkmcnt(2)\n\tds_read_b64_tr_b16 v[204:205], v241\n\tds_read_b64_tr_b16 v[206:207], v241 offset:1536\n\t" \
  "ds_read_b64_tr_b16 v[208:209], v241 offset:3072\n\tds_read_b64_tr_b16 v[210:211], v241 offset:4608\n\t" \
  "ds_read_b64_tr_b16 v[212:213], v241 offset:6144\n\ts_waitcnt lgkmcnt(0)\n\ts_barrier\n\t" \
  "buffer_load_dwordx4 v[194:197], %[t16], %[rs], s81 offen sc1\n\tbuffer_load_dwordx4 v[198:201], %[t16], %[rs], s81 offen offset:1024 sc1\n\t" \
  "s_waitcnt vmcnt(0)\n\t.LRED3%=:\nv_pk_add_f32 v[194:195], v[194:195], v[198:199]\n\tv_pk_add_f32 v[196:197], v[196:197], v[200:201]\n\ts_nop 0\n\t" \
  "v_permlane32_swap_b32 v194, v195\n\tv_permlane32_swap_b32 v196, v197\n\tv_add_f32 v194, v194, v195\n\tv_add_f32 v196, v196, v197\n\ts_nop 1\n\t" \
  "v_permlane16_swap_b32 v194, v196\n\tv_add_f32 v194, v194, v196\n\ts_nop 1\n\tv_add_f32_dpp v194, v194, v194 row_ror:8 row_mask:0xf bank_mask:0xf\n\t" \
  "s_nop 1\n\tv_add_f32_dpp v246, v194, v194 row_ror:4 row_mask:0xf bank_mask:0xf\n\tv_cvt_pk_bf16_f32 v247, 0, v246\n\tv_add_f32 v247, v242, v247\n\t" \
  "v_cvt_pk_bf16_f32 v243, v247, 0\n\tds_write_b16 v240, v243\n\tv_cmp_u_f32 vcc, v246, v246\n\ts_cbranch_vccnz .LSL3%=\n\t.LH3B%=:\n" \
  "s_bitcmp0_b32 11, %[w]\n\ts_cbranch_scc1 .LH3M%=\n\ts_waitcnt lgkmcnt(0)\n\ts_barrier\n\tds_read_b128 v[194:197], %[bo] offset:%[xp]\n\t" \
  "ds_read_b128 v[198:201], %[bo] offset:%[xp]+64\n\tds_read_b128 v[228:231], %[bo] offset:%[xp]+128\n\ts_waitcnt lgkmcnt(2)\n\t" \
  "v_mfma_f32_16x16x32_bf16 v[232:235], v[194:197], v[204:207], 0\n\ts_waitcnt lgkmcnt(1)\n\t" \
  "v_mfma_f32_16x16x32_bf16 v[232:235], v[198:201], v[208:211], v[232:235]\n\ts_waitcnt lgkmcnt(0)\n\t" \
  "v_mfma_f32_16x16x32_bf16 v[232:235], v[228:231], v[212:215], v[232:235]\n\ts_nop 7\n\tbuffer_store_dword v232, %[dt], %[rs], s83 offen sc1\n\t" \
  ".LH3E%=:\nv_lshlrev_b32 v243, 16, v243\n\tds_write_b32 %[t16], v243 offset:%[tab]+32768\n\tds_read_b128 v[240:243], %[t16] offset:%[tab]+16384\n\t" \
  "ds_read_b32 v244, %[t16] offset:%[tab]+32768+8\n\ts_add_u32 s82, %[oG], %[osh]\n\ts_add_u32 s81, %[oG], %[oh]\n\ts_sub_u32 s85, %[dE], 0x28000\n\t" \
  "s_cmp_eq_u32 s80, 0\n\ts_cselect_b32 s85, s85, 0x8000\n\ts_add_u32 s83, %[eC], s85\n\tv_mov_b64 v[210:211], 0\n\tv_mov_b32 v245, 0xc0135761\n\t" \
  "s_waitcnt lgkmcnt(1)\n\tds_read_b64_tr_b16 v[204:205], v241\n\tds_read_b64_tr_b16 v[206:207], v241 offset:2560\n\t" \
  "ds_read_b64_tr_b16 v[208:209], v241 offset:5120\n\tds_read_b32 v242, v242\n\ts_cmp_gt_u32 %[w], 1\n\ts_cbranch_scc1 .LH4M%=\n\t" \
  "s_waitcnt lgkmcnt(0)\n\ts_barrier\n\tbuffer_load_dwordx2 v[202:203], %[ss], %[rs], s82 offen sc1\n\t" \
  "buffer_load_dwordx4 v[194:197], %[s0], %[rs], s81 offen sc1\n\ts_waitcnt vmcnt(1)\n\t.LRED4%=:\n" \
  "v_add_f32_dpp v198, v202, v202 quad_perm:[1,0,3,2] row_mask:0xf bank_mask:0xf\n\t" \
  "v_add_f32_dpp v199, v203, v203 quad_perm:[1,0,3,2] row_mask:0xf bank_mask:0xf\n\ts_waitcnt vmcnt(0)\n\tv_permlane32_swap_b32 v194, v195\n\t" \
  "v_add_f32_dpp v198, v198, v198 quad_perm:[2,3,0,1] row_mask:0xf bank_mask:0xf\n\t" \
  "v_add_f32_dpp v199, v199, v199 quad_perm:[2,3,0,1] row_mask:0xf bank_mask:0xf\n\tv_permlane32_swap_b32 v196, v197\n\tv_add_f32 v194, v194, v195\n\t" \
  "v_add_f32_dpp v198, v198, v198 row_half_mirror row_mask:0xf bank_mask:0xf\n\t" \
  "v_add_f32_dpp v199, v199, v199 row_half_mirror row_mask:0xf bank_mask:0xf\n\tv_add_f32 v196, v196, v197\n\tv_mul_f32 v199, 0x3acccccd, v199\n\t" \
  "v_mul_f32 v198, 0x3acccccd, v198\n\tv_permlane16_swap_b32 v194, v196\n\tv_fma_f32 v198, -v199, v199, v198\n\tv_add_f32 v194, v194, v196\n\t" \
  "v_cvt_pk_bf16_f32 v198, 0, v198\n\tv_cvt_pk_bf16_f32 v199, 0, v199\n\tv_add_f32 v198, |v198|, %[eps]\n\t" \
  "v_add_f32_dpp v194, v194, v194 row_ror:8 row_mask:0xf bank_mask:0xf\n\tv_cvt_pk_bf16_f32 v198, 0, v198\n\tv_sqrt_f32 v198, v198\n\t" \
  "v_fma_f32 v194, -v199, v242, v194\n\tv_cvt_pk_bf16_f32 v198, 0, v198\n\tv_rcp_f32 v198, v198\n\ts_nop 0\n\tv_mul_f32 v246, v194, v198\n\t" \
  "v_cvt_pk_bf16_f32 v247, 0, v246\n\tv_add_f32 v247, v244, v247\n\tv_cvt_pk_bf16_f32 v247, 0, v247\n\tv_mul_f32 v198, v247, v247\n\t" \
  "v_fmamk_f32 v198, v198, 0xbdd2d3e7, v245\n\tv_mul_f32 v198, v198, v247\n\tv_exp_f32 v198, v198\n\tv_cmp_u_f32 vcc, v246, v246\n\t" \
  "v_add_f32 v198, 1.0, v198\n\tv_rcp_f32 v198, v198\n\ts_nop 0\n\tv_mul_f32 v198, v198, v247\n\tv_cvt_pk_bf16_f32 v198, v198, v198\n\t" \
  "ds_write_b16 v240, v198\n\ts_cbranch_vccnz .LSL4%=\n\t.LH4B%=:\ns_waitcnt lgkmcnt(0)\n\ts_barrier\n\tds_read_b128 v[194:197], %[bo] offset:%[hv]\n\t" \
  "ds_read_b128 v[198:201], %[bo] offset:%[hv]+64\n\ts_waitcnt lgkmcnt(1)\n\tv_mfma_f32_16x16x32_bf16 v[232:235], v[194:197], v[204:207], 0\n\t" \
  "s_waitcnt lgkmcnt(0)\n\tv_mfma_f32_16x16x32_bf16 v[232:235], v[198:201], v[208:211], v[232:235]\n\ts_nop 7\n\t" \
  "buffer_store_dword v232, %[dt], %[rs], s83 offen sc1\n\t.LH4E%=:\ns_and_b32 s88, s80, %[far]\n\ts_cbranch_scc1 .LEND%=\n\t" \
  "ds_read_b64 v[240:241], %[t16] offset:%[tab]+24576+8\n\tds_read_b32 v242, %[t16] offset:%[tab]+8192+8\n\t" \
  "ds_read_b64 v[244:245], %[t16] offset:%[tab]+32768\n\ts_sub_u32 s85, %[dR], 0x28000\n\ts_cmp_eq_u32 s80, 0\n\ts_cselect_b32 s85, s85, 0x8000\n\t" \
  "s_add_u32 s81, %[rC], s85\n\ts_mov_b32 s83, %[eQ]\n\tv_mov_b64 v[214:215], 0\n\tv_mov_b64 v[226:227], 0\n\ts_waitcnt lgkmcnt(2)\n\t" \
  "ds_read_b64_tr_b16 v[204:205], v240\n\tds_read_b64_tr_b16 v[216:217], v240 offset:32\n\tds_read_b64_tr_b16 v[206:207], v240 offset:6400\n\t" \
  "ds_read_b64_tr_b16 v[218:219], v240 offset:6432\n\tds_read_b64_tr_b16 v[208:209], v240 offset:12800\n\t" \
  "ds_read_b64_tr_b16 v[220:221], v240 offset:12832\n\tds_read_b64_tr_b16 v[210:211], v240 offset:19200\n\t" \
  "ds_read_b64_tr_b16 v[222:223], v240 offset:19232\n\tds_read_b64_tr_b16 v[212:213], v240 offset:25600\n\t" \
  "ds_read_b64_tr_b16 v[224:225], v240 offset:25632\n\ts_cmp_gt_u32 %[w], 4\n\ts_cbranch_scc1 .LH5M%=\n\ts_waitcnt lgkmcnt(0)\n\ts_barrier\n\t" \
  "buffer_load_dwordx4 v[194:197], %[t16], %[rs], s81 offen sc1\n\tbuffer_load_dwordx4 v[198:201], %[t16], %[rs], s81 offen offset:1024 sc1\n\t" \
  "s_waitcnt vmcnt(0)\n\t.LRED5%=:\nv_pk_add_f32 v[194:195], v[194:195], v[198:199]\n\tv_pk_add_f32 v[196:197], v[196:197], v[200:201]\n\ts_nop 0\n\t" \
  "v_permlane32_swap_b32 v194, v195\n\tv_permlane32_swap_b32 v196, v197\n\tv_add_f32 v194, v194, v195\n\tv_add_f32 v196, v196, v197\n\ts_nop 1\n\t" \
  "v_permlane16_swap_b32 v194, v196\n\tv_add_f32 v194, v194, v196\n\ts_nop 1\n\tv_add_f32_dpp v194, v194, v194 row_ror:8 row_mask:0xf bank_mask:0xf\n\t" \
  "s_nop 1\n\tv_add_f32_dpp v246, v194, v194 row_ror:4 row_mask:0xf bank_mask:0xf\n\tv_cvt_pk_bf16_f32 v247, 0, v246\n\tv_cmp_u_f32 vcc, v246, v246\n\t" \
  "v_add_f32 v247, v245, v247\n\tv_cvt_pk_bf16_f32 v247, 0, v247\n\tv_add_f32 v247, v244, v247\n\tv_cvt_pk_bf16_f32 v243, v247, 0\n\t" \
  "ds_write_b16 v242, v243\n\ts_cbranch_vccnz .LSL5%=\n\t.LH5B%=:\ns_cmp_lg_u32 s80, 0\n\ts_cbranch_scc1 .LFIN%=\n\t.LH5C%=:\ns_waitcnt lgkmcnt(0)\n\t" \
  "s_barrier\n\tds_read_b128 v[194:197], %[bo] offset:%[xp]\n\tds_read_b128 v[198:201], %[bo] offset:%[xp]+64\n\t" \
  "ds_read_b128 v[228:231], %[bo] offset:%[xp]+128\n\ts_waitcnt lgkmcnt(2)\n\tv_mfma_f32_16x16x32_bf16 v[232:235], v[194:197], v[204:207], 0\n\t" \
  "v_mfma_f32_16x16x32_bf16 v[236:239], v[194:197], v[216:219], 0\n\ts_waitcnt lgkmcnt(1)\n\t" \
  "v_mfma_f32_16x16x32_bf16 v[232:235], v[198:201], v[208:211], v[232:235]\n\tv_mfma_f32_16x16x32_bf16 v[236:239], v[198:201], v[220:223], v[236:239]\n\t" \
  "s_waitcnt lgkmcnt(0)\n\tv_mfma_f32_16x16x32_bf16 v[232:235], v[228:231], v[212:215], v[232:235]\n\t" \
  "v_mfma_f32_16x16x32_bf16 v[236:239], v[228:231], v[224:227], v[236:239]\n\ts_nop 6\n\tbuffer_store_dword v232, %[dt], %[rs], s83 offen sc1\n\t" \
  "buffer_store_dword v236, %[dt], %[rs], s83 offen offset:64 sc1\n\tv_lshlrev_b32 v243, 16, v243\n\tds_write_b32 %[t16], v243 offset:%[tab]+32768\n\t" \
  ".LH1%=:\nds_read_b64 v[204:205], %[t16] offset:%[tab]+24576\n\tds_read_b128 v[240:243], %[t16] offset:%[tab]\n\t" \
  "ds_read_b128 v[236:239], %[t16] offset:%[tab]+40960\n\ts_add_u32 %[oG], %[oG], %[dG]\n\ts_add_u32 %[eH], %[eH], %[dH]\n\t" \
  "s_add_u32 %[rC], %[rC], %[dR]\n\ts_add_u32 %[eC], %[eC], %[dE]\n\ts_sub_u32 s81, %[oG], 0x10000\n\ts_add_u32 s82, s81, %[osg]\n\t" \
  "s_mov_b32 s92, 0x00ff00ff\n\ts_mov_b32 s93, 0xff00ff00\n\tv_mov_b64 v[208:209], 0\n\tv_mov_b64 v[210:211], 0\n\tv_mov_b64 v[212:213], 0\n\t" \
  "v_mov_b64 v[214:215], 0\n\tv_mov_b64 v[216:217], 0\n\tv_mov_b64 v[218:219], 0\n\tv_mov_b64 v[220:221], 0\n\tv_mov_b64 v[222:223], 0\n\t" \
  "v_mov_b64 v[224:225], 0\n\tv_mov_b64 v[226:227], 0\n\tv_mov_b64 v[228:229], 0\n\tv_mov_b64 v[230:231], 0\n\tv_mov_b64 v[232:233], 0\n\t" \
  "v_mov_b64 v[234:235], 0\n\ts_waitcnt lgkmcnt(2)\n\tds_read_b32 v205, v205\n\ts_cmp_gt_u32 %[w], 2\n\ts_cbranch_scc1 .LH1X%=\n\t" \
  "s_waitcnt lgkmcnt(0)\n\ts_barrier\n\tbuffer_load_dwordx2 v[202:203], %[ss], %[rs], s82 offen sc1\n\t" \
  "buffer_load_dwordx4 v[194:197], v243, %[rs], s81 offen sc1\n\tbuffer_load_dwordx4 v[198:201], v243, %[rs], s81 offen offset:128 sc1\n\t" \
  "s_waitcnt vmcnt(0)\n\t.LRED1%=:\nv_permlane32_swap_b32 v194, v195\n\tv_permlane32_swap_b32 v196, v197\n\t" \
  "v_add_f32_dpp v206, v203, v203 quad_perm:[1,0,3,2] row_mask:0xf bank_mask:0xf\n\tv_permlane32_swap_b32 v198, v199\n\t" \
  "v_permlane32_swap_b32 v200, v201\n\tv_add_f32 v194, v194, v195\n\tv_add_f32 v196, v196, v197\n\t" \
  "v_add_f32_dpp v206, v206, v206 quad_perm:[2,3,0,1] row_mask:0xf bank_mask:0xf\n\tv_add_f32 v198, v198, v199\n\tv_add_f32 v200, v200, v201\n\t" \
  "v_permlane16_swap_b32 v194, v196\n\tv_add_f32_dpp v206, v206, v206 row_half_mirror row_mask:0xf bank_mask:0xf\n\tv_permlane16_swap_b32 v198, v200\n\t" \
  "v_add_f32 v194, v194, v196\n\tv_add_f32 v198, v198, v200\n\tv_mul_f32 v206, 0x3acccccd, v206\n\t" \
  "v_add_f32_dpp v246, v194, v194 row_ror:8 row_mask:0xf bank_mask:0x3\n\tv_add_f32_dpp v246, v198, v198 row_ror:8 row_mask:0xf bank_mask:0xc\n\t" \
  "v_cvt_pk_bf16_f32 v247, 0, v206\n\tv_fma_f32 v246, -v247, v205, v246\n\tv_cvt_pk_bf16_f32 v247, v246, v246\n\tds_write_b16 v204, v247\n\t" \
  "v_cmp_u_f32 vcc, v246, v246\n\ts_cbranch_vccnz .LSL1%=\n\t.LH1B%=:\ns_waitcnt lgkmcnt(0)\n\ts_barrier\n\tds_read_b128 v[194:197], v236\n\t" \
  "ds_read_b128 v[198:201], v236 offset:64\n\tds_read_b128 v[204:207], v236 offset:128\n\tds_read_b32 v202, v239 offset:%[qsw]\n\t" \
  "s_waitcnt lgkmcnt(3)\n\tv_smfmac_f32_16x16x64_bf16 v[208:211], v[194:197], v[2:9], %[ix]\n\t" \
  "v_smfmac_f32_16x16x64_bf16 v[212:215], v[194:197], v[26:33], %[ix]\n\tv_smfmac_f32_16x16x64_bf16 v[216:219], v[194:197], v[50:57], %[ix]\n\t" \
  "v_smfmac_f32_16x16x64_bf16 v[220:223], v[194:197], v[74:81], %[ix]\n\ts_waitcnt lgkmcnt(2)\n\t" \
  "v_smfmac_f32_16x16x64_bf16 v[208:211], v[198:201], v[10:17], %[ix]\n\tv_smfmac_f32_16x16x64_bf16 v[212:215], v[198:201], v[34:41], %[ix]\n\t" \
  "v_smfmac_f32_16x16x64_bf16 v[216:219], v[198:201], v[58:65], %[ix]\n\tv_smfmac_f32_16x16x64_bf16 v[220:223], v[198:201], v[82:89], %[ix]\n\t" \
  "s_waitcnt lgkmcnt(1)\n\tv_smfmac_f32_16x16x64_bf16 v[208:211], v[204:207], v[18:25], %[ix]\n\t" \
  "v_smfmac_f32_16x16x64_bf16 v[212:215], v[204:207], v[42:49], %[ix]\n\tv_smfmac_f32_16x16x64_bf16 v[216:219], v[204:207], v[66:73], %[ix]\n\t" \
  "v_smfmac_f32_16x16x64_bf16 v[220:223], v[204:207], v[90:97], %[ix]\n\ts_setprio 1\n\ts_nop 3\n\tv_add_f32 v246, v208, v209\n\t" \
  "v_add_f32 v203, v212, v213\n\tv_cndmask_b32 v246, v203, v246, s[92:93]\n\tv_add_f32 v247, v216, v217\n\tv_add_f32 v203, v220, v221\n\t" \
  "v_cndmask_b32 v247, v203, v247, s[92:93]\n\ts_waitcnt lgkmcnt(0)\n\ts_nop 0\n\tv_permlane32_swap_b32 v246, v247\n\ts_nop 1\n\t" \
  "v_add_f32_dpp v246, v247, v246 row_ror:8 row_mask:0xf bank_mask:0xf\n\tv_mul_f32 v246, v246, v202\n\tv_cvt_pk_bf16_f32 v246, 0, v246\n\t" \
  "v_mul_f32 v246, 0x3d93cd3a, v246\n\tv_cvt_pk_bf16_f32 v246, 0, v246\n\tv_mul_f32 v246, 0x3fb8aa3b, v246\n\tv_exp_f32 v203, v246\n\t" \
  "v_cmp_nle_f32 vcc, |v246|, %[small]\n\tv_cvt_pk_bf16_f32 v244, v203, 0\n\tv_add_f32_dpp v203, v203, v203 row_ror:1 row_mask:0xf bank_mask:0xf\n\t" \
  "s_nop 0\n\tv_mov_b32_dpp v245, v244 row_shl:1 row_mask:0xf bank_mask:0xf\n\ts_cbranch_vccnz .LSLM%=\n\t.LPV%=:\ns_setprio 0\n\t" \
  "v_mov_b64_dpp v[208:209], v[244:245] row_newbcast:0 row_mask:0xf bank_mask:0xf\n\t" \
  "v_mov_b64_dpp v[210:211], v[244:245] row_newbcast:2 row_mask:0xf bank_mask:0xf\n\t" \
  "v_add_f32_dpp v203, v203, v203 row_ror:2 row_mask:0xf bank_mask:0xf\n\t" \
  "v_mov_b64_dpp v[212:213], v[244:245] row_newbcast:4 row_mask:0xf bank_mask:0xf\n\t" \
  "v_smfmac_f32_16x16x64_bf16 v[224:227], v[208:211], v[98:105], %[ix] abid:1\n\t" \
  "v_mov_b64_dpp v[214:215], v[244:245] row_newbcast:6 row_mask:0xf bank_mask:0xf\n\t" \
  "v_smfmac_f32_16x16x64_bf16 v[228:231], v[208:211], v[106:113], %[ix] abid:1\n\tv_add_f32_dpp v203, v203, v203 row_ror:4 row_mask:0xf bank_mask:0xf\n\t" \
  "v_smfmac_f32_16x16x64_bf16 v[232:235], v[208:211], v[114:121], %[ix] abid:1\n\t" \
  "v_smfmac_f32_16x16x64_bf16 v[224:227], v[212:215], v[122:129], %[ix] abid:1\n\t" \
  "v_mov_b64_dpp v[208:209], v[244:245] row_newbcast:8 row_mask:0xf bank_mask:0xf\n\t" \
  "v_mov_b64_dpp v[210:211], v[244:245] row_newbcast:10 row_mask:0xf bank_mask:0xf\n\t" \
  "v_smfmac_f32_16x16x64_bf16 v[228:231], v[212:215], v[130:137], %[ix] abid:1\n\tv_add_f32_dpp v203, v203, v203 row_ror:8 row_mask:0xf bank_mask:0xf\n\t" \
  "v_smfmac_f32_16x16x64_bf16 v[232:235], v[212:215], v[138:145], %[ix] abid:1\n\t" \
  "v_smfmac_f32_16x16x64_bf16 v[224:227], v[208:211], v[146:153], %[ix] abid:1\n\t" \
  "v_mov_b64_dpp v[212:213], v[244:245] row_newbcast:12 row_mask:0xf bank_mask:0xf\n\t" \
  "v_mov_b64_dpp v[214:215], v[244:245] row_newbcast:14 row_mask:0xf bank_mask:0xf\n\t" \
  "v_smfmac_f32_16x16x64_bf16 v[228:231], v[208:211], v[154:161], %[ix] abid:1\n\t" \
  "v_add_f32_dpp v203, v203, v203 row_bcast:15 row_mask:0xa bank_mask:0xf\n\t" \
  "v_smfmac_f32_16x16x64_bf16 v[232:235], v[208:211], v[162:169], %[ix] abid:1\n\t" \
  "v_smfmac_f32_16x16x64_bf16 v[224:227], v[212:215], v[170:177], %[ix] abid:1\n\t" \
  "v_smfmac_f32_16x16x64_bf16 v[228:231], v[212:215], v[178:185], %[ix] abid:1\n\t" \
  "v_smfmac_f32_16x16x64_bf16 v[232:235], v[212:215], v[186:193], %[ix] abid:1\n\t" \
  "v_add_f32_dpp v203, v203, v203 row_bcast:31 row_mask:0xc bank_mask:0xf\n\ts_lshl_b64 exec, 1, 63\n\tds_write2_b32 v238, v239, v203 offset1:1\n\t" \
  "s_mov_b64 exec, -1\n\ts_mov_b32 s80, 1\n\ts_mov_b32 s83, %[oD]\n\ts_nop 0\n\tds_write2st64_b32 v237, v224, v228 offset1:1\n\t" \
  "ds_write_b32 v237, v232 offset:512\n\ts_waitcnt lgkmcnt(0)\n\ts_barrier\n\tds_read_b128 v[194:197], v241\n\tds_read_b32 v204, v240\n\t" \
  "v_mov_b32 %[s0], v243\n\ts_waitcnt lgkmcnt(1)\n\t.LCMB%=:\nv_add_f32_dpp v205, v194, v194 row_ror:8 row_mask:0xf bank_mask:0x3\n\t" \
  "v_add_f32_dpp v206, v195, v195 row_ror:8 row_mask:0xf bank_mask:0x3\n\tv_add_f32_dpp v205, v196, v196 row_ror:8 row_mask:0xf bank_mask:0xc\n\t" \
  "v_add_f32_dpp v206, v197, v197 row_ror:8 row_mask:0xf bank_mask:0xc\n\ts_waitcnt lgkmcnt(0)\n\tv_cmp_ne_u32 vcc, 0, v204\n\t" \
  "v_add_f32_dpp v246, v205, v205 row_ror:12 row_mask:0xf bank_mask:0x5\n\tv_add_f32_dpp v246, v206, v206 row_ror:4 row_mask:0xf bank_mask:0xa\n\t" \
  "s_nop 1\n\tv_add_f32_dpp v246, v246, v246 quad_perm:[1,0,3,2] row_mask:0xf bank_mask:0xf\n\ts_cbranch_vccnz .LSLC%=\n\t.LCMS%=:\n" \
  "buffer_store_dword v246, v242, %[rs], s83 offen sc1\n\ts_branch .LLOOP%=\n\t.LWO0%=:\ns_waitcnt lgkmcnt(0)\n\ts_barrier\n\t" \
  "ds_read_b128 v[194:197], v242 offset:%[ov]+0\n\tds_read_b64_tr_b16 v[204:205], v241\n\tds_read_b64_tr_b16 v[206:207], v241 offset:2560\n\t" \
  "ds_read_b64_tr_b16 v[208:209], v241 offset:5120\n\tds_read_b64_tr_b16 v[210:211], v241 offset:7680\n\t" \
  "ds_read_b128 v[198:201], v242 offset:%[ov]+64\n\tds_read_b64_tr_b16 v[212:213], v241 offset:10240\n\t" \
  "ds_read_b64_tr_b16 v[214:215], v241 offset:12800\n\tds_read_b64_tr_b16 v[216:217], v241 offset:15360\n\t" \
  "ds_read_b64_tr_b16 v[218:219], v241 offset:17920\n\tds_read_b128 v[228:231], v242 offset:%[ov]+128\n\t" \
  "ds_read_b64_tr_b16 v[220:221], v241 offset:20480\n\tds_read_b64_tr_b16 v[222:223], v241 offset:23040\n\t" \
  "ds_read_b64_tr_b16 v[224:225], v241 offset:25600\n\tds_read_b64_tr_b16 v[226:227], v241 offset:28160\n\ts_waitcnt lgkmcnt(10)\n\t" \
  "v_smfmac_f32_16x16x64_bf16 v[232:235], v[194:197], v[204:211], %[ix]\n\ts_waitcnt lgkmcnt(5)\n\t" \
  "v_smfmac_f32_16x16x64_bf16 v[232:235], v[198:201], v[212:219], %[ix]\n\ts_waitcnt lgkmcnt(0)\n\t" \
  "v_smfmac_f32_16x16x64_bf16 v[232:235], v[228:231], v[220:227], %[ix]\n\ts_branch .LWOE%=\n\t.LH1X%=:\ns_cmp_eq_u32 %[w], 3\n\t" \
  "s_cbranch_scc1 .LH1S%=\n\ts_cmp_lg_u32 %[w], 7\n\ts_cbranch_scc1 .LH1Y%=\n\ts_mov_b32 s88, %[p1]\n\ts_swappc_b64 s[96:97], s[98:99]\n\t.LH1Y%=:\n" \
  "s_barrier\n\ts_branch .LH1B%=\n\t.LH1S%=:\ns_waitcnt lgkmcnt(0)\n\ts_barrier\n\tbuffer_load_dwordx2 v[202:203], %[ss], %[rs], s82 offen sc1\n\t" \
  "s_waitcnt vmcnt(0)\n\t.LREDS%=:\nv_add_f32_dpp v194, v202, v202 quad_perm:[1,0,3,2] row_mask:0xf bank_mask:0xf\n\t" \
  "v_add_f32_dpp v195, v203, v203 quad_perm:[1,0,3,2] row_mask:0xf bank_mask:0xf\n\ts_nop 0\n\t" \
  "v_add_f32_dpp v194, v194, v194 quad_perm:[2,3,0,1] row_mask:0xf bank_mask:0xf\n\t" \
  "v_add_f32_dpp v195, v195, v195 quad_perm:[2,3,0,1] row_mask:0xf bank_mask:0xf\n\ts_nop 0\n\t" \
  "v_add_f32_dpp v194, v194, v194 row_half_mirror row_mask:0xf bank_mask:0xf\n\t" \
  "v_add_f32_dpp v195, v195, v195 row_half_mirror row_mask:0xf bank_mask:0xf\n\tv_mul_f32 v194, 0x3acccccd, v194\n\tv_mul_f32 v195, 0x3acccccd, v195\n\t" \
  "v_fma_f32 v194, -v195, v195, v194\n\tv_cvt_pk_bf16_f32 v194, 0, v194\n\tv_add_f32 v194, |v194|, %[eps]\n\tv_cvt_pk_bf16_f32 v194, 0, v194\n\t" \
  "v_sqrt_f32 v194, v194\n\ts_nop 0\n\tv_cvt_pk_bf16_f32 v194, 0, v194\n\tv_rcp_f32 v194, v194\n\ts_nop 0\n\tds_write_b32 v239, v194 offset:%[qsw]\n\t" \
  "v_cmp_u_f32 vcc, v194, v194\n\ts_cbranch_vccz .LH1B%=\n\ts_branch .LSLS%=\n\t.LH2M%=:\ns_barrier\n\ts_waitcnt vmcnt(0)\n\ts_branch .LH2B%=\n\t" \
  ".LH3M%=:\ns_waitcnt lgkmcnt(0)\n\ts_barrier\n\ts_branch .LH3E%=\n\t.LH4M%=:\ns_barrier\n\ts_branch .LH4B%=\n\t.LH5M%=:\ns_barrier\n\t" \
  "s_cmp_eq_u32 s80, 0\n\ts_cbranch_scc1 .LH5C%=\n\ts_branch .LFE%=\n\t.LFIN%=:\nbuffer_store_short v243, v241, %[ro], %[oo] offen\n\ts_branch .LFE%=\n\t" \
  ".LH2X%=:\ns_cmp_eq_u32 %[w], 7\n\ts_cbranch_scc1 .LTW%=\n\tds_read_b64 v[240:241], %[t16] offset:%[tab]+16384\n\tv_mov_b64 v[198:199], -1\n\t" \
  "v_mov_b64 v[200:201], -1\n\ts_cmp_lg_u32 s80, 0\n\ts_waitcnt lgkmcnt(0)\n\ts_barrier\n\ts_cbranch_scc1 .LH2Y%=\n\t" \
  "buffer_store_dwordx4 v[198:201], v240, %[rs], 0 offen sc1\n\t.LH2Y%=:\ns_waitcnt vmcnt(0)\n\ts_barrier\n\ts_mov_b32 s83, %[eH]\n\t" \
  "s_cmp_eq_u32 %[w], 6\n\ts_cbranch_scc1 .LSW3%=\n\ts_movk_i32 s88, 0x2000\n\ts_cmp_lg_u32 s80, 0\n\ts_barrier\n\ts_barrier\n\t" \
  "s_cbranch_scc1 .LH3Y%=\n\tbuffer_store_dwordx4 v[198:201], v241, %[rs], 0 offen sc1\n\tbuffer_store_dwordx4 v[198:201], v241, %[rs], s88 offen sc1\n\t" \
  ".LH3Y%=:\ns_barrier\n\ts_waitcnt vmcnt(0)\n\ts_barrier\n\ts_branch .LH4E%=\n\t.LSW3%=:\ns_barrier\n\ts_barrier\n\t" \
  "ds_read_b128 v[194:197], %[bo] offset:%[xp]\n\tds_read_b128 v[204:207], %[ss]\n\tds_read_b128 v[198:201], %[bo] offset:%[xp]+64\n\t" \
  "ds_read_b128 v[208:211], %[ss] offset:64\n\tds_read_b128 v[228:231], %[bo] offset:%[xp]+128\n\tds_read_b128 v[212:215], %[ss] offset:128\n\t" \
  "s_waitcnt lgkmcnt(4)\n\tv_mfma_f32_16x16x32_bf16 v[232:235], v[194:197], v[204:207], 0\n\ts_waitcnt lgkmcnt(2)\n\t" \
  "v_mfma_f32_16x16x32_bf16 v[232:235], v[198:201], v[208:211], v[232:235]\n\ts_waitcnt lgkmcnt(0)\n\t" \
  "v_mfma_f32_16x16x32_bf16 v[232:235], v[228:231], v[212:215], v[232:235]\n\ts_nop 7\n\tbuffer_store_dword v232, %[dt], %[rs], s83 offen sc1\n\t" \
  "s_barrier\n\ts_barrier\n\ts_and_b32 s88, s80, %[far]\n\ts_cbranch_scc1 .LEND%=\n\ts_mov_b32 s83, %[eQ]\n\ts_barrier\n\ts_cmp_lg_u32 s80, 0\n\t" \
  "s_cbranch_scc1 .LFE%=\n\ts_barrier\n\tds_read_b128 v[194:197], %[bo] offset:%[xp]\n\tds_read_b128 v[204:207], %[ss]\n\t" \
  "ds_read_b128 v[198:201], %[bo] offset:%[xp]+64\n\tds_read_b128 v[208:211], %[ss] offset:64\n\tds_read_b128 v[228:231], %[bo] offset:%[xp]+128\n\t" \
  "ds_read_b128 v[212:215], %[ss] offset:128\n\ts_waitcnt lgkmcnt(4)\n\tv_mfma_f32_16x16x32_bf16 v[232:235], v[194:197], v[204:207], 0\n\t" \
  "s_waitcnt lgkmcnt(2)\n\tv_mfma_f32_16x16x32_bf16 v[232:235], v[198:201], v[208:211], v[232:235]\n\ts_waitcnt lgkmcnt(0)\n\t" \
  "v_mfma_f32_16x16x32_bf16 v[232:235], v[228:231], v[212:215], v[232:235]\n\ts_nop 7\n\tbuffer_store_dword v232, %[dt], %[rs], s83 offen sc1\n\t" \
  "s_branch .LH1%=\n\t.LTW%=:\ns_cmp_lg_u32 s80, 0\n\ts_cbranch_scc1 .LTW1%=\n\ts_memrealtime s[94:95]\n\tv_and_b32 v240, 0x3e0, %[t16]\n\t" \
  "v_lshlrev_b32 v241, 9, %[t16]\n\tv_and_b32 v241, 0x2000, v241\n\tv_cmp_eq_u32 vcc, 0, v240\n\tv_mov_b32 v240, %[oor]\n\t" \
  "v_cndmask_b32 v240, v240, v241, vcc\n\tv_mov_b32 v243, %[tag]\n\ts_waitcnt lgkmcnt(0)\n\ts_mov_b32 %[tc], s94\n\tv_mov_b32 v242, s94\n\t" \
  "buffer_store_dwordx2 v[242:243], v240, %[rs], %[cw] offen sc1\n\ts_waitcnt vmcnt(4)\n\ts_mov_b32 s87, %[pw]\n\t.LW1%=:\ns_sub_u32 s87, s87, 1\n\t" \
  "s_cbranch_scc0 .LW1%=\n\ts_barrier\n\ts_waitcnt vmcnt(1)\n\ts_barrier\n\ts_mov_b32 s87, %[spin]\n\t.LTWP%=:\n" \
  "buffer_load_dwordx4 v[194:197], %[t16], %[rs], %[rK] offen sc1\n\tbuffer_load_dwordx4 v[198:201], %[t16], %[rs], %[rK] offen offset:1024 sc1\n\t" \
  "s_waitcnt vmcnt(0)\n\tv_xor_b32 v247, v195, v243\n\tv_xor_b32 v246, v197, v243\n\tv_xor_b32 v199, v199, v243\n\tv_xor_b32 v201, v201, v243\n\t" \
  "v_or3_b32 v247, v247, v246, v199\n\tv_or_b32 v247, v247, v201\n\tv_cmp_ne_u32 vcc, 0, v247\n\ts_cbranch_vccz .LTWQ%=\n\ts_sleep 3\n\t" \
  "s_sub_u32 s87, s87, 1\n\ts_cbranch_scc0 .LTWP%=\n\t.LTWQ%=:\nv_subrev_u32 v194, %[tc], v194\n\tv_subrev_u32 v196, %[tc], v196\n\t" \
  "v_subrev_u32 v198, %[tc], v198\n\tv_subrev_u32 v200, %[tc], v200\n\tv_max_i32 v247, v194, v196\n\tv_max3_i32 v247, v198, v200, v247\n\ts_nop 1\n\t" \
  "v_max_i32_dpp v247, v247, v247 row_ror:1 row_mask:0xf bank_mask:0xf\n\ts_nop 1\n\t" \
  "v_max_i32_dpp v247, v247, v247 row_ror:2 row_mask:0xf bank_mask:0xf\n\ts_nop 1\n\t" \
  "v_max_i32_dpp v247, v247, v247 row_ror:4 row_mask:0xf bank_mask:0xf\n\ts_nop 1\n\t" \
  "v_max_i32_dpp v247, v247, v247 row_ror:8 row_mask:0xf bank_mask:0xf\n\ts_nop 1\n\t" \
  "v_max_i32_dpp v247, v247, v247 row_bcast:15 row_mask:0xa bank_mask:0xf\n\ts_nop 1\n\t" \
  "v_max_i32_dpp v247, v247, v247 row_bcast:31 row_mask:0xc bank_mask:0xf\n\ts_nop 1\n\tv_readlane_b32 s88, v247, 63\n\t" \
  "s_add_u32 %[tc], %[tc], %[p3e]\n\ts_add_u32 %[tc], %[tc], s88\n\ts_mov_b32 %[te], %[tc]\n\ts_add_u32 s94, s98, .LUNT%=-.LCLK%=\n\t" \
  "s_addc_u32 s95, s99, 0\n\ts_swappc_b64 s[96:97], s[94:95]\n\ts_branch .LTW3%=\n\t.LTW1%=:\ns_add_u32 %[tc], %[tc], %[p2]\n\ts_mov_b32 s87, %[pw]\n\t" \
  ".LW2%=:\ns_sub_u32 s87, s87, 1\n\ts_cbranch_scc0 .LW2%=\n\ts_barrier\n\ts_barrier\n\ts_mov_b32 s88, %[p3]\n\ts_swappc_b64 s[96:97], s[98:99]\n\t" \
  ".LTW3%=:\ns_barrier\n\ts_barrier\n\ts_mov_b32 s88, %[p4]\n\ts_swappc_b64 s[96:97], s[98:99]\n\ts_barrier\n\ts_barrier\n\t" \
  "s_and_b32 s88, s80, %[far]\n\ts_cbranch_scc1 .LEND%=\n\ts_mov_b32 s88, %[p5]\n\ts_swappc_b64 s[96:97], s[98:99]\n\ts_barrier\n\t" \
  "s_cmp_lg_u32 s80, 0\n\ts_cbranch_scc1 .LFE%=\n\ts_barrier\n\ts_branch .LH1%=\n\t.LCLK%=:\ns_memrealtime s[94:95]\n\ts_add_u32 %[tc], %[tc], s88\n\t" \
  "s_movk_i32 s87, 64\n\ts_waitcnt lgkmcnt(0)\n\t.LCL1%=:\ns_sub_i32 s88, %[tc], s94\n\ts_cmp_ge_i32 s88, %[rmin]\n\ts_cbranch_scc1 .LUNT%=\n\t" \
  "s_add_u32 %[tc], %[tc], %[rcons]\n\ts_sub_u32 s87, s87, 1\n\ts_cbranch_scc0 .LCL1%=\n\t.LUNT%=:\ns_movk_i32 s87, 0x1000\n\t.LUN1%=:\n" \
  "s_memrealtime s[94:95]\n\ts_sub_u32 s87, s87, 1\n\ts_cbranch_scc1 .LUN2%=\n\ts_waitcnt lgkmcnt(0)\n\ts_sub_i32 s88, %[tc], s94\n\t" \
  "s_cmp_gt_i32 s88, 3\n\ts_cbranch_scc1 .LUN1%=\n\ts_cmp_lt_i32 s88, 1\n\ts_cbranch_scc1 .LUN2%=\n\ts_nop 3\n\ts_cmp_lt_i32 s88, 2\n\t" \
  "s_cbranch_scc1 .LUN2%=\n\ts_nop 3\n\ts_cmp_lt_i32 s88, 3\n\ts_cbranch_scc1 .LUN2%=\n\ts_nop 9\n\t.LUN2%=:\ns_waitcnt lgkmcnt(0)\n\t" \
  "s_setpc_b64 s[96:97]\n\t.LSL2%=:\ns_add_u32 s88, s80, 2\n\ts_cmp_eq_u32 s84, s88\n\ts_cbranch_scc1 .LH2B%=\n\ts_mov_b32 s84, s88\n\t" \
  "s_mov_b32 s87, %[spin]\n\t.LSL2a%=:\nbuffer_load_dwordx2 v[202:203], %[ss], %[rs], s82 offen sc1\n\t" \
  "buffer_load_dwordx4 v[194:197], %[s0], %[rs], s81 offen sc1\n\tbuffer_load_dwordx4 v[198:201], %[s0], %[rs], s81 offen offset:128 sc1\n\t" \
  "s_waitcnt vmcnt(0)\n\tv_max3_u32 v247, v194, v195, v196\n\tv_max3_u32 v247, v247, v197, v198\n\tv_max3_u32 v247, v247, v199, v200\n\t" \
  "v_max3_u32 v247, v247, v201, v202\n\tv_max_u32 v247, v247, v203\n\tv_cmp_eq_u32 vcc, -1, v247\n\ts_cbranch_vccz .LSL2b%=\n\ts_sub_u32 s87, s87, 1\n\t" \
  "s_cbranch_scc0 .LSL2a%=\n\t.LSL2b%=:\nv_cmp_ne_u32 vcc, 0, v202\n\ts_cbranch_vccz .LRED2%=\n\tv_lshrrev_b32 v247, 5, %[t16]\n\t" \
  "v_and_b32 v247, 28, v247\n\tds_bpermute_b32 v247, v247, v202\n\tv_max_f32_dpp v246, v202, v202 quad_perm:[1,0,3,2] row_mask:0xf bank_mask:0xf\n\t" \
  "s_nop 1\n\tv_max_f32_dpp v246, v246, v246 quad_perm:[2,3,0,1] row_mask:0xf bank_mask:0xf\n\ts_nop 1\n\t" \
  "v_max_f32_dpp v246, v246, v246 row_half_mirror row_mask:0xf bank_mask:0xf\n\ts_waitcnt lgkmcnt(0)\n\tv_sub_f32 v247, v247, v246\n\t" \
  "v_sub_f32 v202, v202, v246\n\tv_exp_f32 v247, v247\n\tv_exp_f32 v202, v202\n\ts_nop 0\n\tv_mul_f32 v194, v247, v194\n\tv_mul_f32 v195, v247, v195\n\t" \
  "v_mul_f32 v196, v247, v196\n\tv_mul_f32 v197, v247, v197\n\tv_mul_f32 v198, v247, v198\n\tv_mul_f32 v199, v247, v199\n\tv_mul_f32 v200, v247, v200\n\t" \
  "v_mul_f32 v201, v247, v201\n\tv_mul_f32 v203, v202, v203\n\tv_mov_b32 v202, 0\n\ts_nop 1\n\ts_branch .LRED2%=\n\t.LSL3%=:\ns_add_u32 s88, s80, 4\n\t" \
  "s_cmp_eq_u32 s84, s88\n\ts_cbranch_scc1 .LH3B%=\n\ts_mov_b32 s84, s88\n\ts_mov_b32 s87, %[spin]\n\t.LSL3a%=:\n" \
  "buffer_load_dwordx4 v[194:197], %[t16], %[rs], s81 offen sc1\n\tbuffer_load_dwordx4 v[198:201], %[t16], %[rs], s81 offen offset:1024 sc1\n\t" \
  "s_waitcnt vmcnt(0)\n\tv_max3_u32 v247, v194, v195, v196\n\tv_max3_u32 v247, v247, v197, v198\n\tv_max3_u32 v247, v247, v199, v200\n\t" \
  "v_max_u32 v247, v247, v201\n\tv_cmp_eq_u32 vcc, -1, v247\n\ts_cbranch_vccz .LSL3b%=\n\ts_sleep 1\n\ts_sub_u32 s87, s87, 1\n\t" \
  "s_cbranch_scc0 .LSL3a%=\n\t.LSL3b%=:\ns_branch .LRED3%=\n\t.LSL4%=:\ns_add_u32 s88, s80, 6\n\ts_cmp_eq_u32 s84, s88\n\ts_cbranch_scc1 .LH4B%=\n\t" \
  "s_mov_b32 s84, s88\n\ts_mov_b32 s87, %[spin]\n\t.LSL4a%=:\nbuffer_load_dwordx2 v[202:203], %[ss], %[rs], s82 offen sc1\n\t" \
  "buffer_load_dwordx4 v[194:197], %[s0], %[rs], s81 offen sc1\n\ts_waitcnt vmcnt(0)\n\tv_max3_u32 v247, v194, v195, v196\n\t" \
  "v_max3_u32 v247, v247, v197, v202\n\tv_max_u32 v247, v247, v203\n\tv_cmp_eq_u32 vcc, -1, v247\n\ts_cbranch_vccz .LSL4b%=\n\ts_sleep 1\n\t" \
  "s_sub_u32 s87, s87, 1\n\ts_cbranch_scc0 .LSL4a%=\n\t.LSL4b%=:\ns_branch .LRED4%=\n\t.LSL5%=:\ns_add_u32 s88, s80, 8\n\ts_cmp_eq_u32 s84, s88\n\t" \
  "s_cbranch_scc1 .LH5B%=\n\ts_mov_b32 s84, s88\n\ts_mov_b32 s87, %[spin]\n\t.LSL5a%=:\nbuffer_load_dwordx4 v[194:197], %[t16], %[rs], s81 offen sc1\n\t" \
  "buffer_load_dwordx4 v[198:201], %[t16], %[rs], s81 offen offset:1024 sc1\n\ts_waitcnt vmcnt(0)\n\tv_max3_u32 v247, v194, v195, v196\n\t" \
  "v_max3_u32 v247, v247, v197, v198\n\tv_max3_u32 v247, v247, v199, v200\n\tv_max_u32 v247, v247, v201\n\tv_cmp_eq_u32 vcc, -1, v247\n\t" \
  "s_cbranch_vccz .LSL5b%=\n\ts_sleep 1\n\ts_sub_u32 s87, s87, 1\n\ts_cbranch_scc0 .LSL5a%=\n\t.LSL5b%=:\ns_branch .LRED5%=\n\t.LSL1%=:\n" \
  "s_add_u32 s88, s80, 10\n\ts_cmp_eq_u32 s84, s88\n\ts_cbranch_scc1 .LH1B%=\n\ts_mov_b32 s84, s88\n\ts_mov_b32 s87, %[spin]\n\t.LSL1a%=:\n" \
  "buffer_load_dwordx2 v[202:203], %[ss], %[rs], s82 offen sc1\n\tbuffer_load_dwordx4 v[194:197], v243, %[rs], s81 offen sc1\n\t" \
  "buffer_load_dwordx4 v[198:201], v243, %[rs], s81 offen offset:128 sc1\n\ts_waitcnt vmcnt(0)\n\tv_max3_u32 v247, v194, v195, v196\n\t" \
  "v_max3_u32 v247, v247, v197, v198\n\tv_max3_u32 v247, v247, v199, v200\n\tv_max3_u32 v247, v247, v201, v202\n\tv_max_u32 v247, v247, v203\n\t" \
  "v_cmp_eq_u32 vcc, -1, v247\n\ts_cbranch_vccz .LSL1b%=\n\ts_sleep 1\n\ts_sub_u32 s87, s87, 1\n\ts_cbranch_scc0 .LSL1a%=\n\t.LSL1b%=:\n" \
  "s_branch .LRED1%=\n\t.LSLS%=:\ns_add_u32 s88, s80, 10\n\ts_cmp_eq_u32 s84, s88\n\ts_cbranch_scc1 .LH1B%=\n\ts_mov_b32 s84, s88\n\t" \
  "s_mov_b32 s87, %[spin]\n\t.LSLSa%=:\nbuffer_load_dwordx2 v[202:203], %[ss], %[rs], s82 offen sc1\n\ts_waitcnt vmcnt(0)\n\t" \
  "v_max3_u32 v247, v202, v203, v203\n\tv_cmp_eq_u32 vcc, -1, v247\n\ts_cbranch_vccz .LSLSb%=\n\ts_sleep 1\n\ts_sub_u32 s87, s87, 1\n\t" \
  "s_cbranch_scc0 .LSLSa%=\n\t.LSLSb%=:\ns_branch .LREDS%=\n\t.LSLM%=:\nv_max_f32_dpp v247, v246, v246 row_ror:1 row_mask:0xf bank_mask:0xf\n\t" \
  "s_nop 1\n\tv_max_f32_dpp v247, v247, v247 row_ror:2 row_mask:0xf bank_mask:0xf\n\ts_nop 1\n\t" \
  "v_max_f32_dpp v247, v247, v247 row_ror:4 row_mask:0xf bank_mask:0xf\n\ts_nop 1\n\t" \
  "v_max_f32_dpp v247, v247, v247 row_ror:8 row_mask:0xf bank_mask:0xf\n\ts_nop 1\n\t" \
  "v_max_f32_dpp v247, v247, v247 row_bcast:15 row_mask:0xa bank_mask:0xf\n\ts_nop 1\n\t" \
  "v_max_f32_dpp v247, v247, v247 row_bcast:31 row_mask:0xc bank_mask:0xf\n\ts_nop 1\n\tv_readlane_b32 s88, v247, 63\n\ts_nop 3\n\t" \
  "v_mov_b32 v239, s88\n\tv_sub_f32 v246, v246, v239\n\tv_exp_f32 v203, v246\n\ts_nop 0\n\tv_cvt_pk_bf16_f32 v244, v203, 0\n\t" \
  "v_add_f32_dpp v203, v203, v203 row_ror:1 row_mask:0xf bank_mask:0xf\n\ts_nop 0\n\tv_mov_b32_dpp v245, v244 row_shl:1 row_mask:0xf bank_mask:0xf\n\t" \
  "s_branch .LPV%=\n\t.LSLC%=:\nv_max_f32_dpp v247, v204, v204 row_ror:8 row_mask:0xf bank_mask:0xf\n\ts_nop 1\n\t" \
  "v_max_f32_dpp v247, v247, v247 row_ror:4 row_mask:0xf bank_mask:0xf\n\ts_nop 1\n\t" \
  "v_max_f32_dpp v247, v247, v247 quad_perm:[1,0,3,2] row_mask:0xf bank_mask:0xf\n\tv_sub_f32 v204, v204, v247\n\tv_exp_f32 v204, v204\n\ts_nop 0\n\t" \
  "v_mul_f32 v194, v204, v194\n\tv_mul_f32 v195, v204, v195\n\tv_mul_f32 v196, v204, v196\n\tv_mul_f32 v197, v204, v197\n\ts_nop 0\n\t" \
  "v_add_f32_dpp v205, v194, v194 row_ror:8 row_mask:0xf bank_mask:0x3\n\tv_add_f32_dpp v206, v195, v195 row_ror:8 row_mask:0xf bank_mask:0x3\n\t" \
  "v_add_f32_dpp v205, v196, v196 row_ror:8 row_mask:0xf bank_mask:0xc\n\tv_add_f32_dpp v206, v197, v197 row_ror:8 row_mask:0xf bank_mask:0xc\n\t" \
  "s_nop 0\n\tv_add_f32_dpp v246, v205, v205 row_ror:12 row_mask:0xf bank_mask:0x5\n\t" \
  "v_add_f32_dpp v246, v206, v206 row_ror:4 row_mask:0xf bank_mask:0xa\n\ts_nop 1\n\t" \
  "v_add_f32_dpp v246, v246, v246 quad_perm:[1,0,3,2] row_mask:0xf bank_mask:0xf\n\ts_cmp_lg_u32 %[w], 6\n\ts_cbranch_scc1 .LCMS%=\n\t" \
  "v_cmp_eq_u32 vcc, 0, v242\n\tv_cndmask_b32 v246, v246, v247, vcc\n\ts_branch .LCMS%=\n\t.LFE%=:\n.LEND%=:\n"
static_assert(BLK == 16384 && NT == 512 && QS == 400 && L_SM == 0 && TW == 7 && SW == 6, "constants of the hand-written part");

__global__ void __launch_bounds__(NT, 2)
mega_kernel(const uint16_t* __restrict__ x_in, const uint16_t* __restrict__ kc, const uint16_t* __restrict__ vc,
            const uint16_t* __restrict__ wq, const uint16_t* __restrict__ wo, const uint16_t* __restrict__ w1,
            const uint16_t* __restrict__ b1, const uint16_t* __restrict__ w2, const uint16_t* __restrict__ b2,
            uint16_t* __restrict__ out, unsigned* epoch) {
  extern __shared__ __attribute__((aligned(16))) char lds[];
  uint32_t cp;
  const uint64_t code0 = __builtin_amdgcn_s_getpc() & ~(uint64_t)127;
  asm volatile("global_load_dword %0, %1, %2" : "=v"(cp) : "v"(min((int)((blockIdx.x >> 3) * 1024 + (threadIdx.x >> 6) * 128), CODEPF - 128)),
               "s"(code0) : "memory");
  const int tid0 = threadIdx.x, w0 = __builtin_amdgcn_readfirstlane(tid0 >> 6);
  const int b0 = blockIdx.x;
  const int g0 = (b0 & 7) * 4 + (b0 >> 6);
  uint32_t rowb = (g0 * SK + (SK - WIN) + (b0 & 56) * 8 + w0 * 8) * (2 * DH), rot0 = (g0 & 3) * (512 * 2 * DH);
  int vo = (((tid0 >> 5) ^ (g0 >> 2)) & 1) * (2048 * 384) + ((tid0 >> 4) & 1) * 384 + (tid0 & 15) * 16, vo2 = vo + 256 - (tid0 & 15) * 8;
  rot0 += rowb;
  PINS2(rowb, rot0); PIN1(vo); PIN1(vo2);
  const char* vc0 = (const char*)vc;
  asm volatile("" : "+s"(vc0) : "s"(kc), "s"(wq), "s"(x_in), "s"(w1), "s"(b1), "s"(b2), "s"(epoch), "v"(vo), "v"(vo2), "s"(rot0));
  __builtin_amdgcn_sched_barrier(0);
  unsigned long long* gran_base = (unsigned long long*)epoch + 512;
  char* tail = (char*)(((uintptr_t)epoch + (4u << 20)) & ~(uintptr_t)((2u << 20) - 1));
  const auto rs = __builtin_amdgcn_make_buffer_rsrc(tail, 0, 128 * BLK, 0x00020000);
  auto st4 = [&](int voff, int so, float a) { __builtin_amdgcn_raw_buffer_store_b32(__float_as_uint(a), rs, voff, so, 16); };
  auto trd = [&](int off) -> u32x2 {
    return __builtin_bit_cast(u32x2, __builtin_amdgcn_ds_read_tr16_b64_v4i16((LAS s16x4*)(uintptr_t)(uint32_t)off));
  };
  auto lrd16 = [&](int off) -> u32x4 { return *(const LAS u32x4*)(uintptr_t)(uint32_t)off; };
  auto lrd4 = [&](int off) -> uint32_t { return *(const LAS uint32_t*)(uintptr_t)(uint32_t)off; };
  auto lwr2 = [&](int off, uint32_t v) { *(LAS uint16_t*)(uintptr_t)(uint32_t)off = (uint16_t)v; };
  auto lwr4 = [&](int off, uint32_t v) { *(LAS uint32_t*)(uintptr_t)(uint32_t)off = v; };
  constexpr int sm = L_SM;

  uint32_t tag;
  uint32_t tE = 0;
  uint32_t tcur = 0;
  u32x8 kt[12], vt[12];
  uint32_t ix;
  int t16, bo, sS, dT;

  auto qk_softmax = [&](float& mx, float& ev, f32x4 (&dv)[3], int qb, float qs) -> uint32_t {
    u32x4 qa[3];
    f32x4 d[4];
#pragma unroll
    for (int u = 0; u < 3; ++u) qa[u] = lrd16(qb + 64 * u);
#pragma unroll
    for (int a = 0; a < 1; ++a) { ZERO4(d[0], 204, 205, 206, 207); ZERO4(d[1], 208, 209, 210, 211); ZERO4(d[2], 212, 213, 214, 215); ZERO4(d[3], 216, 217, 218, 219); }
    SB();
#pragma unroll
    for (int u = 0; u < 3; ++u)
#pragma unroll
      for (int a = 0; a < 4; ++a) {
        d[a] = smf<0>(qa[u], kt[3 * a + u], d[a], ix);
        SB();
        if (u == 1 && a == 0) ZERO4(dv[0], 220, 221, 222, 223);
        if (u == 1 && a == 2) ZERO4(dv[1], 224, 225, 226, 227);
        if (u == 2 && a == 0) ZERO4(dv[2], 228, 229, 230, 231);
        SB();
      }
    __builtin_amdgcn_s_setprio(1);
    const bool same = __builtin_amdgcn_inverse_ballot_w64(0xff00ff0000ff00ffull);
    const float f0 = same ? d[0][0] + d[0][1] : d[1][0] + d[1][1], f1 = same ? d[2][0] + d[2][1] : d[3][0] + d[3][1];
    auto r = __builtin_amdgcn_permlane32_swap(__float_as_uint(f0), __float_as_uint(f1), false, false);
    const float sraw = uf(r[0]) + dppv<0x128>(uf(r[1]));
    const float sv = rbf(rbf(sraw) * 0.07216878364870322f) * LOG2E;
    uint64_t big = __builtin_amdgcn_ballot_w64(!(fabsf(sv) <= SMALL));
    PINS(big);
    SB();
    mx = 0.f;
    ev = __builtin_amdgcn_exp2f(sv);
    uint32_t pb = pk2(ev, 0.f);
    PIN1(pb);
    SB();
    if (__builtin_expect(big != 0, 0)) {
      float mxv;
      asm volatile("s_nop 1\n\t"
                   "v_max_f32_dpp %0, %1, %1 row_ror:1 row_mask:0xf bank_mask:0xf\n\ts_nop 1\n\t"
                   "v_max_f32_dpp %0, %0, %0 row_ror:2 row_mask:0xf bank_mask:0xf\n\ts_nop 1\n\t"
                   "v_max_f32_dpp %0, %0, %0 row_ror:4 row_mask:0xf bank_mask:0xf\n\ts_nop 1\n\t"
                   "v_max_f32_dpp %0, %0, %0 row_ror:8 row_mask:0xf bank_mask:0xf\n\ts_nop 1\n\t"
                   "v_max_f32_dpp %0, %0, %0 row_bcast:15 row_mask:0xa bank_mask:0xf\n\ts_nop 1\n\t"
                   "v_max_f32_dpp %0, %0, %0 row_bcast:31 row_mask:0xc bank_mask:0xf\n\ts_nop 0"
                   : "=&v"(mxv) : "v"(sv));
      mx = __int_as_float(__builtin_amdgcn_readlane(__float_as_int(mxv), 63));
      ev = __builtin_amdgcn_exp2f(sv - mx);
      pb = pk2(ev, 0.f);
    }
    __builtin_amdgcn_s_setprio(0);
    return pb;
  };
  auto pv_combine = [&](const u32x4& cT, int od, bool sw) {
    const uint32_t m = lrd4(cT.x);
    u32x4 rv = lrd16(cT.y);
    SB();
    uint64_t shifted = __builtin_amdgcn_ballot_w64(m != 0);
    PINS(shifted);
    SB();
    float o = sum_b320(rv);
    PIN1(o);
    SB();
    if (__builtin_expect(shifted != 0, 0)) {
      float M = fmaxf(uf(m), dppv<0x128>(uf(m)));
      M = fmaxf(M, dppv<0x124>(M));
      M = fmaxf(M, dppv<0xb1>(M));
      const float f = __builtin_amdgcn_exp2f(uf(m) - M);
      rv = __builtin_bit_cast(u32x4, __builtin_bit_cast(f32x4, rv) * f);
      asm volatile("s_nop 1" : "+v"(rv));
      o = sum_b320(rv);
      if (sw && (t16 & 0xc0) == 0) o = M;
    }
    st4(cT.z, od, o);
  };
  int s0;
  int oG, oD, rC, eC, eH, eQ, rK;
  int dG, dH, dR, dE;
  int oD1, cwo, oo, far, scv[9];
  u32x4 rs4, ro4;
  float eps = 1e-5f, small = SMALL;

  {
    const int w = w0, b = b0, lane = tid0 & 63;
    const int g = (b & 7) * 4 + (b >> 6), j = (b >> 3) & 7, jg = b >> 3, jj = b & 7;
    const int ck = lane & 7, jp = lane >> 3, n16 = lane & 15, kq = lane >> 4;
    const int sub = (lane >> 5) + 2 * ((lane >> 4) & 1), trl = 4 * kq + (n16 >> 2), trc = 8 * (lane & 3);
    const int xc = min(16 * w + 4 * (lane & 3) + sub, 79);
    const int gc = 64 * (w < 3 ? w : 0) + 4 * (lane & 8) + 4 * ck + sub, hc = (w & 1) ? 32 + 4 * (lane & 3) + sub : 4 * ck + sub;
    gu64* G = (gu64*)gran_base;
    const uint16_t *kp = (const uint16_t*)((const char*)kc + rowb), *vp = (const uint16_t*)(vc0 + rowb);
    const int rot = g, hx = (g >> 2) & 1;
    const int ko = ((lane & 4) + ((lane & 1) << 1) + ((lane >> 1) & 1)) * 384 + (lane & 8) * 8 + kq * 16, ko2 = ko ^ 64;
#define GRP(lo, hi) "+{v[" #lo ":" #hi "]}"
#define LOADK(t, r0, r1, r2, g0, g1, g2) if constexpr (T == t) \
      asm volatile("global_load_dwordx4 " r0 ", %3, %4 nt\n\tglobal_load_dwordx4 " r1 ", %3, %4 offset:128 nt\n\t" \
                   "global_load_dwordx4 " r2 ", %3, %4 offset:256 nt" \
                   : g0(kt[a]), g1(kt[a + 1]), g2(kt[a + 2]) : "v"(o), "s"(p) : "memory")
    auto load_k = [&](auto Tc) {
      constexpr int T = decltype(Tc)::value, a = 3 * (T >> 1);
      const int o = (T & 2) ? ko2 : ko;
      const uint16_t* p = kp + ((((T >> 1) + rot) & 3) * 512 + ((T & 1) ^ hx) * 2048) * DH;
      LOADK(0, "v[2:5]", "v[10:13]", "v[18:21]", GRP(2, 9), GRP(10, 17), GRP(18, 25));
      LOADK(1, "v[6:9]", "v[14:17]", "v[22:25]", GRP(2, 9), GRP(10, 17), GRP(18, 25));
      LOADK(2, "v[26:29]", "v[34:37]", "v[42:45]", GRP(26, 33), GRP(34, 41), GRP(42, 49));
      LOADK(3, "v[30:33]", "v[38:41]", "v[46:49]", GRP(26, 33), GRP(34, 41), GRP(42, 49));
      LOADK(4, "v[50:53]", "v[58:61]", "v[66:69]", GRP(50, 57), GRP(58, 65), GRP(66, 73));
      LOADK(5, "v[54:57]", "v[62:65]", "v[70:73]", GRP(50, 57), GRP(58, 65), GRP(66, 73));
      LOADK(6, "v[74:77]", "v[82:85]", "v[90:93]", GRP(74, 81), GRP(82, 89), GRP(90, 97));
      LOADK(7, "v[78:81]", "v[86:89]", "v[94:97]", GRP(74, 81), GRP(82, 89), GRP(90, 97));
      SB();
    };
#define LOADV(v, x0, y0, x1, y1, g0, g1, g2) if constexpr (V == v) \
      asm volatile("global_load_dwordx4 " x0 ", %3, %5 nt\n\tglobal_load_dwordx2 " y0 ", %4, %5 nt\n\t" \
                   "global_load_dwordx4 " x1 ", %3, %5 offset:768 nt\n\tglobal_load_dwordx2 " y1 ", %4, %5 offset:768 nt" \
                   : g0(vt[a]), g1(vt[a + 1]), g2(vt[a + 2]) : "v"(vo), "v"(vo2), "s"(p) : "memory")
    auto load_v = [&](auto Vc) {
      constexpr int V = decltype(Vc)::value, a = 3 * (V >> 1);
      const uint16_t* p = V ? vp + (((2 * (V & 1) + (V >> 2) + rot) & 3) * 512 + ((V >> 1) & 1) * 4) * DH : (const uint16_t*)(vc0 + rot0);
      LOADV(0, "v[98:101]", "v[114:115]", "v[106:109]", "v[116:117]", GRP(98, 105), GRP(106, 113), GRP(114, 121));
      LOADV(1, "v[102:105]", "v[118:119]", "v[110:113]", "v[120:121]", GRP(98, 105), GRP(106, 113), GRP(114, 121));
      LOADV(2, "v[122:125]", "v[138:139]", "v[130:133]", "v[140:141]", GRP(122, 129), GRP(130, 137), GRP(138, 145));
      LOADV(3, "v[126:129]", "v[142:143]", "v[134:137]", "v[144:145]", GRP(122, 129), GRP(130, 137), GRP(138, 145));
      LOADV(4, "v[146:149]", "v[162:163]", "v[154:157]", "v[164:165]", GRP(146, 153), GRP(154, 161), GRP(162, 169));
      LOADV(5, "v[150:153]", "v[166:167]", "v[158:161]", "v[168:169]", GRP(146, 153), GRP(154, 161), GRP(162, 169));
      LOADV(6, "v[170:173]", "v[186:187]", "v[178:181]", "v[188:189]", GRP(170, 177), GRP(178, 185), GRP(186, 193));
      LOADV(7, "v[174:177]", "v[190:191]", "v[182:185]", "v[192:193]", GRP(170, 177), GRP(178, 185), GRP(186, 193));
      SB();
    };
    int go[5];
    auto wo_prep = [&]() {
      int l = lane;
      PIN1(l);
#pragma unroll
      for (int i = 0; i < 5; ++i) { const int c = min(64 * i + l, w < 6 ? 319 : 239), row = (c * 205) >> 11; go[i] = row * (2 * DM) + (c - 10 * row) * 16; PIN1(go[i]); }
    };
    auto load_wo = [&]() {
      const uint16_t* src = (w < 6 ? wo + (size_t)(192 * g + 32 * w) * DM : w2 + (size_t)(48 * g + 24 * (w - 6)) * DM) + 80 * j;
      unsigned keep;
      asm volatile("s_mov_b32 %0, m0\n\ts_mov_b32 m0, %7\n\ts_nop 0\n\tglobal_load_lds_dwordx4 %1, %6 nt\n\t"
                   "s_add_u32 m0, m0, 0x400\n\ts_nop 0\n\tglobal_load_lds_dwordx4 %2, %6 nt\n\t"
                   "s_add_u32 m0, m0, 0x400\n\ts_nop 0\n\tglobal_load_lds_dwordx4 %3, %6 nt\n\t"
                   "s_add_u32 m0, m0, 0x400\n\ts_mov_b64 exec, %8\n\tglobal_load_lds_dwordx4 %4, %6 nt\n\t"
                   "s_add_u32 m0, m0, %10\n\ts_mov_b64 exec, %9\n\tglobal_load_lds_dwordx4 %5, %6 nt\n\t"
                   "s_mov_b64 exec, -1\n\ts_mov_b32 m0, %0"
                   : "=&s"(keep) : "v"(go[0]), "v"(go[1]), "v"(go[2]), "v"(go[3]), "v"(go[4]), "s"(src), "s"(w < 7 ? L_WO + w * 5120 : L_W2 + 3840),
                     "s"(w < 6 ? ~0ull : (1ull << 48) - 1), "s"(w < 6 ? ~0ull : 1ull), "s"(w < 6 ? 1024 : 752) : "memory", "scc");
      SB();
    };
    asm volatile("" : "={v[98:105]}"(vt[0]), "={v[106:113]}"(vt[1]), "={v[114:121]}"(vt[2]));
    load_v(IC<0>{});
    uint32_t tp = 0;
    if (w == TW) tp = rt32() + SC0(PO);
    int pstep = SC0(PT);
    auto pace = [&]() {
      int rem;
      if (w == TW) asm volatile("1:\n\ts_memrealtime vcc\n\ts_waitcnt lgkmcnt(0)\n\ts_sub_i32 %0, %1, vcc_lo\n\ts_cmp_lt_u32 %0, 0x800\n\ts_cbranch_scc1 1b\n\ts_add_u32 %1, %1, %2"
                   : "=&s"(rem), "+s"(tp) : "s"(pstep) : "memory", "scc", "vcc");
    };
    const bool early = w < 4;
#define DEFG(n, a0, a1, b0, b1, c0, c1) if constexpr (N == n) asm volatile("" : "={v[" #a0 ":" #a1 "]}"(p[0]), "={v[" #b0 ":" #b1 "]}"(p[1]), "={v[" #c0 ":" #c1 "]}"(p[2]))
    auto first = [&](auto Ic) {
      constexpr int i = decltype(Ic)::value, x = i > 0 && i <= 8 ? kord(i - 1) : i ? vord(i - 8) : 0, N = (x & 1) ? -1 : (i > 0 && i <= 8 ? 0 : 4) + (x >> 1);
      u32x8* p = (i > 0 && i <= 8 ? kt : vt) + 3 * (x >> 1);
      DEFG(0, 2, 9, 10, 17, 18, 25);
      DEFG(1, 26, 33, 34, 41, 42, 49);
      DEFG(2, 50, 57, 58, 65, 66, 73);
      DEFG(3, 74, 81, 82, 89, 90, 97);
      DEFG(4, 98, 105, 106, 113, 114, 121);
      DEFG(5, 122, 129, 130, 137, 138, 145);
      DEFG(6, 146, 153, 154, 161, 162, 169);
      DEFG(7, 170, 177, 178, 185, 186, 193);
    };
    auto tile0 = [&](auto Ic) {
      constexpr int i = decltype(Ic)::value;
      if constexpr (i <= 8) load_k(IC<kord(i - 1)>{}); else load_v(IC<vord(i - 8)>{});
    };
    auto tile = [&](auto Ic) { first(Ic); tile0(Ic); };
    auto tile_e = [&](auto Ic) { first(Ic); if (early) tile0(Ic); SB(); };
    auto tile_l = [&](auto Ic) { SB(); if (!early) tile0(Ic); };
    auto piece = [&](int k, int n) { const int o = n * (b >> 2) + lane * 16; return lane * 16 < n ? O_S + ((b & 3) * NB + k + (o >> 13)) * BLK + (o & 8191) : OOR; };
    const int prb = O_PR + (b >> 3) * BLK + (w >> 2) * 8192 + (b & 7) * 1024 + (w & 3) * 256;
    uint32_t tv = 0;
    PINS(tv);
    if (b < NVOTE && w == SW) {
      __builtin_amdgcn_raw_buffer_store_b128((u32x4){0, 0, 0, 0}, rs, (lane & 12) != 12 && lane < 32 ? prb - 8192 - 512 + (lane >> 4) * 8192 + (lane & 12) * 64 + (lane & 3) * 16 : OOR, 0, 16);
      asm volatile("s_load_dword %0, %1, 0x80" : "+s"(tv) : "s"(tail + prb) : "memory");
    }
    SB();
    uint32_t ep = 0, xf[5] = {0, 0, 0, 0, 0}, xs = 0;
    asm volatile("" : "+v"(xf[0]), "+v"(xf[1]), "+v"(xf[2]), "+v"(xf[3]), "+v"(xf[4]), "+v"(xs), "+v"(ep));
    if (w == 1) asm volatile("global_load_dword %0, %1, off" : "+v"(xs) : "v"(lane < 40 ? b2 + 80 * jj + 2 * lane : b1 + 48 * g + 2 * lane - 80) : "memory");
    if (w == 0)
      asm volatile("global_load_dword %0, %7, %8\n\tglobal_load_dword %1, %7, %8 offset:256\n\tglobal_load_dword %2, %7, %8 offset:512\n\t"
                   "global_load_dword %3, %7, %8 offset:768\n\tglobal_load_dword %4, %7, %8 offset:1024\n\tglobal_load_dword %5, %9, %8\n\tglobal_load_dword %6, %10, %11 sc1"
                   : "+v"(xf[0]), "+v"(xf[1]), "+v"(xf[2]), "+v"(xf[3]), "+v"(xf[4]), "+v"(xs), "+v"(ep)
                   : "v"(4 * lane), "s"(x_in), "v"(160 * jj + 4 * min(lane, 39)), "v"(0), "s"(epoch) : "memory");
    SB();
    {
      int go[4];
#pragma unroll
      for (int r = 0; r < 4; ++r) { const int c = min(64 * r + lane, 249), row = (c * 41) >> 10; go[r] = row * (2 * QD) + min(c - 25 * row, 23) * 16; }
      unsigned keep;
      asm volatile("s_mov_b32 %0, m0\n\ts_mov_b32 m0, %6\n\ts_nop 0\n\tglobal_load_lds_dwordx4 %1, %5 nt\n\t"
                   "s_add_u32 m0, m0, 0x400\n\ts_nop 0\n\tglobal_load_lds_dwordx4 %2, %5 nt\n\t"
                   "s_add_u32 m0, m0, 0x400\n\ts_nop 0\n\tglobal_load_lds_dwordx4 %3, %5 nt\n\t"
                   "s_add_u32 m0, m0, 0x400\n\ts_mov_b64 exec, %7\n\tglobal_load_lds_dwordx4 %4, %5 nt\n\t"
                   "s_mov_b64 exec, -1\n\ts_mov_b32 m0, %0"
                   : "=&s"(keep) : "v"(go[0]), "v"(go[1]), "v"(go[2]), "v"(go[3]),
                     "s"(wq + (size_t)(80 * jj + 10 * w) * QD + 192 * jg), "s"(L_WQ + w * 10 * QS), "s"((1ull << 58) - 1) : "memory", "scc");
    }
    SB();
    {
      const int c = min(lane, 59), row = (c * 43) >> 8;
      unsigned keep;
      asm volatile("s_mov_b32 %0, m0\n\ts_mov_b32 m0, %3\n\ts_mov_b64 exec, %4\n\tglobal_load_lds_dwordx4 %1, %2 nt\n\ts_mov_b64 exec, -1\n\ts_mov_b32 m0, %0"
                   : "=&s"(keep) : "v"(row * (2 * FF) + (c - 6 * row) * 16), "s"(w1 + (size_t)(80 * jj + 10 * w) * FF + 48 * jg), "s"(L_W1 + w * 960), "s"((1ull << 60) - 1) : "memory");
    }
    SB();
    BAR0();
    tile(IC<1>{});
    auto probe = [&](auto Oc) -> uint32_t {
      unsigned long long c0, c1; uint32_t v;
      asm volatile("s_memtime %0\n\ts_waitcnt lgkmcnt(0)\n\ts_load_dword %2, %3, %4\n\ts_waitcnt lgkmcnt(0)\n\ts_memtime %1\n\ts_waitcnt lgkmcnt(0)"
                   : "=&s"(c0), "=&s"(c1), "=&s"(v) : "s"(tail + prb), "n"(decltype(Oc)::value) : "memory");
      return (uint32_t)(c1 - c0);
    };
    {
      t16 = w * 1024 + lane * 16; bo = sm + kq * 16;
      sS = w == SW ? (n16 == 0 ? sm + S_XP + kq * 16 : L_ONE) : ck * 32;
      dT = lane < (w == SW ? 2 : 16) ? lane * 4 : OOR;
      LAS u32x4* T = (LAS u32x4*)(uintptr_t)(uint32_t)(L_TAB + t16);
      const int pk = ((lane >> 1) & 6) | (lane & 1), c4 = 8 * w + ((lane >> 3) & 6) + ((lane >> 1) & 1);
      T[0] = (u32x4){(uint32_t)(sm + S_ML + pk * 16), (uint32_t)(w < 6 ? sm + S_RED + pk * RS + c4 * 16 : sm + S_ML + pk * 16),
                     (uint32_t)(w == SW ? ((lane & ~4) == 0 ? lane : OOR) : (!(lane & 1) && w < 6) ? c4 * 16 + (lane & 12) : OOR), (uint32_t)(jp * 768 + w * 256 + ck * 16)};
      T[5 * NT] = (u32x4){(uint32_t)((kq >> 1) == ((n16 >> 2) & 1) ? sm + S_QB + (n16 & 1) * 192 + (n16 & 8) * 4 + (kq & 1) * 16 : sm + S_ZQ),
                          (uint32_t)(sm + S_RED + w * RS + lane * 4), (uint32_t)(sm + S_ML + w * 16), 0u};
      ix = ((n16 & 1) ? 0xeeeeu : 0x4444u) | (n16 >> 2) * 0x55550000u;
      const int gd = (gc >> 4) & 3, dimo = gd + (gc < 128 ? 8 * (gc & 15) + 4 * (gc >> 6) : 128 + 4 * (gc & 15));
      if (w == 1 && lane < 12) *(LAS u32x4*)(uintptr_t)(uint32_t)(sm + S_ZQ + lane * 16) = (u32x4){0, 0, 0, 0};
      T[NT] = (u32x4){(uint32_t)(sm + S_OV + opos(dimo)), (uint32_t)(L_WO + (w < 5 ? w : 0) * 32 + trl * 160 + trc),
                      (uint32_t)(sm + S_XP + 2 * kpos(xc)), (uint32_t)(L_W1 + min(w, 2) * 32 + trl * 96 + trc)};
      T[2 * NT] = (u32x4){(uint32_t)(w < 5 ? sm + S_HV + 2 * kpos(hc) : piece(0, 768) + (w & 1) * 8192), (uint32_t)(w < 5 ? L_W2 + w * 32 + trl * 160 + trc : piece(6, 1024)),
                          (uint32_t)(L_CS + 768 + 4 * hc), (uint32_t)((w & 1) ? 1024 + jp * 64 + (ck & 3) * 16 : lane * 16)};
      T[3 * NT] = (u32x4){(uint32_t)(sm + S_QB + qpos(gc)), (uint32_t)(L_CS + 4 * gc), (uint32_t)(L_WQ + (w < 6 ? w : 0) * 64 + trl * QS + trc), (uint32_t)(2 * xc)};
    }
    SB();
    BAR0();
    tile(IC<2>{});
    asm volatile("s_waitcnt vmcnt(%8)" : "+v"(ep), "+v"(xf[0]), "+v"(xf[1]), "+v"(xf[2]), "+v"(xf[3]), "+v"(xf[4]), "+v"(xs), "+v"(cp) : "n"(3 * UP + 2) : "memory");
    if (w < 2) lwr4(sm + S_B + w * 256 + lane * 4, xs);
    if (w == 0) {
      float s1 = 0.f;
#pragma unroll
      for (int i = 0; i < 5; ++i) s1 += lo2f(xf[i]) + hi2f(xf[i]);
      const float mean = wsum_bcast(s1) * (1.0f / DM);
      float q0 = 0.f;
#pragma unroll
      for (int i = 0; i < 5; ++i) { const float d0 = lo2f(xf[i]) - mean, d1 = hi2f(xf[i]) - mean; q0 = fmaf(d0, d0, q0); q0 = fmaf(d1, d1, q0); }
      const float var = wsum_bcast(q0) * (1.0f / DM);
      const uint32_t mv = pk2(mean, var);
      const float rinv = __builtin_amdgcn_rcpf(rbf(__builtin_amdgcn_sqrtf(rbf(hi2f(mv) + 1e-5f))));
      if (lane < 48) lwr4(sm + S_XP + 2 * kpos(2 * lane), lane < 40 ? pk2(rbf(lo2f(xs) - lo2f(mv)) * rinv, rbf(hi2f(xs) - lo2f(mv)) * rinv) : 0u);
      if (lane < 48) lwr4(L_ONE + lane * 4, 0x3f803f80u);
      if (lane < 16) lwr2(sm + S_HV + 2 * kpos(48 + lane), 0);
      lwr4(L_FLAG, ep * 1664525u + 1013904223u);
    }
    if (b < NVOTE && (w & 3) != 3) lwr4(L_FLAG + 32 + 4 * w, probe(IC<0>{}));
    SB();
    asm volatile("s_waitcnt vmcnt(%0)" :: "n"(6) : "memory");
    lds_barrier();
    PINS(tv);
    tile_e(IC<3>{});
    tag = __builtin_amdgcn_readfirstlane(lrd4(L_FLAG));
    float pv[2];
    {
      const int xb = n16 >= 4 ? L_ONE : sm + S_XP + kq * 16;
      const u32x4 x0 = lrd16(xb), x1 = lrd16(xb + 64), x2 = lrd16(xb + 128);
#pragma unroll
      for (int r = 0; r < 2; ++r) {
        const int tl = min(2 * w + r, 14), rstr = tl < 12 ? QS : 96;
        const int a0 = (tl < 12 ? L_WQ : L_W1 - 12 * 32) + tl * 32 + trl * rstr + trc;
        f32x4 d = {0.f, 0.f, 0.f, 0.f};
        d = mfma32(x0, cat(trd(a0), trd(a0 + 16 * rstr)), d);
        d = mfma32(x1, cat(trd(a0 + 32 * rstr), trd(a0 + 48 * rstr)), d);
        d = mfma32(x2, cat(trd(a0 + 64 * rstr), (u32x2){0, 0}), d);
        pv[r] = d[0];
      }
    }
    gu64* dst = G + G_P + b * RP + (kq == 0 ? 0 : 192) + n16 + 32 * w;
    const int cu = (lane >> 2) & 7;
    int so = (w & 3) == 0 ? g * BLK + (w >> 2) * 8192 + j * 1024 + lane * 16
             : w == 1 && cu < 5 ? O_C + (2 * j + (cu >> 2)) * BLK + (lane >> 5) * 8192 + (cu & 3) * 2048 + ((g + 7 * cu) & 31) * 64 + (lane & 3) * 16 : OOR;
    if (w == TW) {
      unsigned xcc;
      asm volatile("s_getreg_b32 %0, hwreg(HW_REG_XCC_ID)" : "=s"(xcc));
      const u32x4 l0 = lrd16(L_FLAG + 32), l1 = lrd16(L_FLAG + 48);
      const uint32_t vote = ((min(min(l1.x, l1.y), l1.z) < min(min(l0.x, l0.y), l0.z) ? 1u : 0u) ^ (xcc >> 2)) & 1u;
      pv[1] = uf(vote | ((xcc >> 1) & 2) | (rt32() << 2));
    }
    PIN1(pv[0]); PIN1(pv[1]); PIN1(dst); PIN1(so);
    tile_l(IC<3>{});
    pace();
    BAR0();
    if (kq == 1 || (kq == 0 && w < 6)) {
      __hip_atomic_store(dst, ((unsigned long long)tag << 32) | __float_as_uint(pv[0]), RLX_AGENT);
      if (w < 7) __hip_atomic_store(dst + 16, ((unsigned long long)tag << 32) | __float_as_uint(pv[1]), RLX_AGENT);
    }
    if (w == TW && b < NVOTE && lane == 0) __hip_atomic_store(G + G_F + b, ((unsigned long long)tag << 32) | __float_as_uint(pv[1]), RLX_AGENT);
    if ((w & 2) == 0 && w != 5) __builtin_amdgcn_raw_buffer_store_b128((u32x4){SENT, SENT, SENT, SENT}, rs, so, 0, 16);
    SB();
    tile(IC<RT>{});
    pace();
    BAR0();
    tile_e(IC<RT + 1>{});
    {
      auto rd = [&](int off) { return lo2f(*(const LAS uint16_t*)(uintptr_t)(uint32_t)(sm + S_B + off)); };
      *(LAS f32x4*)(uintptr_t)(uint32_t)(L_TAB + 4 * NT * 16 + t16) = (f32x4){rd(2 * xc), rd(256 + 2 * xc), rd(416 + 2 * hc), 0.f};
    }
    tile_l(IC<RT + 1>{});
    pace();
    BAR0();
    tile(IC<RT + 2>{});
    pace();
    BAR0();
    u32x4 er = {0, 0, 0, 0};
    asm volatile("" : "+v"(er));
    auto ereq = [&](const char* src) { asm volatile("global_load_dwordx4 %0, %1, off sc1" : "+v"(er) : "v"(src) : "memory"); };
    auto efin = [&](auto Nc, const char* src) {
      asm volatile("s_waitcnt vmcnt(%1)" : "+v"(er) : "n"(decltype(Nc)::value) : "memory");
#pragma unroll 1
      for (unsigned sp = 0; sp < SPIN_LIMIT; ++sp) {
        if (__all(tagbad(er, tag) == 0)) break;
        __builtin_amdgcn_s_sleep(4);
        asm volatile("global_load_dwordx4 %0, %1, off sc1\n\ts_waitcnt vmcnt(0)" : "+v"(er) : "v"(src) : "memory");
      }
    };
    {
      const int un = j + 8 * w;
      const bool mine = un < 27;
      const char* src = mine ? (const char*)(G + G_P + (8 * g + jp) * RP) + un * 128 + ck * 16 : (const char*)(G + G_F) + n16 * 16;
      if (mine || w == TW) ereq(src);
      SB();
      tile(IC<RT + 3>{});
      pace();
      BAR0();
      tile_e(IC<RT + 4>{});
      asm volatile("s_waitcnt vmcnt(%0)" :: "n"(nreq(RT, RT + 3)) : "memory");
      if (mine) efin(IC<nreq(RT + 3, RT + 4)>{}, src);
      const float t = swapadd32(uf(er.x), uf(er.z));
      uint32_t sv = __float_as_uint(ror8add(swapadd16(t, t)));
      gu64* sd = G + G_R + g * RP + un * 16 + 2 * ck + (lane >> 5);
      PIN1(sv); PIN1(sd);
      tile_l(IC<RT + 4>{});
      pace();
      BAR0();
      if (mine && !(lane & 24)) __hip_atomic_store(sd, ((unsigned long long)tag << 32) | sv, RLX_AGENT);
      if (w == TW) {
        if (b < NVOTE && lane == 0) __hip_atomic_store(G + G_F + NVOTE + b, ((unsigned long long)tag << 32) | rt32(), RLX_AGENT);
        efin(IC<nreq(RT + 3, RT + 4)>{}, src);
        const uint32_t hx = (uint32_t)__ballot(er.x & 2), hz = (uint32_t)__ballot(er.z & 2);
        lwr4(L_FLAG + 16, (__popcll(__ballot(er.x & 1)) + __popcll(__ballot(er.z & 1))) << 8 | (hx & 1) | ((hz & 1) << 1) | ((hx & 2) << 1) | ((hz & 2) << 2));
        if (SC(PB)) {
          const float d = (float)((int)(er.x - (tp << 2)) >> 2) + (float)((int)(er.z - (tp << 2)) >> 2);
          tp += __builtin_amdgcn_readfirstlane((int)(wsum_bcast(d) * (1.f / 128.f))) + SC(PA);
          pstep = SC(PB);
        }
      }
      tp = __builtin_amdgcn_readfirstlane(tp); pstep = __builtin_amdgcn_readfirstlane(pstep);
      SB();
    }
    tile(IC<RT + 5>{});
    pace();
    lds_barrier();
    tile_e(IC<RT + 6>{});
    {
      const int f0 = __builtin_amdgcn_readfirstlane(lrd4(L_FLAG + 16)), votes = f0 >> 8;
      const int m4 = (f0 & 15) ^ ((SC(CPAR) < 2 ? SC(CPAR) : votes > 2 * NVOTE) ? 15 : 0);
      const int pm = m4 | ((m4 ^ 15) << 4);
      auto par = [&](int x) { return ((pm >> x) & 1) * 8192; };
      const int cw = O_C + (w >> 2) * BLK + (w & 3) * 2048;
      oG = g * BLK + par(jj); oD = oG + (w == SW ? O_SG + j * 32 : j * 768);
      rC = 2 * jj * BLK + par(jj) + cw - w * 1024; eC = 2 * j * BLK + par(j) + cw + ((g + 7 * w) & 31) * 64;
      eH = jg * BLK + par(jg >> 2) + (w == SW ? O_SH + jj * 32 : O_H + (w < 2 ? jj * 128 + w * 64 : 1024 + jj * 64));
      rK = par(jj) + O_DB - TW * 1024;
      dG = O_S + ((jj & 3) * NB + 6 + (g & 3) - g) * BLK; dH = O_S + (((jg >> 2) & 3) * NB + 6 + (jg & 3) - jg) * BLK;
      dR = O_S - O_C + ((jj & 3) * NB + 10 - 2 * jj) * BLK; dE = O_S - O_C + ((j & 3) * NB + 10 - 2 * j) * BLK;
      eQ = jg * BLK + dH - 4 * BLK + par(jg >> 2) + (w == SW ? O_SG + jj * 32 : jj * 768 + w * 128);
#define UNI(a) a = __builtin_amdgcn_readfirstlane(a)
      UNI(oG); UNI(oD); UNI(rC); UNI(eC); UNI(eH); UNI(eQ); UNI(rK);
      oD1 = oD + dG; cwo = O_DB + b * 8; oo = 160 * b; far = min(b >> 3, 1);
      rs4 = (u32x4){(uint32_t)(uintptr_t)tail, (uint32_t)((uintptr_t)tail >> 32) & 0xffffu, 128u * BLK, 0x00020000u};
      ro4 = (u32x4){(uint32_t)(uintptr_t)out, (uint32_t)((uintptr_t)out >> 32) & 0xffffu, 2u * DM, 0x00020000u};
#pragma unroll
      for (int i = 0; i < 9; ++i) { scv[i] = i < 8 ? SC(i) : SC(PW); PINS(scv[i]); }
      PINS2(oG, oD); PINS2(rC, eC); PINS2(eH, eQ); PINS2(rK, oD1); PINS2(dG, dH); PINS2(dR, dE); PINS2(cwo, oo); PINS(far); PINS2(rs4, ro4); PINS2(eps, small);
      PIN1(sS); PIN1(dT);
    }
    tile_l(IC<RT + 6>{});
    pace();
    BAR0();
    {
      const int v0 = min(128 * w + 2 * lane, RP - 2);
      const char* src = w < 4 ? (const char*)(G + G_R + g * RP + v0) : (const char*)(G + G_F + NVOTE) + n16 * 16;
      if (w < 4 || w == TW) ereq(src);
      SB();
      tile(IC<RT + 7>{});
      pace();
      BAR0();
      tile(IC<RT + 8>{});
      pace();
      BAR0();
      if (w == TW && SC(PD)) {
        asm volatile("s_waitcnt vmcnt(%1)" : "+v"(er) : "n"(nreq(RT + 7, RT + 8)) : "memory");
        const bool v0 = er.y == tag, v1 = er.w == tag;
        const float d = (v0 ? (float)(int)(er.x - tp) : 0.f) + (v1 ? (float)(int)(er.z - tp) : 0.f);
        const int n = __popcll(__ballot(v0)) + __popcll(__ballot(v1));
        if (n > 32) {
          tp += __builtin_amdgcn_readfirstlane((int)(wsum_bcast(d) * __builtin_amdgcn_rcpf((float)n))) + SC(PC);
          pstep = SC(PD);
        }
      }
      tp = __builtin_amdgcn_readfirstlane(tp); pstep = __builtin_amdgcn_readfirstlane(pstep);
      SB();
      tile(IC<RT + 9>{});
      if (w < 4) {
        efin(IC<nreq(RT + 7, RT + 9)>{}, src);
        if (v0 < 192) lwr4(sm + S_QB + qpos(v0), pk2(uf(er.x), uf(er.z)));
        else *(LAS u32x2*)(uintptr_t)(uint32_t)(L_CS + 4 * (v0 - 192)) = (u32x2){er.x, er.z};
      }
    }
    SB();
    pace();
    lds_barrier();
    tile_e(IC<RT + 10>{});
    asm volatile("s_waitcnt vmcnt(%12)"
                 : GRP(2, 9)(kt[0]), GRP(10, 17)(kt[1]), GRP(18, 25)(kt[2]), GRP(26, 33)(kt[3]), GRP(34, 41)(kt[4]), GRP(42, 49)(kt[5]),
                   GRP(50, 57)(kt[6]), GRP(58, 65)(kt[7]), GRP(66, 73)(kt[8]), GRP(74, 81)(kt[9]), GRP(82, 89)(kt[10]), GRP(90, 97)(kt[11])
                 : "n"(nreq(9, RT + 9)) : "memory");
    const u32x4 aT = lrd16(L_TAB + 5 * NT * 16 + t16);
    uint32_t pb;
    f32x4 dv[3];
    {
      float mx, ev;
      pb = qk_softmax(mx, ev, dv, aT.x, 1.f);
      const float ls = wsum63(ev);
      if (lane == 63) *(LAS f32x2*)(uintptr_t)(uint32_t)aT.z = (f32x2){mx, ls};
    }
#define VFIX(v, e0, e1, e2, e3, g0, g1, g2) if constexpr (V == v) \
      asm volatile("s_waitcnt vmcnt(%3)\n\tv_swap_b32 v" #e0 ", v" #e2 "\n\tv_swap_b32 v" #e1 ", v" #e3 "\n\ts_nop 0" \
                   : g0(vt[a]), g1(vt[a + 1]), g2(vt[a + 2]) : "n"(decltype(YN)::value) : "memory")
    auto v_fix = [&](auto Vc, auto YN) {
      constexpr int V = decltype(Vc)::value, a = 3 * (V >> 1);
      SB();
      VFIX(0, 100, 101, 106, 107, GRP(98, 105), GRP(106, 113), GRP(114, 121));
      VFIX(1, 104, 105, 110, 111, GRP(98, 105), GRP(106, 113), GRP(114, 121));
      VFIX(2, 124, 125, 130, 131, GRP(122, 129), GRP(130, 137), GRP(138, 145));
      VFIX(3, 128, 129, 134, 135, GRP(122, 129), GRP(130, 137), GRP(138, 145));
      VFIX(4, 148, 149, 154, 155, GRP(146, 153), GRP(154, 161), GRP(162, 169));
      VFIX(5, 152, 153, 158, 159, GRP(146, 153), GRP(154, 161), GRP(162, 169));
      VFIX(6, 172, 173, 178, 179, GRP(170, 177), GRP(178, 185), GRP(186, 193));
      VFIX(7, 176, 177, 182, 183, GRP(170, 177), GRP(178, 185), GRP(186, 193));
      SB();
    };
    auto mm_step = [&](auto Ic, auto YN) {
      constexpr int i = decltype(Ic)::value;
      u32x4 pa = pset<i>(pb);
      PIN1(pa);
      v_fix(IC<2 * i + 1>{}, YN);
      dv[2] = smf<1>(pa, vt[3 * i + 2], dv[2], ix); SB();
      dv[0] = smf<1>(pa, vt[3 * i], dv[0], ix); dv[1] = smf<1>(pa, vt[3 * i + 1], dv[1], ix);
    };
    SB();
    wo_prep();
    tile_l(IC<RT + 10>{});
    pace();
    lds_barrier();
    tile_e(IC<15>{});
    v_fix(IC<0>{}, IC<16>{}); v_fix(IC<2>{}, IC<16>{});
    mm_step(IC<0>{}, IC<16>{}); mm_step(IC<1>{}, IC<12>{}); v_fix(IC<4>{}, IC<8>{});
    tile_l(IC<15>{});
    BAR0();
    load_wo();
    v_fix(IC<6>{}, IC<nreq(14, 15)>{});
    mm_step(IC<2>{}, IC<nreq(15, 15)>{});
    SB();
    u32x4 cT = lrd16(L_TAB + t16);
    PIN1(cT);
    mm_step(IC<3>{}, IC<5>{});
    lwr4(aT.y + 512, __float_as_uint(dv[2][0])); lwr4(aT.y, __float_as_uint(dv[0][0])); lwr4(aT.y + 256, __float_as_uint(dv[1][0]));
    lds_barrier();
    pv_combine(cT, oD, w == SW);
    s0 = cT.w;
  }
  {
    asm volatile(TAIL_ASM
                 : [s0] "+v"(s0), [tc] "+s"(tcur), [te] "+s"(tE), [oG] "+s"(oG), [rC] "+s"(rC), [eC] "+s"(eC), [eH] "+s"(eH)
                 : [t16] "v"(t16), [bo] "v"(bo), [ss] "v"(sS), [dt] "v"(dT), [ix] "v"(ix), [rs] "s"(rs4), [ro] "s"(ro4), [w] "s"(w0), [far] "s"(far),
                   [oD] "s"(oD1), [dG] "s"(dG), [dH] "s"(dH), [dR] "s"(dR), [dE] "s"(dE), [eQ] "s"(eQ), [rK] "s"(rK), [tag] "s"(tag), [cw] "s"(cwo), [oo] "s"(oo),
                   [p3e] "s"(scv[0]), [p2] "s"(scv[1]), [p3] "s"(scv[2]), [p4] "s"(scv[3]), [p5] "s"(scv[4]), [p1] "s"(scv[5]), [rmin] "s"(scv[6]), [rcons] "s"(scv[7]), [pw] "s"(scv[8]),
                   [eps] "s"(eps), [small] "s"(small), [spin] "n"(SPIN_LIMIT), [tab] "n"(L_TAB), [xp] "n"(S_XP), [ov] "n"(S_OV), [hv] "n"(S_HV),
                   [osg] "n"(O_SG), [osh] "n"(O_SH), [oh] "n"(O_H), [oor] "n"(OOR), [qsw] "n"(L_FLAG + 4),
                   "{v[2:9]}"(kt[0]), "{v[10:17]}"(kt[1]), "{v[18:25]}"(kt[2]), "{v[26:33]}"(kt[3]), "{v[34:41]}"(kt[4]), "{v[42:49]}"(kt[5]),
                   "{v[50:57]}"(kt[6]), "{v[58:65]}"(kt[7]), "{v[66:73]}"(kt[8]), "{v[74:81]}"(kt[9]), "{v[82:89]}"(kt[10]), "{v[90:97]}"(kt[11]),
                   "{v[98:105]}"(vt[0]), "{v[106:113]}"(vt[1]), "{v[114:121]}"(vt[2]), "{v[122:129]}"(vt[3]), "{v[130:137]}"(vt[4]),
                   "{v[138:145]}"(vt[5]), "{v[146:153]}"(vt[6]), "{v[154:161]}"(vt[7]), "{v[162:169]}"(vt[8]), "{v[170:177]}"(vt[9]),
                   "{v[178:185]}"(vt[10]), "{v[186:193]}"(vt[11])
                 : "memory", "scc", "vcc",
                   "v194", "v195", "v196", "v197", "v198", "v199", "v200", "v201", "v202", "v203", "v204", "v205", "v206", "v207", "v208", "v209",
                   "v210", "v211", "v212", "v213", "v214", "v215", "v216", "v217", "v218", "v219", "v220", "v221", "v222", "v223", "v224", "v225",
                   "v226", "v227", "v228", "v229", "v230", "v231", "v232", "v233", "v234", "v235", "v236", "v237", "v238", "v239", "v240", "v241",
                   "v242", "v243", "v244", "v245", "v246", "v247", "s80", "s81", "s82", "s83", "s84", "s85", "s86", "s87", "s88", "s89", "s90", "s91",
                   "s92", "s93", "s94", "s95", "s96", "s97", "s98", "s99"
                 );
  }
  if (b0 == 8 && t16 == SW * 1024) __hip_atomic_store((gu32*)epoch, tag, RLX_AGENT);
}


extern "C" void kernel_launch(void* const* d_in, const int* in_sizes, int n_in, void* d_out, int out_size,
                              void* d_ws, size_t ws_size, hipStream_t stream) {
  (void)in_sizes; (void)n_in; (void)out_size; (void)ws_size;
  (void)hipFuncSetAttribute((const void*)mega_kernel, hipFuncAttributeMaxDynamicSharedMemorySize, L_TOTAL);
  mega_kernel<<<NWG, NT, L_TOTAL, stream>>>(
      (const uint16_t*)d_in[0], (const uint16_t*)d_in[1], (const uint16_t*)d_in[2], (const uint16_t*)d_in[3],
      (const uint16_t*)d_in[4], (const uint16_t*)d_in[6], (const uint16_t*)d_in[7], (const uint16_t*)d_in[8],
      (const uint16_t*)d_in[9], (uint16_t*)d_out, (unsigned*)d_ws
  );
}
```

```cpp
#include <hip/hip_runtime.h>
#include <hip/hip_bf16.h>
#include <stdint.h>

#define GAS __attribute__((address_space(1)))
#define LAS __attribute__((address_space(3)))
typedef GAS unsigned long long gu64;
typedef GAS unsigned gu32;
typedef uint32_t u32x2 __attribute__((ext_vector_type(2)));
typedef uint32_t u32x4 __attribute__((ext_vector_type(4)));
typedef uint32_t u32x8 __attribute__((ext_vector_type(8)));
typedef float f32x2 __attribute__((ext_vector_type(2)));
typedef float f32x4 __attribute__((ext_vector_type(4)));
typedef short s16x4 __attribute__((ext_vector_type(4)));
typedef __attribute__((ext_vector_type(2))) __bf16 bf16x2;
typedef __attribute__((ext_vector_type(8))) __bf16 bf16x8;
typedef __attribute__((ext_vector_type(16))) __bf16 bf16x16;
#define RLX_AGENT __ATOMIC_RELAXED, __HIP_MEMORY_SCOPE_AGENT

constexpr int DM = 640, NH = 32, DH = 192, SK = 32768, WIN = 4096, FF = 1536, QD = NH * DH;
constexpr int NWG = 256, NT = 512, NW = NT / 64;
#define SPINB 20
constexpr unsigned SPIN_LIMIT = 1u << SPINB;
constexpr int TW = 7, SW = 6;
#define CODEB 15872
constexpr int CODEPF = CODEB;
#define RSTEP 392
constexpr int UP = 3;
constexpr int nreq(int i0, int i1) { int n = 0; for (int i = i0; i <= i1 && i < 16; ++i) n += i == 15 ? 9 : (i == 0 || i > 8) ? 4 : 3; return n; }
constexpr int kord(int n) { return n; }
constexpr int vord(int n) { return n; }
constexpr int RT = UP + 1;
constexpr float LOG2E = 1.4426950408889634f;
constexpr float SMALL = 40.f;
constexpr uint32_t SENT = 0xffffffffu;
constexpr int OOR = 1 << 22;

constexpr int RP = 27 * 16;
constexpr int G_P = 0, G_R = G_P + NWG * RP, G_F = G_R + NH * RP;
constexpr int BLK = 16384, NB = 14, O_SG = 6144, O_SH = 6400, O_H = 6656, O_C = 32 * BLK, O_S = 48 * BLK, O_DB = O_S + 4 * NB * BLK, O_PR = O_DB + BLK;
constexpr int NVOTE = 32;

constexpr int QS = 400;
constexpr int RS = 800;
constexpr int S_XP = 0, S_OV = 256, S_HV = 768, S_QB = 896, S_ZQ = 1280, S_RED = 1472, S_ML = S_RED + NW * RS, S_B = S_ML + 128, S_END = S_B + 512;
constexpr int L_SM = 0, L_CS = L_SM + S_END, L_ONE = L_CS + 960, L_FLAG = L_ONE + 192, L_SCH = L_FLAG + 64, L_TAB = L_SCH + 64,
              L_WQ = L_TAB + 6 * NT * 16, L_WO = L_WQ + 32768, L_W2 = L_WO + 30720, L_W1 = L_W2 + 8192, L_TOTAL = L_W1 + 8192;
static_assert(L_TOTAL <= 160 * 1024 && L_TOTAL > 80 * 1024 && S_END % 16 == 0, "LDS");

enum { P3E, P2, P3, P4, P5, P1, RMIN, RCONS, CPAR, PO, PT, PA, PB, PC, PD, PW, NSCH };
#define SCHED_INIT {120, 127, 58, 61, 57, 67, 5, 50, 2, 410, 88, 620, 88, 470, 90, 6}
constexpr int sched[NSCH] = SCHED_INIT;
#define SC0(i) sched[i]
#define SC(i) sched[i]

__device__ __forceinline__ float lo2f(uint32_t p) { return __uint_as_float(p << 16); }
__device__ __forceinline__ float hi2f(uint32_t p) { return __uint_as_float(p & 0xffff0000u); }
__device__ __forceinline__ uint32_t pk2(float a, float b) {
  f32x2 v = {a, b};
  return __builtin_bit_cast(uint32_t, __builtin_convertvector(v, bf16x2));
}
__device__ __forceinline__ float rbf(float f) { return __uint_as_float(pk2(0.f, f)); }
template <int CTRL> __device__ __forceinline__ float dppv(float v) {
  return __int_as_float(__builtin_amdgcn_update_dpp(0, __float_as_int(v), CTRL, 0xf, 0xf, false));
}
template <int CTRL, int RM> __device__ __forceinline__ float dpp_add(float v) {
  return v + __int_as_float(__builtin_amdgcn_update_dpp(0, __float_as_int(v), CTRL, RM, 0xf, CTRL < 0x140));
}
__device__ __forceinline__ float wsum63(float v) {
  v = dpp_add<0x111, 0xf>(v); v = dpp_add<0x112, 0xf>(v); v = dpp_add<0x114, 0xf>(v); v = dpp_add<0x118, 0xf>(v);
  v = dpp_add<0x142, 0xa>(v); v = dpp_add<0x143, 0xc>(v);
  return v;
}
__device__ __forceinline__ float wsum_bcast(float v) {
  return __int_as_float(__builtin_amdgcn_readlane(__float_as_int(wsum63(v)), 63));
}
__device__ __forceinline__ float swapadd32(float a, float b) {
  auto r = __builtin_amdgcn_permlane32_swap(__float_as_uint(a), __float_as_uint(b), false, false);
  return __uint_as_float(r[0]) + __uint_as_float(r[1]);
}
__device__ __forceinline__ float swapadd16(float a, float b) {
  auto r = __builtin_amdgcn_permlane16_swap(__float_as_uint(a), __float_as_uint(b), false, false);
  return __uint_as_float(r[0]) + __uint_as_float(r[1]);
}
__device__ __forceinline__ float ror8add(float v) { return v + dppv<0x128>(v); }
__device__ __forceinline__ float sum_b320(const u32x4& v) {
  float a, b, c;
  asm("v_add_f32_dpp %0, %3, %3 row_ror:8 row_mask:0xf bank_mask:0x3\n\tv_add_f32_dpp %1, %4, %4 row_ror:8 row_mask:0xf bank_mask:0x3\n\t"
      "v_add_f32_dpp %0, %5, %5 row_ror:8 row_mask:0xf bank_mask:0xc\n\tv_add_f32_dpp %1, %6, %6 row_ror:8 row_mask:0xf bank_mask:0xc\n\ts_nop 0\n\t"
      "v_add_f32_dpp %2, %0, %0 row_ror:12 row_mask:0xf bank_mask:0x5\n\tv_add_f32_dpp %2, %1, %1 row_ror:4 row_mask:0xf bank_mask:0xa\n\ts_nop 1\n\t"
      "v_add_f32_dpp %2, %2, %2 quad_perm:[1,0,3,2] row_mask:0xf bank_mask:0xf"
      : "=&v"(a), "=&v"(b), "=&v"(c) : "v"(v.x), "v"(v.y), "v"(v.z), "v"(v.w));
  return c;
}
#define ZERO4(d, a, b, c, e) asm volatile("v_mov_b64 v[" #a ":" #b "], 0\n\tv_mov_b64 v[" #c ":" #e "], 0" : "={v[" #a ":" #e "]}"(d))
__device__ __forceinline__ int opos(int k) { return ((k >> 1) & 1) * 192 + (k >> 6) * 64 + (k & 32) + (k & 8) * 2 + (k & 4) * 2 + ((k >> 4) & 1) * 4 + (k & 1) * 2; }
__device__ __forceinline__ void lds_barrier() { asm volatile("s_waitcnt lgkmcnt(0)\n\ts_barrier" ::: "memory"); }
__device__ __forceinline__ uint32_t tagbad(const u32x4& r, uint32_t tag) { return (r.y ^ tag) | (r.w ^ tag); }
__device__ __forceinline__ float uf(uint32_t u) { return __uint_as_float(u); }
__device__ __forceinline__ uint32_t rt32() {
  unsigned long long t;
  asm volatile("s_memrealtime %0\n\ts_waitcnt lgkmcnt(0)" : "=s"(t)::"memory");
  return (uint32_t)t;
}
__device__ __forceinline__ f32x4 mfma32(const u32x4& a, const u32x4& b, const f32x4& c) {
  return __builtin_amdgcn_mfma_f32_16x16x32_bf16(__builtin_bit_cast(bf16x8, a), __builtin_bit_cast(bf16x8, b), c, 0, 0, 0);
}
template <int H> __device__ __forceinline__ f32x4 smf(const u32x4& a, const u32x8& b, const f32x4& c, uint32_t ix) {
  return __builtin_amdgcn_smfmac_f32_16x16x64_bf16(__builtin_bit_cast(bf16x8, a), __builtin_bit_cast(bf16x16, b), c, ix, 0, H);
}
__device__ __forceinline__ int qpos(int v) { return ((v >> 1) & 1) * 192 + (v >> 2) * 4 + (v & 1) * 2; }
__device__ __forceinline__ u32x4 cat(const u32x2& a, const u32x2& b) { return (u32x4){a.x, a.y, b.x, b.y}; }
__device__ __forceinline__ int kpos(int k) { return (k & ~31) | ((k & 12) << 1) | ((k & 16) >> 2) | (k & 3); }

#define PIN1(a) asm volatile("" : "+v"(a))
#define PINS(a) asm volatile("" : "+s"(a))
#define PINS2(a, c) asm volatile("" : "+s"(a), "+s"(c))
#define SB() do { asm volatile("" ::: "memory"); __builtin_amdgcn_sched_barrier(0); } while (0)
#define BAR0() asm volatile("s_barrier" ::: "memory")
template <int V> using IC = std::integral_constant<int, V>;

#define TAIL_ASM \
  "s_branch .LGO%=\n\t.p2align 7\n\ts_nop 0\n\ts_nop 0\n\ts_nop 0\n\ts_nop 0\n\ts_nop 0\n\ts_nop 0\n\ts_nop 0\n\ts_nop 0\n\t.LGO%=:\ns_mov_b32 s80, 0\n\t" \
  "s_mov_b32 s84, -1\n\ts_getpc_b64 s[98:99]\n\t.LPC%=:\ns_add_u32 s98, s98, .LCLK%=-.LPC%=\n\ts_addc_u32 s99, s99, 0\n\t.LLOOP%=:\n" \
  "s_cmp_gt_u32 %[w], 4\n\ts_cbranch_scc1 .LH2X%=\n\tds_read_b64 v[240:241], %[t16] offset:%[tab]+8192\n\ts_mov_b32 s81, %[oG]\n\t" \
  "s_add_u32 s82, s81, %[osg]\n\ts_mov_b32 s83, %[eC]\n\tv_and_b32 v242, 16, %[t16]\n\tv_mov_b64 v[232:233], 0\n\tv_mov_b64 v[234:235], 0\n\t" \
  "v_mad_u32_u24 v242, v242, 12, %[bo]\n\ts_cmp_eq_u32 s80, 0\n\ts_cbranch_scc1 .LH2A%=\n\ts_waitcnt lgkmcnt(0)\n\t" \
  "ds_read_b64_tr_b16 v[204:205], v241\n\tds_read_b64_tr_b16 v[206:207], v241 offset:2560\n\tds_read_b64_tr_b16 v[208:209], v241 offset:5120\n\t" \
  "ds_read_b64_tr_b16 v[210:211], v241 offset:7680\n\tds_read_b64_tr_b16 v[212:213], v241 offset:10240\n\t" \
  "ds_read_b64_tr_b16 v[214:215], v241 offset:12800\n\tds_read_b64_tr_b16 v[216:217], v241 offset:15360\n\t" \
  "ds_read_b64_tr_b16 v[218:219], v241 offset:17920\n\tds_read_b64_tr_b16 v[220:221], v241 offset:20480\n\t" \
  "ds_read_b64_tr_b16 v[222:223], v241 offset:23040\n\tds_read_b64_tr_b16 v[224:225], v241 offset:25600\n\t" \
  "ds_read_b64_tr_b16 v[226:227], v241 offset:28160\n\t.LH2A%=:\ns_cmp_gt_u32 %[w], 2\n\ts_cbranch_scc1 .LH2M%=\n\ts_barrier\n\t" \
  "buffer_load_dwordx2 v[202:203], %[ss], %[rs], s82 offen sc1\n\tbuffer_load_dwordx4 v[194:197], %[s0], %[rs], s81 offen sc1\n\t" \
  "buffer_load_dwordx4 v[198:201], %[s0], %[rs], s81 offen offset:128 sc1\n\ts_waitcnt vmcnt(2) lgkmcnt(0)\n\t.LRED2%=:\n" \
  "v_cmp_ne_u32 s[90:91], 0, v202\n\ts_waitcnt vmcnt(0)\n\tv_permlane32_swap_b32 v194, v195\n\tv_permlane32_swap_b32 v196, v197\n\t" \
  "v_add_f32_dpp v247, v203, v203 quad_perm:[1,0,3,2] row_mask:0xf bank_mask:0xf\n\tv_permlane32_swap_b32 v198, v199\n\t" \
  "v_permlane32_swap_b32 v200, v201\n\tv_add_f32 v194, v194, v195\n\tv_add_f32 v196, v196, v197\n\t" \
  "v_add_f32_dpp v247, v247, v247 quad_perm:[2,3,0,1] row_mask:0xf bank_mask:0xf\n\tv_add_f32 v198, v198, v199\n\tv_add_f32 v200, v200, v201\n\t" \
  "v_permlane16_swap_b32 v194, v196\n\tv_add_f32_dpp v247, v247, v247 row_half_mirror row_mask:0xf bank_mask:0xf\n\tv_permlane16_swap_b32 v198, v200\n\t" \
  "v_add_f32 v194, v194, v196\n\tv_add_f32 v198, v198, v200\n\tv_rcp_f32 v247, v247\n\t" \
  "v_add_f32_dpp v246, v194, v194 row_ror:8 row_mask:0xf bank_mask:0x3\n\tv_add_f32_dpp v246, v198, v198 row_ror:8 row_mask:0xf bank_mask:0xc\n\t" \
  "v_mul_f32 v246, v246, v247\n\tv_cvt_pk_bf16_f32 v247, v246, v246\n\tds_write_b16 v240, v247\n\tv_cmp_u_f32 s[92:93], v246, v246\n\t" \
  "s_or_b64 s[90:91], s[90:91], s[92:93]\n\ts_cbranch_scc1 .LSL2%=\n\t.LH2B%=:\ns_cmp_eq_u32 s80, 0\n\ts_cbranch_scc1 .LWO0%=\n\ts_waitcnt lgkmcnt(0)\n\t" \
  "s_barrier\n\tds_read_b128 v[194:197], v242 offset:%[ov]\n\tds_read_b128 v[198:201], v242 offset:%[ov]+64\n\t" \
  "ds_read_b128 v[228:231], v242 offset:%[ov]+128\n\ts_waitcnt lgkmcnt(2)\n\tv_smfmac_f32_16x16x64_bf16 v[232:235], v[194:197], v[204:211], %[ix]\n\t" \
  "s_waitcnt lgkmcnt(1)\n\tv_smfmac_f32_16x16x64_bf16 v[232:235], v[198:201], v[212:219], %[ix]\n\ts_waitcnt lgkmcnt(0)\n\t" \
  "v_smfmac_f32_16x16x64_bf16 v[232:235], v[228:231], v[220:227], %[ix]\n\t.LWOE%=:\ns_nop 7\n\tv_add_f32 v232, v232, v233\n\t" \
  "buffer_store_dword v232, %[dt], %[rs], s83 offen sc1\n\tds_read_b64 v[240:241], %[t16] offset:%[tab]+8192+8\n\t" \
  "ds_read_b32 %[s0], %[t16] offset:%[tab]+16384+12\n\tds_read_b32 v242, %[t16] offset:%[tab]+32768\n\ts_mov_b32 s81, %[rC]\n\ts_mov_b32 s83, %[eH]\n\t" \
  "v_mov_b64 v[214:215], 0\n\ts_waitcnt lgkmcnt(2)\n\tds_read_b64_tr_b16 v[204:205], v241\n\tds_read_b64_tr_b16 v[206:207], v241 offset:1536\n\t" \
  "ds_read_b64_tr_b16 v[208:209], v241 offset:3072\n\tds_read_b64_tr_b16 v[210:211], v241 offset:4608\n\t" \
  "ds_read_b64_tr_b16 v[212:213], v241 offset:6144\n\ts_waitcnt lgkmcnt(0)\n\ts_barrier\n\t" \
  "buffer_load_dwordx4 v[194:197], %[t16], %[rs], s81 offen sc1\n\tbuffer_load_dwordx4 v[198:201], %[t16], %[rs], s81 offen offset:1024 sc1\n\t" \
  "s_waitcnt vmcnt(0)\n\t.LRED3%=:\nv_pk_add_f32 v[194:195], v[194:195], v[198:199]\n\tv_pk_add_f32 v[196:197], v[196:197], v[200:201]\n\ts_nop 0\n\t" \
  "v_permlane32_swap_b32 v194, v195\n\tv_permlane32_swap_b32 v196, v197\n\tv_add_f32 v194, v194, v195\n\tv_add_f32 v196, v196, v197\n\ts_nop 1\n\t" \
  "v_permlane16_swap_b32 v194, v196\n\tv_add_f32 v194, v194, v196\n\ts_nop 1\n\tv_add_f32_dpp v194, v194, v194 row_ror:8 row_mask:0xf bank_mask:0xf\n\t" \
  "s_nop 1\n\tv_add_f32_dpp v246, v194, v194 row_ror:4 row_mask:0xf bank_mask:0xf\n\tv_cvt_pk_bf16_f32 v247, 0, v246\n\tv_add_f32 v247, v242, v247\n\t" \
  "v_cvt_pk_bf16_f32 v243, v247, 0\n\tds_write_b16 v240, v243\n\tv_cmp_u_f32 vcc, v246, v246\n\ts_cbranch_vccnz .LSL3%=\n\t.LH3B%=:\n" \
  "s_bitcmp0_b32 11, %[w]\n\ts_cbranch_scc1 .LH3M%=\n\ts_waitcnt lgkmcnt(0)\n\ts_barrier\n\tds_read_b128 v[194:197], %[bo] offset:%[xp]\n\t" \
  "ds_read_b128 v[198:201], %[bo] offset:%[xp]+64\n\tds_read_b128 v[228:231], %[bo] offset:%[xp]+128\n\ts_waitcnt lgkmcnt(2)\n\t" \
  "v_mfma_f32_16x16x32_bf16 v[232:235], v[194:197], v[204:207], 0\n\ts_waitcnt lgkmcnt(1)\n\t" \
  "v_mfma_f32_16x16x32_bf16 v[232:235], v[198:201], v[208:211], v[232:235]\n\ts_waitcnt lgkmcnt(0)\n\t" \
  "v_mfma_f32_16x16x32_bf16 v[232:235], v[228:231], v[212:215], v[232:235]\n\ts_nop 7\n\tbuffer_store_dword v232, %[dt], %[rs], s83 offen sc1\n\t" \
  ".LH3E%=:\nv_lshlrev_b32 v243, 16, v243\n\tds_write_b32 %[t16], v243 offset:%[tab]+32768\n\tds_read_b128 v[240:243], %[t16] offset:%[tab]+16384\n\t" \
  "ds_read_b32 v244, %[t16] offset:%[tab]+32768+8\n\ts_add_u32 s82, %[oG], %[osh]\n\ts_add_u32 s81, %[oG], %[oh]\n\ts_sub_u32 s85, %[dE], 0x28000\n\t" \
  "s_cmp_eq_u32 s80, 0\n\ts_cselect_b32 s85, s85, 0x8000\n\ts_add_u32 s83, %[eC], s85\n\tv_mov_b64 v[210:211], 0\n\tv_mov_b32 v245, 0xc0135761\n\t" \
  "s_waitcnt lgkmcnt(1)\n\tds_read_b64_tr_b16 v[204:205], v241\n\tds_read_b64_tr_b16 v[206:207], v241 offset:2560\n\t" \
  "ds_read_b64_tr_b16 v[208:209], v241 offset:5120\n\tds_read_b32 v242, v242\n\ts_cmp_gt_u32 %[w], 1\n\ts_cbranch_scc1 .LH4M%=\n\t" \
  "s_waitcnt lgkmcnt(0)\n\ts_barrier\n\tbuffer_load_dwordx2 v[202:203], %[ss], %[rs], s82 offen sc1\n\t" \
  "buffer_load_dwordx4 v[194:197], %[s0], %[rs], s81 offen sc1\n\ts_waitcnt vmcnt(1)\n\t.LRED4%=:\n" \
  "v_add_f32_dpp v198, v202, v202 quad_perm:[1,0,3,2] row_mask:0xf bank_mask:0xf\n\t" \
  "v_add_f32_dpp v199, v203, v203 quad_perm:[1,0,3,2] row_mask:0xf bank_mask:0xf\n\ts_waitcnt vmcnt(0)\n\tv_permlane32_swap_b32 v194, v195\n\t" \
  "v_add_f32_dpp v198, v198, v198 quad_perm:[2,3,0,1] row_mask:0xf bank_mask:0xf\n\t" \
  "v_add_f32_dpp v199, v199, v199 quad_perm:[2,3,0,1] row_mask:0xf bank_mask:0xf\n\tv_permlane32_swap_b32 v196, v197\n\tv_add_f32 v194, v194, v195\n\t" \
  "v_add_f32_dpp v198, v198, v198 row_half_mirror row_mask:0xf bank_mask:0xf\n\t" \
  "v_add_f32_dpp v199, v199, v199 row_half_mirror row_mask:0xf bank_mask:0xf\n\tv_add_f32 v196, v196, v197\n\tv_mul_f32 v199, 0x3acccccd, v199\n\t" \
  "v_mul_f32 v198, 0x3acccccd, v198\n\tv_permlane16_swap_b32 v194, v196\n\tv_fma_f32 v198, -v199, v199, v198\n\tv_add_f32 v194, v194, v196\n\t" \
  "v_cvt_pk_bf16_f32 v198, 0, v198\n\tv_cvt_pk_bf16_f32 v199, 0, v199\n\tv_add_f32 v198, |v198|, %[eps]\n\t" \
  "v_add_f32_dpp v194, v194, v194 row_ror:8 row_mask:0xf bank_mask:0xf\n\tv_cvt_pk_bf16_f32 v198, 0, v198\n\tv_sqrt_f32 v198, v198\n\t" \
  "v_fma_f32 v194, -v199, v242, v194\n\tv_cvt_pk_bf16_f32 v198, 0, v198\n\tv_rcp_f32 v198, v198\n\ts_nop 0\n\tv_mul_f32 v246, v194, v198\n\t" \
  "v_cvt_pk_bf16_f32 v247, 0, v246\n\tv_add_f32 v247, v244, v247\n\tv_cvt_pk_bf16_f32 v247, 0, v247\n\tv_mul_f32 v198, v247, v247\n\t" \
  "v_fmamk_f32 v198, v198, 0xbdd2d3e7, v245\n\tv_mul_f32 v198, v198, v247\n\tv_exp_f32 v198, v198\n\tv_cmp_u_f32 vcc, v246, v246\n\t" \
  "v_add_f32 v198, 1.0, v198\n\tv_rcp_f32 v198, v198\n\ts_nop 0\n\tv_mul_f32 v198, v198, v247\n\tv_cvt_pk_bf16_f32 v198, v198, v198\n\t" \
  "ds_write_b16 v240, v198\n\ts_cbranch_vccnz .LSL4%=\n\t.LH4B%=:\ns_waitcnt lgkmcnt(0)\n\ts_barrier\n\tds_read_b128 v[194:197], %[bo] offset:%[hv]\n\t" \
  "ds_read_b128 v[198:201], %[bo] offset:%[hv]+64\n\ts_waitcnt lgkmcnt(1)\n\tv_mfma_f32_16x16x32_bf16 v[232:235], v[194:197], v[204:207], 0\n\t" \
  "s_waitcnt lgkmcnt(0)\n\tv_mfma_f32_16x16x32_bf16 v[232:235], v[198:201], v[208:211], v[232:235]\n\ts_nop 7\n\t" \
  "buffer_store_dword v232, %[dt], %[rs], s83 offen sc1\n\t.LH4E%=:\ns_and_b32 s88, s80, %[far]\n\ts_cbranch_scc1 .LEND%=\n\t" \
  "ds_read_b64 v[240:241], %[t16] offset:%[tab]+24576+8\n\tds_read_b32 v242, %[t16] offset:%[tab]+8192+8\n\t" \
  "ds_read_b64 v[244:245], %[t16] offset:%[tab]+32768\n\ts_sub_u32 s85, %[dR], 0x28000\n\ts_cmp_eq_u32 s80, 0\n\ts_cselect_b32 s85, s85, 0x8000\n\t" \
  "s_add_u32 s81, %[rC], s85\n\ts_mov_b32 s83, %[eQ]\n\tv_mov_b64 v[214:215], 0\n\tv_mov_b64 v[226:227], 0\n\ts_waitcnt lgkmcnt(2)\n\t" \
  "ds_read_b64_tr_b16 v[204:205], v240\n\tds_read_b64_tr_b16 v[216:217], v240 offset:32\n\tds_read_b64_tr_b16 v[206:207], v240 offset:6400\n\t" \
  "ds_read_b64_tr_b16 v[218:219], v240 offset:6432\n\tds_read_b64_tr_b16 v[208:209], v240 offset:12800\n\t" \
  "ds_read_b64_tr_b16 v[220:221], v240 offset:12832\n\tds_read_b64_tr_b16 v[210:211], v240 offset:19200\n\t" \
  "ds_read_b64_tr_b16 v[222:223], v240 offset:19232\n\tds_read_b64_tr_b16 v[212:213], v240 offset:25600\n\t" \
  "ds_read_b64_tr_b16 v[224:225], v240 offset:25632\n\ts_cmp_gt_u32 %[w], 4\n\ts_cbranch_scc1 .LH5M%=\n\ts_waitcnt lgkmcnt(0)\n\ts_barrier\n\t" \
  "buffer_load_dwordx4 v[194:197], %[t16], %[rs], s81 offen sc1\n\tbuffer_load_dwordx4 v[198:201], %[t16], %[rs], s81 offen offset:1024 sc1\n\t" \
  "s_waitcnt vmcnt(0)\n\t.LRED5%=:\nv_pk_add_f32 v[194:195], v[194:195], v[198:199]\n\tv_pk_add_f32 v[196:197], v[196:197], v[200:201]\n\ts_nop 0\n\t" \
  "v_permlane32_swap_b32 v194, v195\n\tv_permlane32_swap_b32 v196, v197\n\tv_add_f32 v194, v194, v195\n\tv_add_f32 v196, v196, v197\n\ts_nop 1\n\t" \
  "v_permlane16_swap_b32 v194, v196\n\tv_add_f32 v194, v194, v196\n\ts_nop 1\n\tv_add_f32_dpp v194, v194, v194 row_ror:8 row_mask:0xf bank_mask:0xf\n\t" \
  "s_nop 1\n\tv_add_f32_dpp v246, v194, v194 row_ror:4 row_mask:0xf bank_mask:0xf\n\tv_cvt_pk_bf16_f32 v247, 0, v246\n\tv_cmp_u_f32 vcc, v246, v246\n\t" \
  "v_add_f32 v247, v245, v247\n\tv_cvt_pk_bf16_f32 v247, 0, v247\n\tv_add_f32 v247, v244, v247\n\tv_cvt_pk_bf16_f32 v243, v247, 0\n\t" \
  "ds_write_b16 v242, v243\n\ts_cbranch_vccnz .LSL5%=\n\t.LH5B%=:\ns_cmp_lg_u32 s80, 0\n\ts_cbranch_scc1 .LFIN%=\n\t.LH5C%=:\ns_waitcnt lgkmcnt(0)\n\t" \
  "s_barrier\n\tds_read_b128 v[194:197], %[bo] offset:%[xp]\n\tds_read_b128 v[198:201], %[bo] offset:%[xp]+64\n\t" \
  "ds_read_b128 v[228:231], %[bo] offset:%[xp]+128\n\ts_waitcnt lgkmcnt(2)\n\tv_mfma_f32_16x16x32_bf16 v[232:235], v[194:197], v[204:207], 0\n\t" \
  "v_mfma_f32_16x16x32_bf16 v[236:239], v[194:197], v[216:219], 0\n\ts_waitcnt lgkmcnt(1)\n\t" \
  "v_mfma_f32_16x16x32_bf16 v[232:235], v[198:201], v[208:211], v[232:235]\n\tv_mfma_f32_16x16x32_bf16 v[236:239], v[198:201], v[220:223], v[236:239]\n\t" \
  "s_waitcnt lgkmcnt(0)\n\tv_mfma_f32_16x16x32_bf16 v[232:235], v[228:231], v[212:215], v[232:235]\n\t" \
  "v_mfma_f32_16x16x32_bf16 v[236:239], v[228:231], v[224:227], v[236:239]\n\ts_nop 6\n\tbuffer_store_dword v232, %[dt], %[rs], s83 offen sc1\n\t" \
  "buffer_store_dword v236, %[dt], %[rs], s83 offen offset:64 sc1\n\tv_lshlrev_b32 v243, 16, v243\n\tds_write_b32 %[t16], v243 offset:%[tab]+32768\n\t" \
  ".LH1%=:\nds_read_b64 v[204:205], %[t16] offset:%[tab]+24576\n\tds_read_b128 v[240:243], %[t16] offset:%[tab]\n\t" \
  "ds_read_b128 v[236:239], %[t16] offset:%[tab]+40960\n\tds_read_b32 v248, %[t16] offset:%[tab]+32768+12\n\ts_mul_i32 s88, %[w], %[rsp]\n\t" \
  "v_add_u32 v249, s88, %[bo]\n\ts_add_u32 %[oG], %[oG], %[dG]\n\ts_add_u32 %[eH], %[eH], %[dH]\n\ts_add_u32 %[rC], %[rC], %[dR]\n\t" \
  "s_add_u32 %[eC], %[eC], %[dE]\n\ts_sub_u32 s81, %[oG], 0x10000\n\ts_add_u32 s82, s81, %[osg]\n\ts_mov_b32 s92, 0x00ff00ff\n\t" \
  "s_mov_b32 s93, 0xff00ff00\n\tv_mov_b64 v[208:209], 0\n\tv_mov_b64 v[210:211], 0\n\tv_mov_b64 v[212:213], 0\n\tv_mov_b64 v[214:215], 0\n\t" \
  "v_mov_b64 v[216:217], 0\n\tv_mov_b64 v[218:219], 0\n\tv_mov_b64 v[220:221], 0\n\tv_mov_b64 v[222:223], 0\n\tv_mov_b64 v[224:225], 0\n\t" \
  "v_mov_b64 v[226:227], 0\n\tv_mov_b64 v[228:229], 0\n\tv_mov_b64 v[230:231], 0\n\tv_mov_b64 v[232:233], 0\n\tv_mov_b64 v[234:235], 0\n\t" \
  "s_waitcnt lgkmcnt(3)\n\tds_read_b32 v205, v205\n\ts_cmp_gt_u32 %[w], 2\n\ts_cbranch_scc1 .LH1X%=\n\ts_waitcnt lgkmcnt(0)\n\ts_barrier\n\t" \
  "buffer_load_dwordx2 v[202:203], %[ss], %[rs], s82 offen sc1\n\tbuffer_load_dwordx4 v[194:197], v243, %[rs], s81 offen sc1\n\t" \
  "buffer_load_dwordx4 v[198:201], v243, %[rs], s81 offen offset:128 sc1\n\ts_waitcnt vmcnt(0)\n\t.LRED1%=:\nv_permlane32_swap_b32 v194, v195\n\t" \
  "v_permlane32_swap_b32 v196, v197\n\tv_add_f32_dpp v206, v203, v203 quad_perm:[1,0,3,2] row_mask:0xf bank_mask:0xf\n\t" \
  "v_permlane32_swap_b32 v198, v199\n\tv_permlane32_swap_b32 v200, v201\n\tv_add_f32 v194, v194, v195\n\tv_add_f32 v196, v196, v197\n\t" \
  "v_add_f32_dpp v206, v206, v206 quad_perm:[2,3,0,1] row_mask:0xf bank_mask:0xf\n\tv_add_f32 v198, v198, v199\n\tv_add_f32 v200, v200, v201\n\t" \
  "v_permlane16_swap_b32 v194, v196\n\tv_add_f32_dpp v206, v206, v206 row_half_mirror row_mask:0xf bank_mask:0xf\n\tv_permlane16_swap_b32 v198, v200\n\t" \
  "v_add_f32 v194, v194, v196\n\tv_add_f32 v198, v198, v200\n\tv_mul_f32 v206, 0x3acccccd, v206\n\t" \
  "v_add_f32_dpp v246, v194, v194 row_ror:8 row_mask:0xf bank_mask:0x3\n\tv_add_f32_dpp v246, v198, v198 row_ror:8 row_mask:0xf bank_mask:0xc\n\t" \
  "v_cvt_pk_bf16_f32 v247, 0, v206\n\tv_fma_f32 v246, -v247, v205, v246\n\tv_cvt_pk_bf16_f32 v247, v246, v246\n\tds_write_b16 v204, v247\n\t" \
  "v_cmp_u_f32 vcc, v246, v246\n\ts_cbranch_vccnz .LSL1%=\n\t.LH1B%=:\ns_waitcnt lgkmcnt(0)\n\ts_barrier\n\tds_read_b128 v[194:197], v236\n\t" \
  "ds_read_b128 v[198:201], v236 offset:64\n\tds_read_b128 v[204:207], v236 offset:128\n\tds_read_b32 v202, v239 offset:%[qsw]\n\t" \
  "s_waitcnt lgkmcnt(3)\n\tv_smfmac_f32_16x16x64_bf16 v[208:211], v[194:197], v[2:9], %[ix]\n\t" \
  "v_smfmac_f32_16x16x64_bf16 v[212:215], v[194:197], v[26:33], %[ix]\n\tv_smfmac_f32_16x16x64_bf16 v[216:219], v[194:197], v[50:57], %[ix]\n\t" \
  "v_smfmac_f32_16x16x64_bf16 v[220:223], v[194:197], v[74:81], %[ix]\n\ts_waitcnt lgkmcnt(2)\n\t" \
  "v_smfmac_f32_16x16x64_bf16 v[208:211], v[198:201], v[10:17], %[ix]\n\tv_smfmac_f32_16x16x64_bf16 v[212:215], v[198:201], v[34:41], %[ix]\n\t" \
  "v_smfmac_f32_16x16x64_bf16 v[216:219], v[198:201], v[58:65], %[ix]\n\tv_smfmac_f32_16x16x64_bf16 v[220:223], v[198:201], v[82:89], %[ix]\n\t" \
  "s_waitcnt lgkmcnt(1)\n\tv_smfmac_f32_16x16x64_bf16 v[208:211], v[204:207], v[18:25], %[ix]\n\t" \
  "v_smfmac_f32_16x16x64_bf16 v[212:215], v[204:207], v[42:49], %[ix]\n\tv_smfmac_f32_16x16x64_bf16 v[216:219], v[204:207], v[66:73], %[ix]\n\t" \
  "v_smfmac_f32_16x16x64_bf16 v[220:223], v[204:207], v[90:97], %[ix]\n\ts_setprio 1\n\ts_nop 3\n\tv_add_f32 v246, v208, v209\n\t" \
  "v_add_f32 v203, v212, v213\n\tv_cndmask_b32 v246, v203, v246, s[92:93]\n\tv_add_f32 v247, v216, v217\n\tv_add_f32 v203, v220, v221\n\t" \
  "v_cndmask_b32 v247, v203, v247, s[92:93]\n\ts_waitcnt lgkmcnt(0)\n\ts_nop 0\n\tv_permlane32_swap_b32 v246, v247\n\ts_nop 1\n\t" \
  "v_add_f32_dpp v246, v247, v246 row_ror:8 row_mask:0xf bank_mask:0xf\n\tv_mul_f32 v246, v246, v202\n\tv_cvt_pk_bf16_f32 v246, 0, v246\n\t" \
  "v_mul_f32 v246, 0x3d93cd3a, v246\n\tv_cvt_pk_bf16_f32 v246, 0, v246\n\tv_mul_f32 v246, 0x3fb8aa3b, v246\n\tv_exp_f32 v203, v246\n\t" \
  "v_cmp_nle_f32 vcc, |v246|, %[small]\n\tv_cvt_pk_bf16_f32 v244, v203, 0\n\ts_cbranch_vccnz .LSLM%=\n\t.LPV%=:\nds_write_b32 v248, v244\n\t" \
  "ds_read_b128 v[208:211], v249 offset:%[red]+0\n\tds_read_b128 v[212:215], v249 offset:%[red]+64\n\tds_read_b128 v[216:219], v249 offset:%[red]+128\n\t" \
  "ds_read_b128 v[220:223], v249 offset:%[red]+192\n\ts_setprio 0\n\tv_add_f32_dpp v203, v203, v203 row_ror:1 row_mask:0xf bank_mask:0xf\n\ts_nop 1\n\t" \
  "v_add_f32_dpp v203, v203, v203 row_ror:2 row_mask:0xf bank_mask:0xf\n\ts_nop 1\n\t" \
  "v_add_f32_dpp v203, v203, v203 row_ror:4 row_mask:0xf bank_mask:0xf\n\ts_nop 1\n\t" \
  "v_add_f32_dpp v203, v203, v203 row_ror:8 row_mask:0xf bank_mask:0xf\n\ts_nop 1\n\t" \
  "v_add_f32_dpp v203, v203, v203 row_bcast:15 row_mask:0xa bank_mask:0xf\n\ts_nop 1\n\ts_waitcnt lgkmcnt(3)\n\t" \
  "v_smfmac_f32_16x16x64_bf16 v[224:227], v[208:211], v[98:105], %[ix] abid:1\n\t" \
  "v_smfmac_f32_16x16x64_bf16 v[228:231], v[208:211], v[106:113], %[ix] abid:1\n\t" \
  "v_smfmac_f32_16x16x64_bf16 v[232:235], v[208:211], v[114:121], %[ix] abid:1\n\ts_waitcnt lgkmcnt(2)\n\t" \
  "v_smfmac_f32_16x16x64_bf16 v[224:227], v[212:215], v[122:129], %[ix] abid:1\n\t" \
  "v_smfmac_f32_16x16x64_bf16 v[228:231], v[212:215], v[130:137], %[ix] abid:1\n\t" \
  "v_smfmac_f32_16x16x64_bf16 v[232:235], v[212:215], v[138:145], %[ix] abid:1\n\ts_waitcnt lgkmcnt(1)\n\t" \
  "v_smfmac_f32_16x16x64_bf16 v[224:227], v[216:219], v[146:153], %[ix] abid:1\n\t" \
  "v_smfmac_f32_16x16x64_bf16 v[228:231], v[216:219], v[154:161], %[ix] abid:1\n\t" \
  "v_smfmac_f32_16x16x64_bf16 v[232:235], v[216:219], v[162:169], %[ix] abid:1\n\ts_waitcnt lgkmcnt(0)\n\t" \
  "v_smfmac_f32_16x16x64_bf16 v[224:227], v[220:223], v[170:177], %[ix] abid:1\n\t" \
  "v_smfmac_f32_16x16x64_bf16 v[228:231], v[220:223], v[178:185], %[ix] abid:1\n\t" \
  "v_smfmac_f32_16x16x64_bf16 v[232:235], v[220:223], v[186:193], %[ix] abid:1\n\t" \
  "v_add_f32_dpp v203, v203, v203 row_bcast:31 row_mask:0xc bank_mask:0xf\n\ts_lshl_b64 exec, 1, 63\n\tds_write2_b32 v238, v239, v203 offset1:1\n\t" \
  "s_mov_b64 exec, -1\n\ts_mov_b32 s80, 1\n\ts_mov_b32 s83, %[oD]\n\ts_nop 0\n\tds_write2st64_b32 v237, v224, v228 offset1:1\n\t" \
  "ds_write_b32 v237, v232 offset:512\n\ts_waitcnt lgkmcnt(0)\n\ts_barrier\n\tds_read_b128 v[194:197], v241\n\tds_read_b32 v204, v240\n\t" \
  "v_mov_b32 %[s0], v243\n\ts_waitcnt lgkmcnt(1)\n\t.LCMB%=:\nv_add_f32_dpp v205, v194, v194 row_ror:8 row_mask:0xf bank_mask:0x3\n\t" \
  "v_add_f32_dpp v206, v195, v195 row_ror:8 row_mask:0xf bank_mask:0x3\n\tv_add_f32_dpp v205, v196, v196 row_ror:8 row_mask:0xf bank_mask:0xc\n\t" \
  "v_add_f32_dpp v206, v197, v197 row_ror:8 row_mask:0xf bank_mask:0xc\n\ts_waitcnt lgkmcnt(0)\n\tv_cmp_ne_u32 vcc, 0, v204\n\t" \
  "v_add_f32_dpp v246, v205, v205 row_ror:12 row_mask:0xf bank_mask:0x5\n\tv_add_f32_dpp v246, v206, v206 row_ror:4 row_mask:0xf bank_mask:0xa\n\t" \
  "s_nop 1\n\tv_add_f32_dpp v246, v246, v246 quad_perm:[1,0,3,2] row_mask:0xf bank_mask:0xf\n\ts_cbranch_vccnz .LSLC%=\n\t.LCMS%=:\n" \
  "buffer_store_dword v246, v242, %[rs], s83 offen sc1\n\ts_branch .LLOOP%=\n\t.LWO0%=:\ns_waitcnt lgkmcnt(0)\n\ts_barrier\n\t" \
  "ds_read_b128 v[194:197], v242 offset:%[ov]+0\n\tds_read_b64_tr_b16 v[204:205], v241\n\tds_read_b64_tr_b16 v[206:207], v241 offset:2560\n\t" \
  "ds_read_b64_tr_b16 v[208:209], v241 offset:5120\n\tds_read_b64_tr_b16 v[210:211], v241 offset:7680\n\t" \
  "ds_read_b128 v[198:201], v242 offset:%[ov]+64\n\tds_read_b64_tr_b16 v[212:213], v241 offset:10240\n\t" \
  "ds_read_b64_tr_b16 v[214:215], v241 offset:12800\n\tds_read_b64_tr_b16 v[216:217], v241 offset:15360\n\t" \
  "ds_read_b64_tr_b16 v[218:219], v241 offset:17920\n\tds_read_b128 v[228:231], v242 offset:%[ov]+128\n\t" \
  "ds_read_b64_tr_b16 v[220:221], v241 offset:20480\n\tds_read_b64_tr_b16 v[222:223], v241 offset:23040\n\t" \
  "ds_read_b64_tr_b16 v[224:225], v241 offset:25600\n\tds_read_b64_tr_b16 v[226:227], v241 offset:28160\n\ts_waitcnt lgkmcnt(10)\n\t" \
  "v_smfmac_f32_16x16x64_bf16 v[232:235], v[194:197], v[204:211], %[ix]\n\ts_waitcnt lgkmcnt(5)\n\t" \
  "v_smfmac_f32_16x16x64_bf16 v[232:235], v[198:201], v[212:219], %[ix]\n\ts_waitcnt lgkmcnt(0)\n\t" \
  "v_smfmac_f32_16x16x64_bf16 v[232:235], v[228:231], v[220:227], %[ix]\n\ts_branch .LWOE%=\n\t.LH1X%=:\ns_cmp_eq_u32 %[w], 3\n\t" \
  "s_cbranch_scc1 .LH1S%=\n\ts_cmp_lg_u32 %[w], 7\n\ts_cbranch_scc1 .LH1Y%=\n\ts_mov_b32 s88, %[p1]\n\ts_swappc_b64 s[96:97], s[98:99]\n\t.LH1Y%=:\n" \
  "s_barrier\n\ts_branch .LH1B%=\n\t.LH1S%=:\ns_waitcnt lgkmcnt(0)\n\ts_barrier\n\tbuffer_load_dwordx2 v[202:203], %[ss], %[rs], s82 offen sc1\n\t" \
  "s_waitcnt vmcnt(0)\n\t.LREDS%=:\nv_add_f32_dpp v194, v202, v202 quad_perm:[1,0,3,2] row_mask:0xf bank_mask:0xf\n\t" \
  "v_add_f32_dpp v195, v203, v203 quad_perm:[1,0,3,2] row_mask:0xf bank_mask:0xf\n\ts_nop 0\n\t" \
  "v_add_f32_dpp v194, v194, v194 quad_perm:[2,3,0,1] row_mask:0xf bank_mask:0xf\n\t" \
  "v_add_f32_dpp v195, v195, v195 quad_perm:[2,3,0,1] row_mask:0xf bank_mask:0xf\n\ts_nop 0\n\t" \
  "v_add_f32_dpp v194, v194, v194 row_half_mirror row_mask:0xf bank_mask:0xf\n\t" \
  "v_add_f32_dpp v195, v195, v195 row_half_mirror row_mask:0xf bank_mask:0xf\n\tv_mul_f32 v194, 0x3acccccd, v194\n\tv_mul_f32 v195, 0x3acccccd, v195\n\t" \
  "v_fma_f32 v194, -v195, v195, v194\n\tv_cvt_pk_bf16_f32 v194, 0, v194\n\tv_add_f32 v194, |v194|, %[eps]\n\tv_cvt_pk_bf16_f32 v194, 0, v194\n\t" \
  "v_sqrt_f32 v194, v194\n\ts_nop 0\n\tv_cvt_pk_bf16_f32 v194, 0, v194\n\tv_rcp_f32 v194, v194\n\ts_nop 0\n\tds_write_b32 v239, v194 offset:%[qsw]\n\t" \
  "v_cmp_u_f32 vcc, v194, v194\n\ts_cbranch_vccz .LH1B%=\n\ts_branch .LSLS%=\n\t.LH2M%=:\ns_barrier\n\ts_waitcnt vmcnt(0)\n\ts_branch .LH2B%=\n\t" \
  ".LH3M%=:\ns_waitcnt lgkmcnt(0)\n\ts_barrier\n\ts_branch .LH3E%=\n\t.LH4M%=:\ns_barrier\n\ts_branch .LH4B%=\n\t.LH5M%=:\ns_barrier\n\t" \
  "s_cmp_eq_u32 s80, 0\n\ts_cbranch_scc1 .LH5C%=\n\ts_branch .LFE%=\n\t.LFIN%=:\nbuffer_store_short v243, v241, %[ro], %[oo] offen\n\ts_branch .LFE%=\n\t" \
  ".LH2X%=:\ns_cmp_eq_u32 %[w], 7\n\ts_cbranch_scc1 .LTW%=\n\tds_read_b64 v[240:241], %[t16] offset:%[tab]+16384\n\tv_mov_b64 v[198:199], -1\n\t" \
  "v_mov_b64 v[200:201], -1\n\ts_cmp_lg_u32 s80, 0\n\ts_waitcnt lgkmcnt(0)\n\ts_barrier\n\ts_cbranch_scc1 .LH2Y%=\n\t" \
  "buffer_store_dwordx4 v[198:201], v240, %[rs], 0 offen sc1\n\t.LH2Y%=:\ns_waitcnt vmcnt(0)\n\ts_barrier\n\ts_mov_b32 s83, %[eH]\n\t" \
  "s_cmp_eq_u32 %[w], 6\n\ts_cbranch_scc1 .LSW3%=\n\ts_movk_i32 s88, 0x2000\n\ts_cmp_lg_u32 s80, 0\n\ts_barrier\n\ts_barrier\n\t" \
  "s_cbranch_scc1 .LH3Y%=\n\tbuffer_store_dwordx4 v[198:201], v241, %[rs], 0 offen sc1\n\tbuffer_store_dwordx4 v[198:201], v241, %[rs], s88 offen sc1\n\t" \
  ".LH3Y%=:\ns_barrier\n\ts_waitcnt vmcnt(0)\n\ts_barrier\n\ts_branch .LH4E%=\n\t.LSW3%=:\ns_barrier\n\ts_barrier\n\t" \
  "ds_read_b128 v[194:197], %[bo] offset:%[xp]\n\tds_read_b128 v[204:207], %[ss]\n\tds_read_b128 v[198:201], %[bo] offset:%[xp]+64\n\t" \
  "ds_read_b128 v[208:211], %[ss] offset:64\n\tds_read_b128 v[228:231], %[bo] offset:%[xp]+128\n\tds_read_b128 v[212:215], %[ss] offset:128\n\t" \
  "s_waitcnt lgkmcnt(4)\n\tv_mfma_f32_16x16x32_bf16 v[232:235], v[194:197], v[204:207], 0\n\ts_waitcnt lgkmcnt(2)\n\t" \
  "v_mfma_f32_16x16x32_bf16 v[232:235], v[198:201], v[208:211], v[232:235]\n\ts_waitcnt lgkmcnt(0)\n\t" \
  "v_mfma_f32_16x16x32_bf16 v[232:235], v[228:231], v[212:215], v[232:235]\n\ts_nop 7\n\tbuffer_store_dword v232, %[dt], %[rs], s83 offen sc1\n\t" \
  "s_barrier\n\ts_barrier\n\ts_and_b32 s88, s80, %[far]\n\ts_cbranch_scc1 .LEND%=\n\ts_mov_b32 s83, %[eQ]\n\ts_barrier\n\ts_cmp_lg_u32 s80, 0\n\t" \
  "s_cbranch_scc1 .LFE%=\n\ts_barrier\n\tds_read_b128 v[194:197], %[bo] offset:%[xp]\n\tds_read_b128 v[204:207], %[ss]\n\t" \
  "ds_read_b128 v[198:201], %[bo] offset:%[xp]+64\n\tds_read_b128 v[208:211], %[ss] offset:64\n\tds_read_b128 v[228:231], %[bo] offset:%[xp]+128\n\t" \
  "ds_read_b128 v[212:215], %[ss] offset:128\n\ts_waitcnt lgkmcnt(4)\n\tv_mfma_f32_16x16x32_bf16 v[232:235], v[194:197], v[204:207], 0\n\t" \
  "s_waitcnt lgkmcnt(2)\n\tv_mfma_f32_16x16x32_bf16 v[232:235], v[198:201], v[208:211], v[232:235]\n\ts_waitcnt lgkmcnt(0)\n\t" \
  "v_mfma_f32_16x16x32_bf16 v[232:235], v[228:231], v[212:215], v[232:235]\n\ts_nop 7\n\tbuffer_store_dword v232, %[dt], %[rs], s83 offen sc1\n\t" \
  "s_branch .LH1%=\n\t.LTW%=:\ns_cmp_lg_u32 s80, 0\n\ts_cbranch_scc1 .LTW1%=\n\ts_memrealtime s[94:95]\n\tv_and_b32 v240, 0x3e0, %[t16]\n\t" \
  "v_lshlrev_b32 v241, 9, %[t16]\n\tv_and_b32 v241, 0x2000, v241\n\tv_cmp_eq_u32 vcc, 0, v240\n\tv_mov_b32 v240, %[oor]\n\t" \
  "v_cndmask_b32 v240, v240, v241, vcc\n\tv_mov_b32 v243, %[tag]\n\ts_waitcnt lgkmcnt(0)\n\ts_mov_b32 %[tc], s94\n\tv_mov_b32 v242, s94\n\t" \
  "buffer_store_dwordx2 v[242:243], v240, %[rs], %[cw] offen sc1\n\ts_waitcnt vmcnt(4)\n\ts_mov_b32 s87, %[pw]\n\t.LW1%=:\ns_sub_u32 s87, s87, 1\n\t" \
  "s_cbranch_scc0 .LW1%=\n\ts_barrier\n\ts_waitcnt vmcnt(1)\n\ts_barrier\n\ts_mov_b32 s87, %[spin]\n\t.LTWP%=:\n" \
  "buffer_load_dwordx4 v[194:197], %[t16], %[rs], %[rK] offen sc1\n\tbuffer_load_dwordx4 v[198:201], %[t16], %[rs], %[rK] offen offset:1024 sc1\n\t" \
  "s_waitcnt vmcnt(0)\n\tv_xor_b32 v247, v195, v243\n\tv_xor_b32 v246, v197, v243\n\tv_xor_b32 v199, v199, v243\n\tv_xor_b32 v201, v201, v243\n\t" \
  "v_or3_b32 v247, v247, v246, v199\n\tv_or_b32 v247, v247, v201\n\tv_cmp_ne_u32 vcc, 0, v247\n\ts_cbranch_vccz .LTWQ%=\n\ts_sleep 3\n\t" \
  "s_sub_u32 s87, s87, 1\n\ts_cbranch_scc0 .LTWP%=\n\t.LTWQ%=:\nv_subrev_u32 v194, %[tc], v194\n\tv_subrev_u32 v196, %[tc], v196\n\t" \
  "v_subrev_u32 v198, %[tc], v198\n\tv_subrev_u32 v200, %[tc], v200\n\tv_max_i32 v247, v194, v196\n\tv_max3_i32 v247, v198, v200, v247\n\ts_nop 1\n\t" \
  "v_max_i32_dpp v247, v247, v247 row_ror:1 row_mask:0xf bank_mask:0xf\n\ts_nop 1\n\t" \
  "v_max_i32_dpp v247, v247, v247 row_ror:2 row_mask:0xf bank_mask:0xf\n\ts_nop 1\n\t" \
  "v_max_i32_dpp v247, v247, v247 row_ror:4 row_mask:0xf bank_mask:0xf\n\ts_nop 1\n\t" \
  "v_max_i32_dpp v247, v247, v247 row_ror:8 row_mask:0xf bank_mask:0xf\n\ts_nop 1\n\t" \
  "v_max_i32_dpp v247, v247, v247 row_bcast:15 row_mask:0xa bank_mask:0xf\n\ts_nop 1\n\t" \
  "v_max_i32_dpp v247, v247, v247 row_bcast:31 row_mask:0xc bank_mask:0xf\n\ts_nop 1\n\tv_readlane_b32 s88, v247, 63\n\t" \
  "s_add_u32 %[tc], %[tc], %[p3e]\n\ts_add_u32 %[tc], %[tc], s88\n\ts_mov_b32 %[te], %[tc]\n\ts_add_u32 s94, s98, .LUNT%=-.LCLK%=\n\t" \
  "s_addc_u32 s95, s99, 0\n\ts_swappc_b64 s[96:97], s[94:95]\n\ts_branch .LTW3%=\n\t.LTW1%=:\ns_add_u32 %[tc], %[tc], %[p2]\n\ts_mov_b32 s87, %[pw]\n\t" \
  ".LW2%=:\ns_sub_u32 s87, s87, 1\n\ts_cbranch_scc0 .LW2%=\n\ts_barrier\n\ts_barrier\n\ts_mov_b32 s88, %[p3]\n\ts_swappc_b64 s[96:97], s[98:99]\n\t" \
  ".LTW3%=:\ns_barrier\n\ts_barrier\n\ts_mov_b32 s88, %[p4]\n\ts_swappc_b64 s[96:97], s[98:99]\n\ts_barrier\n\ts_barrier\n\t" \
  "s_and_b32 s88, s80, %[far]\n\ts_cbranch_scc1 .LEND%=\n\ts_mov_b32 s88, %[p5]\n\ts_swappc_b64 s[96:97], s[98:99]\n\ts_barrier\n\t" \
  "s_cmp_lg_u32 s80, 0\n\ts_cbranch_scc1 .LFE%=\n\ts_barrier\n\ts_branch .LH1%=\n\t.LCLK%=:\ns_memrealtime s[94:95]\n\ts_add_u32 %[tc], %[tc], s88\n\t" \
  "s_movk_i32 s87, 64\n\ts_waitcnt lgkmcnt(0)\n\t.LCL1%=:\ns_sub_i32 s88, %[tc], s94\n\ts_cmp_ge_i32 s88, %[rmin]\n\ts_cbranch_scc1 .LUNT%=\n\t" \
  "s_add_u32 %[tc], %[tc], %[rcons]\n\ts_sub_u32 s87, s87, 1\n\ts_cbranch_scc0 .LCL1%=\n\t.LUNT%=:\ns_movk_i32 s87, 0x1000\n\t.LUN1%=:\n" \
  "s_memrealtime s[94:95]\n\ts_sub_u32 s87, s87, 1\n\ts_cbranch_scc1 .LUN2%=\n\ts_waitcnt lgkmcnt(0)\n\ts_sub_i32 s88, %[tc], s94\n\t" \
  "s_cmp_gt_i32 s88, 3\n\ts_cbranch_scc1 .LUN1%=\n\ts_cmp_lt_i32 s88, 1\n\ts_cbranch_scc1 .LUN2%=\n\ts_nop 3\n\ts_cmp_lt_i32 s88, 2\n\t" \
  "s_cbranch_scc1 .LUN2%=\n\ts_nop 3\n\ts_cmp_lt_i32 s88, 3\n\ts_cbranch_scc1 .LUN2%=\n\ts_nop 9\n\t.LUN2%=:\ns_waitcnt lgkmcnt(0)\n\t" \
  "s_setpc_b64 s[96:97]\n\t.LSL2%=:\ns_add_u32 s88, s80, 2\n\ts_cmp_eq_u32 s84, s88\n\ts_cbranch_scc1 .LH2B%=\n\ts_mov_b32 s84, s88\n\t" \
  "s_mov_b32 s87, %[spin]\n\t.LSL2a%=:\nbuffer_load_dwordx2 v[202:203], %[ss], %[rs], s82 offen sc1\n\t" \
  "buffer_load_dwordx4 v[194:197], %[s0], %[rs], s81 offen sc1\n\tbuffer_load_dwordx4 v[198:201], %[s0], %[rs], s81 offen offset:128 sc1\n\t" \
  "s_waitcnt vmcnt(0)\n\tv_max3_u32 v247, v194, v195, v196\n\tv_max3_u32 v247, v247, v197, v198\n\tv_max3_u32 v247, v247, v199, v200\n\t" \
  "v_max3_u32 v247, v247, v201, v202\n\tv_max_u32 v247, v247, v203\n\tv_cmp_eq_u32 vcc, -1, v247\n\ts_cbranch_vccz .LSL2b%=\n\ts_sub_u32 s87, s87, 1\n\t" \
  "s_cbranch_scc0 .LSL2a%=\n\t.LSL2b%=:\nv_cmp_ne_u32 vcc, 0, v202\n\ts_cbranch_vccz .LRED2%=\n\tv_lshrrev_b32 v247, 5, %[t16]\n\t" \
  "v_and_b32 v247, 28, v247\n\tds_bpermute_b32 v247, v247, v202\n\tv_max_f32_dpp v246, v202, v202 quad_perm:[1,0,3,2] row_mask:0xf bank_mask:0xf\n\t" \
  "s_nop 1\n\tv_max_f32_dpp v246, v246, v246 quad_perm:[2,3,0,1] row_mask:0xf bank_mask:0xf\n\ts_nop 1\n\t" \
  "v_max_f32_dpp v246, v246, v246 row_half_mirror row_mask:0xf bank_mask:0xf\n\ts_waitcnt lgkmcnt(0)\n\tv_sub_f32 v247, v247, v246\n\t" \
  "v_sub_f32 v202, v202, v246\n\tv_exp_f32 v247, v247\n\tv_exp_f32 v202, v202\n\ts_nop 0\n\tv_mul_f32 v194, v247, v194\n\tv_mul_f32 v195, v247, v195\n\t" \
  "v_mul_f32 v196, v247, v196\n\tv_mul_f32 v197, v247, v197\n\tv_mul_f32 v198, v247, v198\n\tv_mul_f32 v199, v247, v199\n\tv_mul_f32 v200, v247, v200\n\t" \
  "v_mul_f32 v201, v247, v201\n\tv_mul_f32 v203, v202, v203\n\tv_mov_b32 v202, 0\n\ts_nop 1\n\ts_branch .LRED2%=\n\t.LSL3%=:\ns_add_u32 s88, s80, 4\n\t" \
  "s_cmp_eq_u32 s84, s88\n\ts_cbranch_scc1 .LH3B%=\n\ts_mov_b32 s84, s88\n\ts_mov_b32 s87, %[spin]\n\t.LSL3a%=:\n" \
  "buffer_load_dwordx4 v[194:197], %[t16], %[rs], s81 offen sc1\n\tbuffer_load_dwordx4 v[198:201], %[t16], %[rs], s81 offen offset:1024 sc1\n\t" \
  "s_waitcnt vmcnt(0)\n\tv_max3_u32 v247, v194, v195, v196\n\tv_max3_u32 v247, v247, v197, v198\n\tv_max3_u32 v247, v247, v199, v200\n\t" \
  "v_max_u32 v247, v247, v201\n\tv_cmp_eq_u32 vcc, -1, v247\n\ts_cbranch_vccz .LSL3b%=\n\ts_sleep 1\n\ts_sub_u32 s87, s87, 1\n\t" \
  "s_cbranch_scc0 .LSL3a%=\n\t.LSL3b%=:\ns_branch .LRED3%=\n\t.LSL4%=:\ns_add_u32 s88, s80, 6\n\ts_cmp_eq_u32 s84, s88\n\ts_cbranch_scc1 .LH4B%=\n\t" \
  "s_mov_b32 s84, s88\n\ts_mov_b32 s87, %[spin]\n\t.LSL4a%=:\nbuffer_load_dwordx2 v[202:203], %[ss], %[rs], s82 offen sc1\n\t" \
  "buffer_load_dwordx4 v[194:197], %[s0], %[rs], s81 offen sc1\n\ts_waitcnt vmcnt(0)\n\tv_max3_u32 v247, v194, v195, v196\n\t" \
  "v_max3_u32 v247, v247, v197, v202\n\tv_max_u32 v247, v247, v203\n\tv_cmp_eq_u32 vcc, -1, v247\n\ts_cbranch_vccz .LSL4b%=\n\ts_sleep 1\n\t" \
  "s_sub_u32 s87, s87, 1\n\ts_cbranch_scc0 .LSL4a%=\n\t.LSL4b%=:\ns_branch .LRED4%=\n\t.LSL5%=:\ns_add_u32 s88, s80, 8\n\ts_cmp_eq_u32 s84, s88\n\t" \
  "s_cbranch_scc1 .LH5B%=\n\ts_mov_b32 s84, s88\n\ts_mov_b32 s87, %[spin]\n\t.LSL5a%=:\nbuffer_load_dwordx4 v[194:197], %[t16], %[rs], s81 offen sc1\n\t" \
  "buffer_load_dwordx4 v[198:201], %[t16], %[rs], s81 offen offset:1024 sc1\n\ts_waitcnt vmcnt(0)\n\tv_max3_u32 v247, v194, v195, v196\n\t" \
  "v_max3_u32 v247, v247, v197, v198\n\tv_max3_u32 v247, v247, v199, v200\n\tv_max_u32 v247, v247, v201\n\tv_cmp_eq_u32 vcc, -1, v247\n\t" \
  "s_cbranch_vccz .LSL5b%=\n\ts_sleep 1\n\ts_sub_u32 s87, s87, 1\n\ts_cbranch_scc0 .LSL5a%=\n\t.LSL5b%=:\ns_branch .LRED5%=\n\t.LSL1%=:\n" \
  "s_add_u32 s88, s80, 10\n\ts_cmp_eq_u32 s84, s88\n\ts_cbranch_scc1 .LH1B%=\n\ts_mov_b32 s84, s88\n\ts_mov_b32 s87, %[spin]\n\t.LSL1a%=:\n" \
  "buffer_load_dwordx2 v[202:203], %[ss], %[rs], s82 offen sc1\n\tbuffer_load_dwordx4 v[194:197], v243, %[rs], s81 offen sc1\n\t" \
  "buffer_load_dwordx4 v[198:201], v243, %[rs], s81 offen offset:128 sc1\n\ts_waitcnt vmcnt(0)\n\tv_max3_u32 v247, v194, v195, v196\n\t" \
  "v_max3_u32 v247, v247, v197, v198\n\tv_max3_u32 v247, v247, v199, v200\n\tv_max3_u32 v247, v247, v201, v202\n\tv_max_u32 v247, v247, v203\n\t" \
  "v_cmp_eq_u32 vcc, -1, v247\n\ts_cbranch_vccz .LSL1b%=\n\ts_sleep 1\n\ts_sub_u32 s87, s87, 1\n\ts_cbranch_scc0 .LSL1a%=\n\t.LSL1b%=:\n" \
  "s_branch .LRED1%=\n\t.LSLS%=:\ns_add_u32 s88, s80, 10\n\ts_cmp_eq_u32 s84, s88\n\ts_cbranch_scc1 .LH1B%=\n\ts_mov_b32 s84, s88\n\t" \
  "s_mov_b32 s87, %[spin]\n\t.LSLSa%=:\nbuffer_load_dwordx2 v[202:203], %[ss], %[rs], s82 offen sc1\n\ts_waitcnt vmcnt(0)\n\t" \
  "v_max3_u32 v247, v202, v203, v203\n\tv_cmp_eq_u32 vcc, -1, v247\n\ts_cbranch_vccz .LSLSb%=\n\ts_sleep 1\n\ts_sub_u32 s87, s87, 1\n\t" \
  "s_cbranch_scc0 .LSLSa%=\n\t.LSLSb%=:\ns_branch .LREDS%=\n\t.LSLM%=:\nv_max_f32_dpp v247, v246, v246 row_ror:1 row_mask:0xf bank_mask:0xf\n\t" \
  "s_nop 1\n\tv_max_f32_dpp v247, v247, v247 row_ror:2 row_mask:0xf bank_mask:0xf\n\ts_nop 1\n\t" \
  "v_max_f32_dpp v247, v247, v247 row_ror:4 row_mask:0xf bank_mask:0xf\n\ts_nop 1\n\t" \
  "v_max_f32_dpp v247, v247, v247 row_ror:8 row_mask:0xf bank_mask:0xf\n\ts_nop 1\n\t" \
  "v_max_f32_dpp v247, v247, v247 row_bcast:15 row_mask:0xa bank_mask:0xf\n\ts_nop 1\n\t" \
  "v_max_f32_dpp v247, v247, v247 row_bcast:31 row_mask:0xc bank_mask:0xf\n\ts_nop 1\n\tv_readlane_b32 s88, v247, 63\n\ts_nop 3\n\t" \
  "v_mov_b32 v239, s88\n\tv_sub_f32 v246, v246, v239\n\tv_exp_f32 v203, v246\n\ts_nop 0\n\tv_cvt_pk_bf16_f32 v244, v203, 0\n\ts_branch .LPV%=\n\t" \
  ".LSLC%=:\nv_max_f32_dpp v247, v204, v204 row_ror:8 row_mask:0xf bank_mask:0xf\n\ts_nop 1\n\t" \
  "v_max_f32_dpp v247, v247, v247 row_ror:4 row_mask:0xf bank_mask:0xf\n\ts_nop 1\n\t" \
  "v_max_f32_dpp v247, v247, v247 quad_perm:[1,0,3,2] row_mask:0xf bank_mask:0xf\n\tv_sub_f32 v204, v204, v247\n\tv_exp_f32 v204, v204\n\ts_nop 0\n\t" \
  "v_mul_f32 v194, v204, v194\n\tv_mul_f32 v195, v204, v195\n\tv_mul_f32 v196, v204, v196\n\tv_mul_f32 v197, v204, v197\n\ts_nop 0\n\t" \
  "v_add_f32_dpp v205, v194, v194 row_ror:8 row_mask:0xf bank_mask:0x3\n\tv_add_f32_dpp v206, v195, v195 row_ror:8 row_mask:0xf bank_mask:0x3\n\t" \
  "v_add_f32_dpp v205, v196, v196 row_ror:8 row_mask:0xf bank_mask:0xc\n\tv_add_f32_dpp v206, v197, v197 row_ror:8 row_mask:0xf bank_mask:0xc\n\t" \
  "s_nop 0\n\tv_add_f32_dpp v246, v205, v205 row_ror:12 row_mask:0xf bank_mask:0x5\n\t" \
  "v_add_f32_dpp v246, v206, v206 row_ror:4 row_mask:0xf bank_mask:0xa\n\ts_nop 1\n\t" \
  "v_add_f32_dpp v246, v246, v246 quad_perm:[1,0,3,2] row_mask:0xf bank_mask:0xf\n\ts_cmp_lg_u32 %[w], 6\n\ts_cbranch_scc1 .LCMS%=\n\t" \
  "v_cmp_eq_u32 vcc, 0, v242\n\tv_cndmask_b32 v246, v246, v247, vcc\n\ts_branch .LCMS%=\n\t.LFE%=:\n.LEND%=:\n"
static_assert(BLK == 16384 && NT == 512 && QS == 400 && L_SM == 0 && TW == 7 && SW == 6, "constants of the hand-written part");

__global__ void __launch_bounds__(NT, 2)
mega_kernel(const uint16_t* __restrict__ x_in, const uint16_t* __restrict__ kc, const uint16_t* __restrict__ vc,
            const uint16_t* __restrict__ wq, const uint16_t* __restrict__ wo, const uint16_t* __restrict__ w1,
            const uint16_t* __restrict__ b1, const uint16_t* __restrict__ w2, const uint16_t* __restrict__ b2,
            uint16_t* __restrict__ out, unsigned* epoch) {
  extern __shared__ __attribute__((aligned(16))) char lds[];
  uint32_t cp;
  const uint64_t code0 = __builtin_amdgcn_s_getpc() & ~(uint64_t)127;
  asm volatile("global_load_dword %0, %1, %2" : "=v"(cp) : "v"(min((int)((blockIdx.x >> 3) * 1024 + (threadIdx.x >> 6) * 128), CODEPF - 128)),
               "s"(code0) : "memory");
  const int tid0 = threadIdx.x, w0 = __builtin_amdgcn_readfirstlane(tid0 >> 6);
  const int b0 = blockIdx.x;
  const int g0 = (b0 & 7) * 4 + (b0 >> 6);
  const int rw = (b0 & 56) * 8 + w0 * 8 + RSTEP * g0;
  uint32_t rowb = (g0 * SK + (SK - WIN)) * (2 * DH), rot0 = (rw & (WIN - 1)) * (2 * DH);
  int vo = ((tid0 >> 4) & 3) * 384 + (tid0 & 15) * 16, vo2 = vo + 256 - (tid0 & 15) * 8;
  rot0 += rowb;
  PINS2(rowb, rot0); PIN1(vo); PIN1(vo2);
  const char* vc0 = (const char*)vc;
  asm volatile("" : "+s"(vc0) : "s"(kc), "s"(wq), "s"(x_in), "s"(w1), "s"(b1), "s"(b2), "s"(epoch), "v"(vo), "v"(vo2), "s"(rot0));
  __builtin_amdgcn_sched_barrier(0);
  unsigned long long* gran_base = (unsigned long long*)epoch + 512;
  char* tail = (char*)(((uintptr_t)epoch + (4u << 20)) & ~(uintptr_t)((2u << 20) - 1));
  const auto rs = __builtin_amdgcn_make_buffer_rsrc(tail, 0, 128 * BLK, 0x00020000);
  auto st4 = [&](int voff, int so, float a) { __builtin_amdgcn_raw_buffer_store_b32(__float_as_uint(a), rs, voff, so, 16); };
  auto trd = [&](int off) -> u32x2 {
    return __builtin_bit_cast(u32x2, __builtin_amdgcn_ds_read_tr16_b64_v4i16((LAS s16x4*)(uintptr_t)(uint32_t)off));
  };
  auto lrd16 = [&](int off) -> u32x4 { return *(const LAS u32x4*)(uintptr_t)(uint32_t)off; };
  auto lrd4 = [&](int off) -> uint32_t { return *(const LAS uint32_t*)(uintptr_t)(uint32_t)off; };
  auto lwr2 = [&](int off, uint32_t v) { *(LAS uint16_t*)(uintptr_t)(uint32_t)off = (uint16_t)v; };
  auto lwr4 = [&](int off, uint32_t v) { *(LAS uint32_t*)(uintptr_t)(uint32_t)off = v; };
  constexpr int sm = L_SM;

  uint32_t tag;
  uint32_t tE = 0;
  uint32_t tcur = 0;
  u32x8 kt[12], vt[12];
  uint32_t ix;
  int t16, bo, sS, dT;

  auto qk_softmax = [&](float& mx, float& ev, f32x4 (&dv)[3], int qb, float qs) -> uint32_t {
    u32x4 qa[3];
    f32x4 d[4];
#pragma unroll
    for (int u = 0; u < 3; ++u) qa[u] = lrd16(qb + 64 * u);
#pragma unroll
    for (int a = 0; a < 1; ++a) { ZERO4(d[0], 204, 205, 206, 207); ZERO4(d[1], 208, 209, 210, 211); ZERO4(d[2], 212, 213, 214, 215); ZERO4(d[3], 216, 217, 218, 219); }
    SB();
#pragma unroll
    for (int u = 0; u < 3; ++u)
#pragma unroll
      for (int a = 0; a < 4; ++a) {
        d[a] = smf<0>(qa[u], kt[3 * a + u], d[a], ix);
        SB();
        if (u == 1 && a == 0) ZERO4(dv[0], 220, 221, 222, 223);
        if (u == 1 && a == 2) ZERO4(dv[1], 224, 225, 226, 227);
        if (u == 2 && a == 0) ZERO4(dv[2], 228, 229, 230, 231);
        SB();
      }
    __builtin_amdgcn_s_setprio(1);
    const bool same = __builtin_amdgcn_inverse_ballot_w64(0xff00ff0000ff00ffull);
    const float f0 = same ? d[0][0] + d[0][1] : d[1][0] + d[1][1], f1 = same ? d[2][0] + d[2][1] : d[3][0] + d[3][1];
    auto r = __builtin_amdgcn_permlane32_swap(__float_as_uint(f0), __float_as_uint(f1), false, false);
    const float sraw = uf(r[0]) + dppv<0x128>(uf(r[1]));
    const float sv = rbf(rbf(sraw) * 0.07216878364870322f) * LOG2E;
    uint64_t big = __builtin_amdgcn_ballot_w64(!(fabsf(sv) <= SMALL));
    PINS(big);
    SB();
    mx = 0.f;
    ev = __builtin_amdgcn_exp2f(sv);
    uint32_t pb = pk2(ev, 0.f);
    PIN1(pb);
    SB();
    if (__builtin_expect(big != 0, 0)) {
      float mxv;
      asm volatile("s_nop 1\n\t"
                   "v_max_f32_dpp %0, %1, %1 row_ror:1 row_mask:0xf bank_mask:0xf\n\ts_nop 1\n\t"
                   "v_max_f32_dpp %0, %0, %0 row_ror:2 row_mask:0xf bank_mask:0xf\n\ts_nop 1\n\t"
                   "v_max_f32_dpp %0, %0, %0 row_ror:4 row_mask:0xf bank_mask:0xf\n\ts_nop 1\n\t"
                   "v_max_f32_dpp %0, %0, %0 row_ror:8 row_mask:0xf bank_mask:0xf\n\ts_nop 1\n\t"
                   "v_max_f32_dpp %0, %0, %0 row_bcast:15 row_mask:0xa bank_mask:0xf\n\ts_nop 1\n\t"
                   "v_max_f32_dpp %0, %0, %0 row_bcast:31 row_mask:0xc bank_mask:0xf\n\ts_nop 0"
                   : "=&v"(mxv) : "v"(sv));
      mx = __int_as_float(__builtin_amdgcn_readlane(__float_as_int(mxv), 63));
      ev = __builtin_amdgcn_exp2f(sv - mx);
      pb = pk2(ev, 0.f);
    }
    __builtin_amdgcn_s_setprio(0);
    return pb;
  };
  auto pv_combine = [&](const u32x4& cT, int od, bool sw) {
    const uint32_t m = lrd4(cT.x);
    u32x4 rv = lrd16(cT.y);
    SB();
    uint64_t shifted = __builtin_amdgcn_ballot_w64(m != 0);
    PINS(shifted);
    SB();
    float o = sum_b320(rv);
    PIN1(o);
    SB();
    if (__builtin_expect(shifted != 0, 0)) {
      float M = fmaxf(uf(m), dppv<0x128>(uf(m)));
      M = fmaxf(M, dppv<0x124>(M));
      M = fmaxf(M, dppv<0xb1>(M));
      const float f = __builtin_amdgcn_exp2f(uf(m) - M);
      rv = __builtin_bit_cast(u32x4, __builtin_bit_cast(f32x4, rv) * f);
      asm volatile("s_nop 1" : "+v"(rv));
      o = sum_b320(rv);
      if (sw && (t16 & 0xc0) == 0) o = M;
    }
    st4(cT.z, od, o);
  };
  int s0;
  int oG, oD, rC, eC, eH, eQ, rK;
  int dG, dH, dR, dE;
  int oD1, cwo, oo, far, scv[9];
  u32x4 rs4, ro4;
  float eps = 1e-5f, small = SMALL;

  {
    const int w = w0, b = b0, lane = tid0 & 63;
    const int g = (b & 7) * 4 + (b >> 6), j = (b >> 3) & 7, jg = b >> 3, jj = b & 7;
    const int ck = lane & 7, jp = lane >> 3, n16 = lane & 15, kq = lane >> 4;
    const int sub = (lane >> 5) + 2 * ((lane >> 4) & 1), trl = 4 * kq + (n16 >> 2), trc = 8 * (lane & 3);
    const int xc = min(16 * w + 4 * (lane & 3) + sub, 79);
    const int gc = 64 * (w < 3 ? w : 0) + 4 * (lane & 8) + 4 * ck + sub, hc = (w & 1) ? 32 + 4 * (lane & 3) + sub : 4 * ck + sub;
    gu64* G = (gu64*)gran_base;
    const uint16_t *kp = (const uint16_t*)((const char*)kc + rowb), *vp = (const uint16_t*)(vc0 + rowb);
    const int ko = (lane & 7) * 384 + (lane & 8) * 8 + kq * 16, ko2 = ko ^ 64;
#define GRP(lo, hi) "+{v[" #lo ":" #hi "]}"
#define LOADK(t, r0, r1, r2, g0, g1, g2) if constexpr (T == t) \
      asm volatile("global_load_dwordx4 " r0 ", %3, %4 nt\n\tglobal_load_dwordx4 " r1 ", %3, %4 offset:128 nt\n\t" \
                   "global_load_dwordx4 " r2 ", %3, %4 offset:256 nt" \
                   : g0(kt[a]), g1(kt[a + 1]), g2(kt[a + 2]) : "v"(o), "s"(p) : "memory")
    auto load_k = [&](auto Tc) {
      constexpr int T = decltype(Tc)::value, a = 3 * (T >> 1);
      const int o = (T & 2) ? ko2 : ko;
      const uint16_t* p = kp + ((512 * T + rw) & (WIN - 1)) * DH;
      LOADK(0, "v[2:5]", "v[10:13]", "v[18:21]", GRP(2, 9), GRP(10, 17), GRP(18, 25));
      LOADK(1, "v[6:9]", "v[14:17]", "v[22:25]", GRP(2, 9), GRP(10, 17), GRP(18, 25));
      LOADK(2, "v[26:29]", "v[34:37]", "v[42:45]", GRP(26, 33), GRP(34, 41), GRP(42, 49));
      LOADK(3, "v[30:33]", "v[38:41]", "v[46:49]", GRP(26, 33), GRP(34, 41), GRP(42, 49));
      LOADK(4, "v[50:53]", "v[58:61]", "v[66:69]", GRP(50, 57), GRP(58, 65), GRP(66, 73));
      LOADK(5, "v[54:57]", "v[62:65]", "v[70:73]", GRP(50, 57), GRP(58, 65), GRP(66, 73));
      LOADK(6, "v[74:77]", "v[82:85]", "v[90:93]", GRP(74, 81), GRP(82, 89), GRP(90, 97));
      LOADK(7, "v[78:81]", "v[86:89]", "v[94:97]", GRP(74, 81), GRP(82, 89), GRP(90, 97));
      SB();
    };
#define LOADV(v, x0, y0, x1, y1, g0, g1, g2) if constexpr (V == v) \
      asm volatile("global_load_dwordx4 " x0 ", %3, %5 nt\n\tglobal_load_dwordx2 " y0 ", %4, %5 nt\n\t" \
                   "global_load_dwordx4 " x1 ", %3, %5 offset:1536 nt\n\tglobal_load_dwordx2 " y1 ", %4, %5 offset:1536 nt" \
                   : g0(vt[a]), g1(vt[a + 1]), g2(vt[a + 2]) : "v"(vo), "v"(vo2), "s"(p) : "memory")
    auto load_v = [&](auto Vc) {
      constexpr int V = decltype(Vc)::value, a = 3 * (V >> 1);
      const uint16_t* p = V ? vp + ((512 * V + rw) & (WIN - 1)) * DH : (const uint16_t*)(vc0 + rot0);
      LOADV(0, "v[98:101]", "v[114:115]", "v[102:105]", "v[118:119]", GRP(98, 105), GRP(106, 113), GRP(114, 121));
      LOADV(1, "v[106:109]", "v[116:117]", "v[110:113]", "v[120:121]", GRP(98, 105), GRP(106, 113), GRP(114, 121));
      LOADV(2, "v[122:125]", "v[138:139]", "v[126:129]", "v[142:143]", GRP(122, 129), GRP(130, 137), GRP(138, 145));
      LOADV(3, "v[130:133]", "v[140:141]", "v[134:137]", "v[144:145]", GRP(122, 129), GRP(130, 137), GRP(138, 145));
      LOADV(4, "v[146:149]", "v[162:163]", "v[150:153]", "v[166:167]", GRP(146, 153), GRP(154, 161), GRP(162, 169));
      LOADV(5, "v[154:157]", "v[164:165]", "v[158:161]", "v[168:169]", GRP(146, 153), GRP(154, 161), GRP(162, 169));
      LOADV(6, "v[170:173]", "v[186:187]", "v[174:177]", "v[190:191]", GRP(170, 177), GRP(178, 185), GRP(186, 193));
      LOADV(7, "v[178:181]", "v[188:189]", "v[182:185]", "v[192:193]", GRP(170, 177), GRP(178, 185), GRP(186, 193));
      SB();
    };
    int go[5];
    auto wo_prep = [&]() {
      int l = lane;
      PIN1(l);
#pragma unroll
      for (int i = 0; i < 5; ++i) { const int c = min(64 * i + l, w < 6 ? 319 : 239), row = (c * 205) >> 11; go[i] = row * (2 * DM) + (c - 10 * row) * 16; PIN1(go[i]); }
    };
    auto load_wo = [&]() {
      const uint16_t* src = (w < 6 ? wo + (size_t)(192 * g + 32 * w) * DM : w2 + (size_t)(48 * g + 24 * (w - 6)) * DM) + 80 * j;
      unsigned keep;
      asm volatile("s_mov_b32 %0, m0\n\ts_mov_b32 m0, %7\n\ts_nop 0\n\tglobal_load_lds_dwordx4 %1, %6 nt\n\t"
                   "s_add_u32 m0, m0, 0x400\n\ts_nop 0\n\tglobal_load_lds_dwordx4 %2, %6 nt\n\t"
                   "s_add_u32 m0, m0, 0x400\n\ts_nop 0\n\tglobal_load_lds_dwordx4 %3, %6 nt\n\t"
                   "s_add_u32 m0, m0, 0x400\n\ts_mov_b64 exec, %8\n\tglobal_load_lds_dwordx4 %4, %6 nt\n\t"
                   "s_add_u32 m0, m0, %10\n\ts_mov_b64 exec, %9\n\tglobal_load_lds_dwordx4 %5, %6 nt\n\t"
                   "s_mov_b64 exec, -1\n\ts_mov_b32 m0, %0"
                   : "=&s"(keep) : "v"(go[0]), "v"(go[1]), "v"(go[2]), "v"(go[3]), "v"(go[4]), "s"(src), "s"(w < 7 ? L_WO + w * 5120 : L_W2 + 3840),
                     "s"(w < 6 ? ~0ull : (1ull << 48) - 1), "s"(w < 6 ? ~0ull : 1ull), "s"(w < 6 ? 1024 : 752) : "memory", "scc");
      SB();
    };
    asm volatile("" : "={v[98:105]}"(vt[0]), "={v[106:113]}"(vt[1]), "={v[114:121]}"(vt[2]));
    load_v(IC<0>{});
    uint32_t tp = 0;
    if (w == TW) tp = rt32() + SC0(PO);
    int pstep = SC0(PT);
    auto pace = [&]() {
      int rem;
      if (w == TW) asm volatile("1:\n\ts_memrealtime vcc\n\ts_waitcnt lgkmcnt(0)\n\ts_sub_i32 %0, %1, vcc_lo\n\ts_cmp_lt_u32 %0, 0x800\n\ts_cbranch_scc1 1b\n\ts_add_u32 %1, %1, %2"
                   : "=&s"(rem), "+s"(tp) : "s"(pstep) : "memory", "scc", "vcc");
    };
    const bool early = w < 4;
#define DEFG(n, a0, a1, b0, b1, c0, c1) if constexpr (N == n) asm volatile("" : "={v[" #a0 ":" #a1 "]}"(p[0]), "={v[" #b0 ":" #b1 "]}"(p[1]), "={v[" #c0 ":" #c1 "]}"(p[2]))
    auto first = [&](auto Ic) {
      constexpr int i = decltype(Ic)::value, x = i > 0 && i <= 8 ? kord(i - 1) : i ? vord(i - 8) : 0, N = (x & 1) ? -1 : (i > 0 && i <= 8 ? 0 : 4) + (x >> 1);
      u32x8* p = (i > 0 && i <= 8 ? kt : vt) + 3 * (x >> 1);
      DEFG(0, 2, 9, 10, 17, 18, 25);
      DEFG(1, 26, 33, 34, 41, 42, 49);
      DEFG(2, 50, 57, 58, 65, 66, 73);
      DEFG(3, 74, 81, 82, 89, 90, 97);
      DEFG(4, 98, 105, 106, 113, 114, 121);
      DEFG(5, 122, 129, 130, 137, 138, 145);
      DEFG(6, 146, 153, 154, 161, 162, 169);
      DEFG(7, 170, 177, 178, 185, 186, 193);
    };
    auto tile0 = [&](auto Ic) {
      constexpr int i = decltype(Ic)::value;
      if constexpr (i <= 8) load_k(IC<kord(i - 1)>{}); else load_v(IC<vord(i - 8)>{});
    };
    auto tile = [&](auto Ic) { first(Ic); tile0(Ic); };
    auto tile_e = [&](auto Ic) { first(Ic); if (early) tile0(Ic); SB(); };
    auto tile_l = [&](auto Ic) { SB(); if (!early) tile0(Ic); };
    auto piece = [&](int k, int n) { const int o = n * (b >> 2) + lane * 16; return lane * 16 < n ? O_S + ((b & 3) * NB + k + (o >> 13)) * BLK + (o & 8191) : OOR; };
    const int prb = O_PR + (b >> 3) * BLK + (w >> 2) * 8192 + (b & 7) * 1024 + (w & 3) * 256;
    uint32_t tv = 0;
    PINS(tv);
    if (b < NVOTE && w == SW) {
      __builtin_amdgcn_raw_buffer_store_b128((u32x4){0, 0, 0, 0}, rs, (lane & 12) != 12 && lane < 32 ? prb - 8192 - 512 + (lane >> 4) * 8192 + (lane & 12) * 64 + (lane & 3) * 16 : OOR, 0, 16);
      asm volatile("s_load_dword %0, %1, 0x80" : "+s"(tv) : "s"(tail + prb) : "memory");
    }
    SB();
    uint32_t ep = 0, xf[5] = {0, 0, 0, 0, 0}, xs = 0;
    asm volatile("" : "+v"(xf[0]), "+v"(xf[1]), "+v"(xf[2]), "+v"(xf[3]), "+v"(xf[4]), "+v"(xs), "+v"(ep));
    if (w == 1) asm volatile("global_load_dword %0, %1, off" : "+v"(xs) : "v"(lane < 40 ? b2 + 80 * jj + 2 * lane : b1 + 48 * g + 2 * lane - 80) : "memory");
    if (w == 0)
      asm volatile("global_load_dword %0, %7, %8\n\tglobal_load_dword %1, %7, %8 offset:256\n\tglobal_load_dword %2, %7, %8 offset:512\n\t"
                   "global_load_dword %3, %7, %8 offset:768\n\tglobal_load_dword %4, %7, %8 offset:1024\n\tglobal_load_dword %5, %9, %8\n\tglobal_load_dword %6, %10, %11 sc1"
                   : "+v"(xf[0]), "+v"(xf[1]), "+v"(xf[2]), "+v"(xf[3]), "+v"(xf[4]), "+v"(xs), "+v"(ep)
                   : "v"(4 * lane), "s"(x_in), "v"(160 * jj + 4 * min(lane, 39)), "v"(0), "s"(epoch) : "memory");
    SB();
    {
      int go[4];
#pragma unroll
      for (int r = 0; r < 4; ++r) { const int c = min(64 * r + lane, 249), row = (c * 41) >> 10; go[r] = row * (2 * QD) + min(c - 25 * row, 23) * 16; }
      unsigned keep;
      asm volatile("s_mov_b32 %0, m0\n\ts_mov_b32 m0, %6\n\ts_nop 0\n\tglobal_load_lds_dwordx4 %1, %5 nt\n\t"
                   "s_add_u32 m0, m0, 0x400\n\ts_nop 0\n\tglobal_load_lds_dwordx4 %2, %5 nt\n\t"
                   "s_add_u32 m0, m0, 0x400\n\ts_nop 0\n\tglobal_load_lds_dwordx4 %3, %5 nt\n\t"
                   "s_add_u32 m0, m0, 0x400\n\ts_mov_b64 exec, %7\n\tglobal_load_lds_dwordx4 %4, %5 nt\n\t"
                   "s_mov_b64 exec, -1\n\ts_mov_b32 m0, %0"
                   : "=&s"(keep) : "v"(go[0]), "v"(go[1]), "v"(go[2]), "v"(go[3]),
                     "s"(wq + (size_t)(80 * jj + 10 * w) * QD + 192 * jg), "s"(L_WQ + w * 10 * QS), "s"((1ull << 58) - 1) : "memory", "scc");
    }
    SB();
    {
      const int c = min(lane, 59), row = (c * 43) >> 8;
      unsigned keep;
      asm volatile("s_mov_b32 %0, m0\n\ts_mov_b32 m0, %3\n\ts_mov_b64 exec, %4\n\tglobal_load_lds_dwordx4 %1, %2 nt\n\ts_mov_b64 exec, -1\n\ts_mov_b32 m0, %0"
                   : "=&s"(keep) : "v"(row * (2 * FF) + (c - 6 * row) * 16), "s"(w1 + (size_t)(80 * jj + 10 * w) * FF + 48 * jg), "s"(L_W1 + w * 960), "s"((1ull << 60) - 1) : "memory");
    }
    SB();
    BAR0();
    tile(IC<1>{});
    auto probe = [&](auto Oc) -> uint32_t {
      unsigned long long c0, c1; uint32_t v;
      asm volatile("s_memtime %0\n\ts_waitcnt lgkmcnt(0)\n\ts_load_dword %2, %3, %4\n\ts_waitcnt lgkmcnt(0)\n\ts_memtime %1\n\ts_waitcnt lgkmcnt(0)"
                   : "=&s"(c0), "=&s"(c1), "=&s"(v) : "s"(tail + prb), "n"(decltype(Oc)::value) : "memory");
      return (uint32_t)(c1 - c0);
    };
    {
      t16 = w * 1024 + lane * 16; bo = sm + kq * 16;
      sS = w == SW ? (n16 == 0 ? sm + S_XP + kq * 16 : L_ONE) : ck * 32;
      dT = lane < (w == SW ? 2 : 16) ? lane * 4 : OOR;
      LAS u32x4* T = (LAS u32x4*)(uintptr_t)(uint32_t)(L_TAB + t16);
      const int pk = ((lane >> 1) & 6) | (lane & 1), c4 = 8 * w + ((lane >> 3) & 6) + ((lane >> 1) & 1);
      T[0] = (u32x4){(uint32_t)(sm + S_ML + pk * 16), (uint32_t)(w < 6 ? sm + S_RED + pk * RS + c4 * 16 : sm + S_ML + pk * 16),
                     (uint32_t)(w == SW ? ((lane & ~4) == 0 ? lane : OOR) : (!(lane & 1) && w < 6) ? c4 * 16 + (lane & 12) : OOR), (uint32_t)(jp * 768 + w * 256 + ck * 16)};
      T[5 * NT] = (u32x4){(uint32_t)((kq >> 1) == ((n16 >> 2) & 1) ? sm + S_QB + (n16 & 1) * 192 + (n16 & 8) * 4 + (kq & 1) * 16 : sm + S_ZQ),
                          (uint32_t)(sm + S_RED + w * RS + lane * 4), (uint32_t)(sm + S_ML + w * 16), 0u};
      ix = ((n16 & 1) ? 0xeeeeu : 0x4444u) | (n16 >> 2) * 0x55550000u;
      const int gd = (gc >> 4) & 3, dimo = gd + (gc < 128 ? 8 * (gc & 15) + 4 * (gc >> 6) : 128 + 4 * (gc & 15));
      if (w == 1 && lane < 12) *(LAS u32x4*)(uintptr_t)(uint32_t)(sm + S_ZQ + lane * 16) = (u32x4){0, 0, 0, 0};
      T[NT] = (u32x4){(uint32_t)(sm + S_OV + opos(dimo)), (uint32_t)(L_WO + (w < 5 ? w : 0) * 32 + trl * 160 + trc),
                      (uint32_t)(sm + S_XP + 2 * kpos(xc)), (uint32_t)(L_W1 + min(w, 2) * 32 + trl * 96 + trc)};
      T[2 * NT] = (u32x4){(uint32_t)(w < 5 ? sm + S_HV + 2 * kpos(hc) : piece(0, 768) + (w & 1) * 8192), (uint32_t)(w < 5 ? L_W2 + w * 32 + trl * 160 + trc : piece(6, 1024)),
                          (uint32_t)(L_CS + 768 + 4 * hc), (uint32_t)((w & 1) ? 1024 + jp * 64 + (ck & 3) * 16 : lane * 16)};
      T[3 * NT] = (u32x4){(uint32_t)(sm + S_QB + qpos(gc)), (uint32_t)(L_CS + 4 * gc), (uint32_t)(L_WQ + (w < 6 ? w : 0) * 64 + trl * QS + trc), (uint32_t)(2 * xc)};
    }
    SB();
    BAR0();
    tile(IC<2>{});
    asm volatile("s_waitcnt vmcnt(%8)" : "+v"(ep), "+v"(xf[0]), "+v"(xf[1]), "+v"(xf[2]), "+v"(xf[3]), "+v"(xf[4]), "+v"(xs), "+v"(cp) : "n"(3 * UP + 2) : "memory");
    if (w < 2) lwr4(sm + S_B + w * 256 + lane * 4, xs);
    if (w == 0) {
      float s1 = 0.f;
#pragma unroll
      for (int i = 0; i < 5; ++i) s1 += lo2f(xf[i]) + hi2f(xf[i]);
      const float mean = wsum_bcast(s1) * (1.0f / DM);
      float q0 = 0.f;
#pragma unroll
      for (int i = 0; i < 5; ++i) { const float d0 = lo2f(xf[i]) - mean, d1 = hi2f(xf[i]) - mean; q0 = fmaf(d0, d0, q0); q0 = fmaf(d1, d1, q0); }
      const float var = wsum_bcast(q0) * (1.0f / DM);
      const uint32_t mv = pk2(mean, var);
      const float rinv = __builtin_amdgcn_rcpf(rbf(__builtin_amdgcn_sqrtf(rbf(hi2f(mv) + 1e-5f))));
      if (lane < 48) lwr4(sm + S_XP + 2 * kpos(2 * lane), lane < 40 ? pk2(rbf(lo2f(xs) - lo2f(mv)) * rinv, rbf(hi2f(xs) - lo2f(mv)) * rinv) : 0u);
      if (lane < 48) lwr4(L_ONE + lane * 4, 0x3f803f80u);
      if (lane < 16) lwr2(sm + S_HV + 2 * kpos(48 + lane), 0);
      lwr4(L_FLAG, ep * 1664525u + 1013904223u);
    }
    if (b < NVOTE && (w & 3) != 3) lwr4(L_FLAG + 32 + 4 * w, probe(IC<0>{}));
    SB();
    asm volatile("s_waitcnt vmcnt(%0)" :: "n"(6) : "memory");
    lds_barrier();
    PINS(tv);
    tile_e(IC<3>{});
    tag = __builtin_amdgcn_readfirstlane(lrd4(L_FLAG));
    float pv[2];
    {
      const int xb = n16 >= 4 ? L_ONE : sm + S_XP + kq * 16;
      const u32x4 x0 = lrd16(xb), x1 = lrd16(xb + 64), x2 = lrd16(xb + 128);
#pragma unroll
      for (int r = 0; r < 2; ++r) {
        const int tl = min(2 * w + r, 14), rstr = tl < 12 ? QS : 96;
        const int a0 = (tl < 12 ? L_WQ : L_W1 - 12 * 32) + tl * 32 + trl * rstr + trc;
        f32x4 d = {0.f, 0.f, 0.f, 0.f};
        d = mfma32(x0, cat(trd(a0), trd(a0 + 16 * rstr)), d);
        d = mfma32(x1, cat(trd(a0 + 32 * rstr), trd(a0 + 48 * rstr)), d);
        d = mfma32(x2, cat(trd(a0 + 64 * rstr), (u32x2){0, 0}), d);
        pv[r] = d[0];
      }
    }
    gu64* dst = G + G_P + b * RP + (kq == 0 ? 0 : 192) + n16 + 32 * w;
    const int cu = (lane >> 2) & 7;
    int so = (w & 3) == 0 ? g * BLK + (w >> 2) * 8192 + j * 1024 + lane * 16
             : w == 1 && cu < 5 ? O_C + (2 * j + (cu >> 2)) * BLK + (lane >> 5) * 8192 + (cu & 3) * 2048 + ((g + 7 * cu) & 31) * 64 + (lane & 3) * 16 : OOR;
    if (w == TW) {
      unsigned xcc;
      asm volatile("s_getreg_b32 %0, hwreg(HW_REG_XCC_ID)" : "=s"(xcc));
      const u32x4 l0 = lrd16(L_FLAG + 32), l1 = lrd16(L_FLAG + 48);
      const uint32_t vote = ((min(min(l1.x, l1.y), l1.z) < min(min(l0.x, l0.y), l0.z) ? 1u : 0u) ^ (xcc >> 2)) & 1u;
      pv[1] = uf(vote | ((xcc >> 1) & 2) | (rt32() << 2));
    }
    PIN1(pv[0]); PIN1(pv[1]); PIN1(dst); PIN1(so);
    tile_l(IC<3>{});
    pace();
    BAR0();
    if (kq == 1 || (kq == 0 && w < 6)) {
      __hip_atomic_store(dst, ((unsigned long long)tag << 32) | __float_as_uint(pv[0]), RLX_AGENT);
      if (w < 7) __hip_atomic_store(dst + 16, ((unsigned long long)tag << 32) | __float_as_uint(pv[1]), RLX_AGENT);
    }
    if (w == TW && b < NVOTE && lane == 0) __hip_atomic_store(G + G_F + b, ((unsigned long long)tag << 32) | __float_as_uint(pv[1]), RLX_AGENT);
    if ((w & 2) == 0 && w != 5) __builtin_amdgcn_raw_buffer_store_b128((u32x4){SENT, SENT, SENT, SENT}, rs, so, 0, 16);
    SB();
    tile(IC<RT>{});
    pace();
    BAR0();
    tile_e(IC<RT + 1>{});
    {
      auto rd = [&](int off) { return lo2f(*(const LAS uint16_t*)(uintptr_t)(uint32_t)(sm + S_B + off)); };
      *(LAS f32x4*)(uintptr_t)(uint32_t)(L_TAB + 4 * NT * 16 + t16) = (f32x4){rd(2 * xc), rd(256 + 2 * xc), rd(416 + 2 * hc),
          uf(sm + S_RED + w * RS + (lane >> 5) * 128 + (lane & 8) * 8 + (lane & 6) * 8 + (lane & 1) * 8 + ((lane >> 4) & 1) * 4)};
    }
    tile_l(IC<RT + 1>{});
    pace();
    BAR0();
    tile(IC<RT + 2>{});
    pace();
    BAR0();
    u32x4 er = {0, 0, 0, 0};
    asm volatile("" : "+v"(er));
    auto ereq = [&](const char* src) { asm volatile("global_load_dwordx4 %0, %1, off sc1" : "+v"(er) : "v"(src) : "memory"); };
    auto efin = [&](auto Nc, const char* src) {
      asm volatile("s_waitcnt vmcnt(%1)" : "+v"(er) : "n"(decltype(Nc)::value) : "memory");
#pragma unroll 1
      for (unsigned sp = 0; sp < SPIN_LIMIT; ++sp) {
        if (__all(tagbad(er, tag) == 0)) break;
        __builtin_amdgcn_s_sleep(4);
        asm volatile("global_load_dwordx4 %0, %1, off sc1\n\ts_waitcnt vmcnt(0)" : "+v"(er) : "v"(src) : "memory");
      }
    };
    {
      const int un = j + 8 * w;
      const bool mine = un < 27;
      const char* src = mine ? (const char*)(G + G_P + (8 * g + jp) * RP) + un * 128 + ck * 16 : (const char*)(G + G_F) + n16 * 16;
      if (mine || w == TW) ereq(src);
      SB();
      tile(IC<RT + 3>{});
      pace();
      BAR0();
      tile_e(IC<RT + 4>{});
      asm volatile("s_waitcnt vmcnt(%0)" :: "n"(nreq(RT, RT + 3)) : "memory");
      if (mine) efin(IC<nreq(RT + 3, RT + 4)>{}, src);
      const float t = swapadd32(uf(er.x), uf(er.z));
      uint32_t sv = __float_as_uint(ror8add(swapadd16(t, t)));
      gu64* sd = G + G_R + g * RP + un * 16 + 2 * ck + (lane >> 5);
      PIN1(sv); PIN1(sd);
      tile_l(IC<RT + 4>{});
      pace();
      BAR0();
      if (mine && !(lane & 24)) __hip_atomic_store(sd, ((unsigned long long)tag << 32) | sv, RLX_AGENT);
      if (w == TW) {
        if (b < NVOTE && lane == 0) __hip_atomic_store(G + G_F + NVOTE + b, ((unsigned long long)tag << 32) | rt32(), RLX_AGENT);
        efin(IC<nreq(RT + 3, RT + 4)>{}, src);
        const uint32_t hx = (uint32_t)__ballot(er.x & 2), hz = (uint32_t)__ballot(er.z & 2);
        lwr4(L_FLAG + 16, (__popcll(__ballot(er.x & 1)) + __popcll(__ballot(er.z & 1))) << 8 | (hx & 1) | ((hz & 1) << 1) | ((hx & 2) << 1) | ((hz & 2) << 2));
        if (SC(PB)) {
          const float d = (float)((int)(er.x - (tp << 2)) >> 2) + (float)((int)(er.z - (tp << 2)) >> 2);
          tp += __builtin_amdgcn_readfirstlane((int)(wsum_bcast(d) * (1.f / 128.f))) + SC(PA);
          pstep = SC(PB);
        }
      }
      tp = __builtin_amdgcn_readfirstlane(tp); pstep = __builtin_amdgcn_readfirstlane(pstep);
      SB();
    }
    tile(IC<RT + 5>{});
    pace();
    lds_barrier();
    tile_e(IC<RT + 6>{});
    {
      const int f0 = __builtin_amdgcn_readfirstlane(lrd4(L_FLAG + 16)), votes = f0 >> 8;
      const int m4 = (f0 & 15) ^ ((SC(CPAR) < 2 ? SC(CPAR) : votes > 2 * NVOTE) ? 15 : 0);
      const int pm = m4 | ((m4 ^ 15) << 4);
      auto par = [&](int x) { return ((pm >> x) & 1) * 8192; };
      const int cw = O_C + (w >> 2) * BLK + (w & 3) * 2048;
      oG = g * BLK + par(jj); oD = oG + (w == SW ? O_SG + j * 32 : j * 768);
      rC = 2 * jj * BLK + par(jj) + cw - w * 1024; eC = 2 * j * BLK + par(j) + cw + ((g + 7 * w) & 31) * 64;
      eH = jg * BLK + par(jg >> 2) + (w == SW ? O_SH + jj * 32 : O_H + (w < 2 ? jj * 128 + w * 64 : 1024 + jj * 64));
      rK = par(jj) + O_DB - TW * 1024;
      dG = O_S + ((jj & 3) * NB + 6 + (g & 3) - g) * BLK; dH = O_S + (((jg >> 2) & 3) * NB + 6 + (jg & 3) - jg) * BLK;
      dR = O_S - O_C + ((jj & 3) * NB + 10 - 2 * jj) * BLK; dE = O_S - O_C + ((j & 3) * NB + 10 - 2 * j) * BLK;
      eQ = jg * BLK + dH - 4 * BLK + par(jg >> 2) + (w == SW ? O_SG + jj * 32 : jj * 768 + w * 128);
#define UNI(a) a = __builtin_amdgcn_readfirstlane(a)
      UNI(oG); UNI(oD); UNI(rC); UNI(eC); UNI(eH); UNI(eQ); UNI(rK);
      oD1 = oD + dG; cwo = O_DB + b * 8; oo = 160 * b; far = min(b >> 3, 1);
      rs4 = (u32x4){(uint32_t)(uintptr_t)tail, (uint32_t)((uintptr_t)tail >> 32) & 0xffffu, 128u * BLK, 0x00020000u};
      ro4 = (u32x4){(uint32_t)(uintptr_t)out, (uint32_t)((uintptr_t)out >> 32) & 0xffffu, 2u * DM, 0x00020000u};
#pragma unroll
      for (int i = 0; i < 9; ++i) { scv[i] = i < 8 ? SC(i) : SC(PW); PINS(scv[i]); }
      PINS2(oG, oD); PINS2(rC, eC); PINS2(eH, eQ); PINS2(rK, oD1); PINS2(dG, dH); PINS2(dR, dE); PINS2(cwo, oo); PINS(far); PINS2(rs4, ro4); PINS2(eps, small);
      PIN1(sS); PIN1(dT);
    }
    tile_l(IC<RT + 6>{});
    pace();
    BAR0();
    {
      const int v0 = min(128 * w + 2 * lane, RP - 2);
      const char* src = w < 4 ? (const char*)(G + G_R + g * RP + v0) : (const char*)(G + G_F + NVOTE) + n16 * 16;
      if (w < 4 || w == TW) ereq(src);
      SB();
      tile(IC<RT + 7>{});
      pace();
      BAR0();
      tile(IC<RT + 8>{});
      pace();
      BAR0();
      if (w == TW && SC(PD)) {
        asm volatile("s_waitcnt vmcnt(%1)" : "+v"(er) : "n"(nreq(RT + 7, RT + 8)) : "memory");
        const bool v0 = er.y == tag, v1 = er.w == tag;
        const float d = (v0 ? (float)(int)(er.x - tp) : 0.f) + (v1 ? (float)(int)(er.z - tp) : 0.f);
        const int n = __popcll(__ballot(v0)) + __popcll(__ballot(v1));
        if (n > 32) {
          tp += __builtin_amdgcn_readfirstlane((int)(wsum_bcast(d) * __builtin_amdgcn_rcpf((float)n))) + SC(PC);
          pstep = SC(PD);
        }
      }
      tp = __builtin_amdgcn_readfirstlane(tp); pstep = __builtin_amdgcn_readfirstlane(pstep);
      SB();
      tile(IC<RT + 9>{});
      if (w < 4) {
        efin(IC<nreq(RT + 7, RT + 9)>{}, src);
        if (v0 < 192) lwr4(sm + S_QB + qpos(v0), pk2(uf(er.x), uf(er.z)));
        else *(LAS u32x2*)(uintptr_t)(uint32_t)(L_CS + 4 * (v0 - 192)) = (u32x2){er.x, er.z};
      }
    }
    SB();
    pace();
    lds_barrier();
    tile_e(IC<RT + 10>{});
    asm volatile("s_waitcnt vmcnt(%12)"
                 : GRP(2, 9)(kt[0]), GRP(10, 17)(kt[1]), GRP(18, 25)(kt[2]), GRP(26, 33)(kt[3]), GRP(34, 41)(kt[4]), GRP(42, 49)(kt[5]),
                   GRP(50, 57)(kt[6]), GRP(58, 65)(kt[7]), GRP(66, 73)(kt[8]), GRP(74, 81)(kt[9]), GRP(82, 89)(kt[10]), GRP(90, 97)(kt[11])
                 : "n"(nreq(9, RT + 9)) : "memory");
    const u32x4 aT = lrd16(L_TAB + 5 * NT * 16 + t16);
    const int pr = sm + S_RED + w * RS + kq * 16;
    f32x4 dv[3];
    {
      float mx, ev;
      lwr4(lrd4(L_TAB + 4 * NT * 16 + 12 + t16), qk_softmax(mx, ev, dv, aT.x, 1.f));
      const float ls = wsum63(ev);
      if (lane == 63) *(LAS f32x2*)(uintptr_t)(uint32_t)aT.z = (f32x2){mx, ls};
    }
#define VFIX(n, e0, e1, e2, e3, e4, e5, e6, e7, g0, g1, g2) if constexpr (i == n) \
      asm volatile("s_waitcnt vmcnt(%3)\n\tv_swap_b32 v" #e0 ", v" #e1 "\n\tv_swap_b32 v" #e2 ", v" #e3 "\n\tv_swap_b32 v" #e4 ", v" #e5 "\n\tv_swap_b32 v" #e6 ", v" #e7 "\n\ts_nop 0" \
                   : g0(vt[a]), g1(vt[a + 1]), g2(vt[a + 2]) : "n"(decltype(YN)::value) : "memory")
    auto mm_step = [&](auto Ic, auto YN) {
      constexpr int i = decltype(Ic)::value, a = 3 * i;
      u32x4 pa = lrd16(pr + 64 * i);
      PIN1(pa);
      SB();
      VFIX(0, 100, 106, 101, 107, 104, 110, 105, 111, GRP(98, 105), GRP(106, 113), GRP(114, 121));
      VFIX(1, 124, 130, 125, 131, 128, 134, 129, 135, GRP(122, 129), GRP(130, 137), GRP(138, 145));
      VFIX(2, 148, 154, 149, 155, 152, 158, 153, 159, GRP(146, 153), GRP(154, 161), GRP(162, 169));
      VFIX(3, 172, 178, 173, 179, 176, 182, 177, 183, GRP(170, 177), GRP(178, 185), GRP(186, 193));
      SB();
      dv[2] = smf<1>(pa, vt[3 * i + 2], dv[2], ix); SB();
      dv[0] = smf<1>(pa, vt[3 * i], dv[0], ix); dv[1] = smf<1>(pa, vt[3 * i + 1], dv[1], ix);
    };
    SB();
    wo_prep();
    tile_l(IC<RT + 10>{});
    pace();
    lds_barrier();
    tile_e(IC<15>{});
    mm_step(IC<0>{}, IC<12>{}); mm_step(IC<1>{}, IC<12>{});
    tile_l(IC<15>{});
    BAR0();
    load_wo();
    mm_step(IC<2>{}, IC<nreq(14, 15)>{});
    SB();
    u32x4 cT = lrd16(L_TAB + t16);
    PIN1(cT);
    mm_step(IC<3>{}, IC<5>{});
    lwr4(aT.y + 512, __float_as_uint(dv[2][0])); lwr4(aT.y, __float_as_uint(dv[0][0])); lwr4(aT.y + 256, __float_as_uint(dv[1][0]));
    lds_barrier();
    pv_combine(cT, oD, w == SW);
    s0 = cT.w;
  }
  {
    asm volatile(TAIL_ASM
                 : [s0] "+v"(s0), [tc] "+s"(tcur), [te] "+s"(tE), [oG] "+s"(oG), [rC] "+s"(rC), [eC] "+s"(eC), [eH] "+s"(eH)
                 : [t16] "v"(t16), [bo] "v"(bo), [ss] "v"(sS), [dt] "v"(dT), [ix] "v"(ix), [rs] "s"(rs4), [ro] "s"(ro4), [w] "s"(w0), [far] "s"(far),
                   [oD] "s"(oD1), [dG] "s"(dG), [dH] "s"(dH), [dR] "s"(dR), [dE] "s"(dE), [eQ] "s"(eQ), [rK] "s"(rK), [tag] "s"(tag), [cw] "s"(cwo), [oo] "s"(oo),
                   [p3e] "s"(scv[0]), [p2] "s"(scv[1]), [p3] "s"(scv[2]), [p4] "s"(scv[3]), [p5] "s"(scv[4]), [p1] "s"(scv[5]), [rmin] "s"(scv[6]), [rcons] "s"(scv[7]), [pw] "s"(scv[8]),
                   [eps] "s"(eps), [small] "s"(small), [spin] "n"(SPIN_LIMIT), [tab] "n"(L_TAB), [xp] "n"(S_XP), [ov] "n"(S_OV), [hv] "n"(S_HV),
                   [osg] "n"(O_SG), [osh] "n"(O_SH), [oh] "n"(O_H), [oor] "n"(OOR), [qsw] "n"(L_FLAG + 4), [red] "n"(S_RED), [rsp] "n"(RS),
                   "{v[2:9]}"(kt[0]), "{v[10:17]}"(kt[1]), "{v[18:25]}"(kt[2]), "{v[26:33]}"(kt[3]), "{v[34:41]}"(kt[4]), "{v[42:49]}"(kt[5]),
                   "{v[50:57]}"(kt[6]), "{v[58:65]}"(kt[7]), "{v[66:73]}"(kt[8]), "{v[74:81]}"(kt[9]), "{v[82:89]}"(kt[10]), "{v[90:97]}"(kt[11]),
                   "{v[98:105]}"(vt[0]), "{v[106:113]}"(vt[1]), "{v[114:121]}"(vt[2]), "{v[122:129]}"(vt[3]), "{v[130:137]}"(vt[4]),
                   "{v[138:145]}"(vt[5]), "{v[146:153]}"(vt[6]), "{v[154:161]}"(vt[7]), "{v[162:169]}"(vt[8]), "{v[170:177]}"(vt[9]),
                   "{v[178:185]}"(vt[10]), "{v[186:193]}"(vt[11])
                 : "memory", "scc", "vcc",
                   "v194", "v195", "v196", "v197", "v198", "v199", "v200", "v201", "v202", "v203", "v204", "v205", "v206", "v207", "v208", "v209",
                   "v210", "v211", "v212", "v213", "v214", "v215", "v216", "v217", "v218", "v219", "v220", "v221", "v222", "v223", "v224", "v225",
                   "v226", "v227", "v228", "v229", "v230", "v231", "v232", "v233", "v234", "v235", "v236", "v237", "v238", "v239", "v240", "v241",
                   "v242", "v243", "v244", "v245", "v246", "v247", "v248", "v249", "s80", "s81", "s82", "s83", "s84", "s85", "s86", "s87", "s88", "s89", "s90", "s91",
                   "s92", "s93", "s94", "s95", "s96", "s97", "s98", "s99"
                 );
  }
  if (b0 == 8 && t16 == SW * 1024) __hip_atomic_store((gu32*)epoch, tag, RLX_AGENT);
}


extern "C" void kernel_launch(void* const* d_in, const int* in_sizes, int n_in, void* d_out, int out_size,
                              void* d_ws, size_t ws_size, hipStream_t stream) {
  (void)in_sizes; (void)n_in; (void)out_size; (void)ws_size;
  (void)hipFuncSetAttribute((const void*)mega_kernel, hipFuncAttributeMaxDynamicSharedMemorySize, L_TOTAL);
  mega_kernel<<<NWG, NT, L_TOTAL, stream>>>(
      (const uint16_t*)d_in[0], (const uint16_t*)d_in[1], (const uint16_t*)d_in[2], (const uint16_t*)d_in[3],
      (const uint16_t*)d_in[4], (const uint16_t*)d_in[6], (const uint16_t*)d_in[7], (const uint16_t*)d_in[8],
      (const uint16_t*)d_in[9], (uint16_t*)d_out, (unsigned*)d_ws
  );
}
```
